```python
import torch

def _softmax(x, axis=-1):
    return torch.softmax(x, dim=axis)
def _gelu(x):
    return torch.nn.functional.gelu(x, approximate="tanh")
def _silu(x):
    return torch.nn.functional.silu(x)
def _sigmoid(x):
    return torch.sigmoid(x)
def _layernorm(x, axis=-1, eps=1e-5):
    m = x.mean(dim=axis, keepdim=True)
    v = x.var(dim=axis, keepdim=True, unbiased=False)
    return (x - m) / torch.sqrt(v + eps)
def _scatter_add(tgt, idx, src, axis=0):
    return tgt.clone().index_add_(axis, idx, src)
def _segment_sum(src, seg, n):
    out = torch.zeros((n,) + src.shape[1:], dtype=src.dtype)
    return out.index_add_(0, seg, src)
def _pad(x, pw):
    flat = tuple(v for pair in reversed(pw) for v in pair)
    return torch.nn.functional.pad(x, flat)

kernel_name = 'recurrent_scan__grouped_kv_mqa__moe_arch_mod__prompt'

def setup_inputs():

    _w = (lambda a: (lambda t: t.to(torch.bfloat16) if t.is_floating_point() else t)(torch.as_tensor(a)))
    def _A_setup():
        B, S, D, H = 1, 2048, 1024, 32
        Dh = D // H
        return {
            "x": _w((lambda *s: torch.randn(*s, dtype=torch.bfloat16) * 0.05)(B, S, D)),
            "wq": _w((lambda *s: torch.randn(*s, dtype=torch.bfloat16) * 0.05)(D, H * Dh)),
            "wk": _w((lambda *s: torch.randn(*s, dtype=torch.bfloat16) * 0.05)(D, Dh)),
            "wv": _w((lambda *s: torch.randn(*s, dtype=torch.bfloat16) * 0.05)(D, Dh)),
            "wo": _w((lambda *s: torch.randn(*s, dtype=torch.bfloat16) * 0.05)(H * Dh, D)),
        }
    _A_in = _A_setup()

    def _B_setup():
        B, S, D, F, C = 1, 2048, 1024, 3584, 1
        return {
            "x": _w((lambda *s: torch.randn(*s, dtype=torch.bfloat16) * 0.05)(B, S, D)),
            "w_r": _w((lambda *s: torch.randn(*s, dtype=torch.bfloat16) * 0.05)(D)),
            "w_gate": _w((lambda *s: torch.randn(*s, dtype=torch.bfloat16) * 0.05)(D, F)),
            "w_up": _w((lambda *s: torch.randn(*s, dtype=torch.bfloat16) * 0.05)(D, F)),
            "w_down": _w((lambda *s: torch.randn(*s, dtype=torch.bfloat16) * 0.05)(F, D)),
        }
    _B_in = _B_setup()

    return {
        "A_x": _A_in["x"],
        "A_wq": _A_in["wq"],
        "A_wk": _A_in["wk"],
        "A_wv": _A_in["wv"],
        "A_wo": _A_in["wo"],
        "B_x": _B_in["x"],
        "B_w_r": _B_in["w_r"],
        "B_w_gate": _B_in["w_gate"],
        "B_w_up": _B_in["w_up"],
        "B_w_down": _B_in["w_down"],
    }

def reference(A_x, A_wq, A_wk, A_wv, A_wo, B_x, B_w_r, B_w_gate, B_w_up, B_w_down):
    def _A_ref(x, wq, wk, wv, wo):
        B, S, D = x.shape
        H = 32
        Dh = D // H
        q = (lambda x, *d: x.permute(*d))((x @ wq).reshape(B, S, H, Dh), 0, 2, 1, 3)
        k = x @ wk
        v = x @ wv
        scores = torch.einsum("bhqd,bkd->bhqk", q, k) * (Dh ** -0.5)
        p = _softmax(scores, axis=-1)
        o = torch.einsum("bhqk,bkd->bhqd", p, v)
        return (lambda x, *d: x.permute(*d))(o, 0, 2, 1, 3).reshape(B, S, H * Dh) @ wo

    def _B_ref(x, w_r, w_gate, w_up, w_down):
        B, S, D = x.shape
        C = 1
        r = x @ w_r
        idx = torch.argsort(-r, axis=-1)[:, :C]
        idx_e = (lambda x, n, axis: torch.repeat_interleave(x, n, dim=axis))(idx.reshape(B, C, 1), D, axis=2)
        x_sel = (lambda x, i, axis: torch.take_along_dim(x, i, dim=axis))(x, idx_e, axis=1)
        g = _silu(x_sel @ w_gate)
        u = x_sel @ w_up
        y = (g * u) @ w_down
        r_sel = _sigmoid((lambda x, i, axis: torch.take_along_dim(x, i, dim=axis))(r, idx, axis=1))
        y = y * r_sel.reshape(B, C, 1)
        b_off = (torch.arange(B) * S).reshape(B, 1)
        flat_idx = (idx + b_off).reshape(B * C)
        zero = (x * 0.0).reshape(B * S, D)
        upd = _scatter_add(zero, flat_idx, y.reshape(B * C, D), axis=0)
        return x + upd.reshape(B, S, D)

    x = A_x
    xn = _layernorm(x, axis=-1)
    s = _A_ref(xn, A_wq, A_wk, A_wv, A_wo)
    g = _silu(_B_ref(xn, B_w_r, B_w_gate, B_w_up, B_w_down))
    return x + s * g
```

```cpp
#include <hip/hip_runtime.h>
#include <hip/hip_bf16.h>
#include <stdint.h>
typedef unsigned short u16;
__device__ __forceinline__ float b2f(u16 v) { return __uint_as_float((unsigned)v << 16); }
__device__ __forceinline__ u16 f2b(float f) { return __builtin_bit_cast(u16, (__bf16)f); }
__device__ __forceinline__ float rb(float f) { return b2f(f2b(f)); }
constexpr int S = 2048, D = 1024, F = 3584;
__global__ void ln_k(const u16* x, const u16* wr, u16* xn, float* r) {
    __shared__ float red[256];
    int t = blockIdx.x, tid = threadIdx.x; float v[4]; float s = 0;
    for (int i = 0; i < 4; ++i) { v[i] = b2f(x[t * D + tid * 4 + i]); s += v[i]; }
    red[tid] = s; __syncthreads(); for (int o = 128; o; o >>= 1) { if (tid < o) red[tid] += red[tid + o]; __syncthreads(); }
    float mean = red[0] / D; __syncthreads();
    float q = 0; for (int i = 0; i < 4; ++i) q += (v[i] - mean) * (v[i] - mean);
    red[tid] = q; __syncthreads(); for (int o = 128; o; o >>= 1) { if (tid < o) red[tid] += red[tid + o]; __syncthreads(); }
    float var = red[0] / D; __syncthreads();
    float m = rb(mean), sd = rb(sqrtf(rb(rb(var) + 1e-5f)));
    float a = 0;
    for (int i = 0; i < 4; ++i) { float y = rb(rb(v[i] - m) / sd); xn[t * D + tid * 4 + i] = f2b(y); a += y * b2f(wr[tid * 4 + i]); }
    red[tid] = a; __syncthreads(); for (int o = 128; o; o >>= 1) { if (tid < o) red[tid] += red[tid + o]; __syncthreads(); }
    if (tid == 0) r[t] = rb(red[0]);
}
__global__ void gemm_k(const u16* A, const u16* B, u16* C, int M, int N, int K, float scale) {
    __shared__ float a[16][17], b[16][17];
    int tx = threadIdx.x, ty = threadIdx.y, row = blockIdx.y * 16 + ty, col = blockIdx.x * 16 + tx; float acc = 0;
    for (int k0 = 0; k0 < K; k0 += 16) { a[ty][tx] = b2f(A[(size_t)row * K + k0 + tx]); b[ty][tx] = b2f(B[(size_t)(k0 + ty) * N + col]); __syncthreads();
        for (int k = 0; k < 16; ++k) acc += a[ty][k] * b[k][tx]; __syncthreads(); }
    C[(size_t)row * N + col] = f2b(acc * scale);
}
__global__ void attn_k(const u16* q, const u16* k, const u16* v, u16* o) {
    __shared__ float ks[64][32], vs[64][32];
    int idx = blockIdx.x * 256 + threadIdx.x; float qq[32], acc[32]; for (int d = 0; d < 32; ++d) { qq[d] = b2f(q[(size_t)idx * 32 + d]); acc[d] = 0; }
    float m = -1e30f, l = 0;
    for (int j0 = 0; j0 < S; j0 += 64) { __syncthreads(); for (int i = threadIdx.x; i < 2048; i += 256) { ks[i >> 5][i & 31] = b2f(k[j0 * 32 + i]); vs[i >> 5][i & 31] = b2f(v[j0 * 32 + i]); } __syncthreads();
        for (int j = 0; j < 64; ++j) { float s = 0; for (int d = 0; d < 32; ++d) s += qq[d] * ks[j][d];
            if (s > m) { float c = exp2f(m - s); l *= c; for (int d = 0; d < 32; ++d) acc[d] *= c; m = s; }
            float p = rb(exp2f(s - m)); l += p; for (int d = 0; d < 32; ++d) acc[d] += p * vs[j][d]; } }
    for (int d = 0; d < 32; ++d) o[(size_t)idx * 32 + d] = f2b(acc[d] / l);
}
__global__ void moe1_k(const u16* xn, const float* r, const u16* wg, const u16* wu, u16* h, int* idxo) {
    __shared__ int sidx; if (threadIdx.x == 0) { int bi = 0; float bv = r[0]; for (int i = 1; i < S; ++i) if (r[i] > bv) { bv = r[i]; bi = i; } sidx = bi; if (blockIdx.x == 0) *idxo = bi; } __syncthreads();
    int f = blockIdx.x * 256 + threadIdx.x; float g = 0, u = 0; for (int k = 0; k < D; ++k) { float xv = b2f(xn[sidx * D + k]); g += xv * b2f(wg[(size_t)k * F + f]); u += xv * b2f(wu[(size_t)k * F + f]); }
    g = rb(g); u = rb(u); float sg = rb(g / (1.f + expf(-g))); h[f] = f2b(sg * u);
}
__global__ void moe2_k(const u16* h, const u16* wd, const float* r, const int* idx, float* y) {
    int n = blockIdx.x * 256 + threadIdx.x; float a = 0; for (int f = 0; f < F; ++f) a += b2f(h[f]) * b2f(wd[(size_t)f * D + n]);
    float rs = rb(1.f / (1.f + expf(-r[*idx]))); y[n] = rb(rb(a) * rs);
}
__global__ void fin_k(const u16* x, const u16* xn, const u16* s, const float* y, const int* idx, u16* out) {
    int i = blockIdx.x * 256 + threadIdx.x; int t = i >> 10, n = i & 1023; float b = b2f(xn[i]); if (t == *idx) b = rb(b + y[n]);
    float g = rb(b / (1.f + expf(-b))); out[i] = f2b(b2f(x[i]) + rb(b2f(s[i]) * g));
}
extern "C" void kernel_launch(void* const* d_in, const int* in_sizes, int n_in, void* d_out, int out_size, void* d_ws, size_t ws_size, hipStream_t stream) {
    const u16 *x = (const u16*)d_in[0], *wq = (const u16*)d_in[1], *wk = (const u16*)d_in[2], *wv = (const u16*)d_in[3], *wo = (const u16*)d_in[4];
    const u16 *wr = (const u16*)d_in[6], *wg = (const u16*)d_in[7], *wu = (const u16*)d_in[8], *wd = (const u16*)d_in[9];
    char* w = (char*)d_ws; u16* xn = (u16*)w; u16* q = (u16*)(w + (4 << 20)); u16* o = (u16*)(w + (8 << 20)); u16* s = (u16*)(w + (12 << 20));
    u16* k = (u16*)(w + (16 << 20)); u16* v = (u16*)(w + (17 << 20)); float* r = (float*)(w + (18 << 20)); u16* h = (u16*)(w + (19 << 20)); float* y = (float*)(w + (20 << 20)); int* idx = (int*)(w + (21 << 20));
    ln_k<<<S, 256, 0, stream>>>(x, wr, xn, r);
    gemm_k<<<dim3(64, 128), dim3(16, 16), 0, stream>>>(xn, wq, q, S, 1024, 1024, 0.17677669529f * 1.44269504089f);
    gemm_k<<<dim3(2, 128), dim3(16, 16), 0, stream>>>(xn, wk, k, S, 32, 1024, 1.f);
    gemm_k<<<dim3(2, 128), dim3(16, 16), 0, stream>>>(xn, wv, v, S, 32, 1024, 1.f);
    attn_k<<<256, 256, 0, stream>>>(q, k, v, o);
    gemm_k<<<dim3(64, 128), dim3(16, 16), 0, stream>>>(o, wo, s, S, 1024, 1024, 1.f);
    moe1_k<<<14, 256, 0, stream>>>(xn, r, wg, wu, h, idx);
    moe2_k<<<4, 256, 0, stream>>>(h, wd, r, idx, y);
    fin_k<<<S * D / 256, 256, 0, stream>>>(x, xn, s, y, idx, (u16*)d_out);
}
```

```cpp
#include <hip/hip_runtime.h>
#include <hip/hip_bf16.h>
#include <stdint.h>

typedef unsigned long long u64;
typedef float f32x16 __attribute__((ext_vector_type(16)));
typedef float f32x4 __attribute__((ext_vector_type(4)));
typedef float f32x2 __attribute__((ext_vector_type(2)));
typedef unsigned u32x2 __attribute__((ext_vector_type(2)));
typedef unsigned u32x4 __attribute__((ext_vector_type(4)));
typedef unsigned u32x8 __attribute__((ext_vector_type(8)));
typedef __bf16 bf16x8 __attribute__((ext_vector_type(8)));
typedef __bf16 bf16x2 __attribute__((ext_vector_type(2)));
typedef short s16x4 __attribute__((ext_vector_type(4)));
#define LAS __attribute__((address_space(3)))
#define GAS __attribute__((address_space(1)))
#define DI __device__ __forceinline__

constexpr unsigned WS_EPOCH = 0, WS_FLAG1 = 4096, WS_STATS = 8192, WS_HG = 32768, WS_FLAG4 = 65536, WS_KV = 131072, WS_YG = 393216, WS_O = 1u << 20;
constexpr unsigned WS_DBG = 6u << 20;
constexpr unsigned SLOT = 24576, L_WKV = 73728, L_PART = 139264, L_QX = 24576, L_MISC = 155648, LDS_BYTES = 163840;
constexpr unsigned M_STAT = L_MISC, M_RP = L_MISC + 1024, M_JUNK = L_MISC + 2048, M_CTL = L_MISC + 3072, M_XSEL = L_MISC + 4096, M_GU = L_MISC + 6144, M_H = L_MISC + 7168;
constexpr unsigned KA = 122880, KB = 0, R2 = 32768, R3 = 65536, R4 = 98304, PRIV = 20480;
constexpr unsigned SPIN_CAP = 1u << 20;

DI float bf_lo(unsigned v) { return __uint_as_float(v << 16); }
DI float bf_hi(unsigned v) { return __uint_as_float(v & 0xffff0000u); }
DI unsigned pk2(float lo, float hi) { f32x2 v = {lo, hi}; return __builtin_bit_cast(unsigned, __builtin_convertvector(v, bf16x2)); }
DI float rb(float f) { return __uint_as_float((unsigned)__builtin_bit_cast(unsigned short, (__bf16)f) << 16); }
DI float xsum16(float v) { auto r = __builtin_amdgcn_permlane16_swap(__float_as_uint(v), __float_as_uint(v), false, false); return __uint_as_float(r[0]) + __uint_as_float(r[1]); }
DI float xsum32(float v) { auto r = __builtin_amdgcn_permlane32_swap(__float_as_uint(v), __float_as_uint(v), false, false); return __uint_as_float(r[0]) + __uint_as_float(r[1]); }
DI float xmax32(float v) { auto r = __builtin_amdgcn_permlane32_swap(__float_as_uint(v), __float_as_uint(v), false, false); return fmaxf(__uint_as_float(r[0]), __uint_as_float(r[1])); }
DI f32x16 mfma32(u32x4 a, u32x4 b, f32x16 c) { return __builtin_amdgcn_mfma_f32_32x32x16_bf16(__builtin_bit_cast(bf16x8, a), __builtin_bit_cast(bf16x8, b), c, 0, 0, 0); }
DI f32x4 mfma16(u32x4 a, u32x4 b, f32x4 c) { return __builtin_amdgcn_mfma_f32_16x16x32_bf16(__builtin_bit_cast(bf16x8, a), __builtin_bit_cast(bf16x8, b), c, 0, 0, 0); }
DI u32x2 trd(unsigned a) { return __builtin_bit_cast(u32x2, __builtin_amdgcn_ds_read_tr16_b64_v4i16((LAS s16x4*)a)); }
DI u32x4 trfrag(unsigned a, unsigned step) { u32x2 lo = trd(a), hi = trd(a + step); return (u32x4){lo[0], lo[1], hi[0], hi[1]}; }
DI u32x4 lds16(unsigned a) { return *(LAS u32x4*)a; }
DI float silu(float v) { return v * __builtin_amdgcn_rcpf(1.f + __builtin_amdgcn_exp2f(-1.44269504089f * v)); }
DI u64 ld8_sc1(const void* p) { return __hip_atomic_load((GAS const u64*)p, __ATOMIC_RELAXED, __HIP_MEMORY_SCOPE_AGENT); }
DI void st8_sc1(void* p, u64 v) { __hip_atomic_store((GAS u64*)p, v, __ATOMIC_RELAXED, __HIP_MEMORY_SCOPE_AGENT); }
DI int fresh_lane() { int l; asm volatile("v_mbcnt_lo_u32_b32 %0, -1, 0\n\tv_mbcnt_hi_u32_b32 %0, -1, %0" : "=v"(l)); return l; }
#define RELANE() const int lane = fresh_lane(), l15 = lane & 15, g4 = lane >> 4, l31 = lane & 31, h2 = lane >> 5; (void)l15; (void)g4; (void)l31; (void)h2
DI u64 mix(u32x2 e) { u64 z = (((u64)e[1] << 32) | e[0]) + 0x9E3779B97F4A7C15ull; z = (z ^ (z >> 30)) * 0xBF58476D1CE4E5B9ull; z = (z ^ (z >> 27)) * 0x94D049BB133111EBull; return z ^ (z >> 31); }
constexpr int CODE_BACK = 0, CODE_SPAN = 36608;
DI void dma1(const void* sbase, unsigned voff, unsigned m0v) { asm volatile("s_mov_b32 m0, %2\n\ts_nop 0\n\tglobal_load_lds_dwordx4 %0, %1" :: "v"(voff), "s"(sbase), "s"(m0v) : "memory"); }
DI void dma4(const void* sbase, unsigned voff, unsigned m0v) {
    asm volatile("s_mov_b32 m0, %2\n\ts_nop 0\n\tglobal_load_lds_dwordx4 %0, %1\n\tglobal_load_lds_dwordx4 %0, %1 offset:1024\n\tglobal_load_lds_dwordx4 %0, %1 offset:2048\n\tglobal_load_lds_dwordx4 %0, %1 offset:3072"
                 :: "v"(voff), "s"(sbase), "s"(m0v) : "memory"); }
DI void dma4_sc1(const void* sbase, unsigned voff, unsigned m0v) {
    asm volatile("s_mov_b32 m0, %2\n\ts_nop 0\n\tglobal_load_lds_dwordx4 %0, %1 sc1\n\tglobal_load_lds_dwordx4 %0, %1 offset:1024 sc1\n\tglobal_load_lds_dwordx4 %0, %1 offset:2048 sc1\n\tglobal_load_lds_dwordx4 %0, %1 offset:3072 sc1"
                 :: "v"(voff), "s"(sbase), "s"(m0v) : "memory"); }
#define BAR_ALL() asm volatile("s_waitcnt vmcnt(0) lgkmcnt(0)\n\ts_barrier" ::: "memory")
#define BAR_LDS() asm volatile("s_waitcnt lgkmcnt(0)\n\ts_barrier" ::: "memory")
#define WAIT_VM(n) asm volatile("s_waitcnt vmcnt(" #n ")" ::: "memory")

#define STAMP(i) do { } while (0)

#define EX(A,i) "v_exp_f32 v[" #A "+" #i "], v[" #A "+" #i "]\n"
#define E4(A,a,b,c,d) EX(A,a) EX(A,b) EX(A,c) EX(A,d)
#define CV(p,A,i) "v_cvt_pk_bf16_f32 v[104+" #p "], v[" #A "+" #i "], v[" #A "+" #i "+1]\n"
#define RDX(dst, o, k) "ds_read_b128 " dst ", %[ad] offset:" o "+" #k "\n"
#define M32(x) "v_mfma_f32_32x32x16_bf16 " x "\n"
#define TILE(A, B, o, X1, X2) \
    M32("v[" #B ":" #B "+15], v[112:115], v[96:99], v[64:79]") RDX("v[112:115]", o, 0) X1 E4(A,0,1,2,3) \
    M32("v[80:95], v[124:127], v[108:111], v[80:95]") RDX("v[124:127]", o, 3072) E4(A,4,5,6,7) \
    "v_smfmac_f32_16x16x64_bf16 v[24:27], v[28:31], v[104:111], v23\n" "s_waitcnt lgkmcnt(2)\n" CV(0,A,0) CV(1,A,2) CV(2,A,4) CV(3,A,6) \
    M32("v[" #B ":" #B "+15], v[116:119], v[100:103], v[" #B ":" #B "+15]") RDX("v[116:119]", o, 1024) X2 E4(A,8,9,10,11) \
    M32("v[80:95], v[120:123], v[104:107], v[80:95]") RDX("v[120:123]", o, 2048) E4(A,12,13,14,15) \
    "s_waitcnt lgkmcnt(2)\n" CV(4,A,8) CV(5,A,10) CV(6,A,12) CV(7,A,14)
#define NO ""
#define WOA(k) "s_add_u32 m0, %[ma], " #k "\n s_add_u32 s70, s70, 0x8000\n s_addc_u32 s71, s71, 0\n global_load_lds_dwordx4 %[vw], s[70:71]\n"
#define WOB(k) "s_add_u32 m0, %[mb], " #k "\n s_add_u32 s70, s70, 0x8000\n s_addc_u32 s71, s71, 0\n global_load_lds_dwordx4 %[vw], s[70:71]\n"
#define MOE(k) "s_add_u32 m0, %[mm], " #k "\n s_add_u32 s72, s72, %[ms]\n s_addc_u32 s73, s73, 0\n global_load_lds_dwordx4 %[vm], s[72:73]\n"
#define KV0 "s_mov_b32 m0, %[mk]\n s_nop 0\n global_load_lds_dwordx4 %[vl], %[sk] sc1\n"
#define KVN(k) "global_load_lds_dwordx4 %[vl], %[sk] offset:" #k " sc1\n"
#define BODY(a0,a1,b0,b1,c0,c1,d0,d1,e0,e1,f0,f1,g0,g1,h0,h1) asm volatile("s_nop 1\n s_setprio 3\n" TILE(32,48,"0",a0,a1) TILE(48,32,"4096",b0,b1) TILE(32,48,"8192",c0,c1) TILE(48,32,"12288",d0,d1) \
    "s_setprio 2\n" TILE(32,48,"16384",e0,e1) TILE(48,32,"20480",f0,f1) "s_setprio 1\n" TILE(32,48,"24576",g0,g1) "s_setprio 0\n" TILE(48,32,"28672",h0,h1) "s_waitcnt lgkmcnt(0)\n s_nop 7\n" \
    : "+{v[32:47]}"(SA), "={v[48:63]}"(SB), "+{v[80:95]}"(O), "+{v[24:27]}"(L), "+{v[104:111]}"(P), "+{v[112:119]}"(K), "+{v[120:127]}"(V), "+{s[70:71]}"(swo), "+{s[72:73]}"(smo) \
    : "{v[64:79]}"(NEG), "{v[96:103]}"(Q), "{v[28:31]}"(SEL), "{v23}"(IDX), [ad] "v"(ad), [vl] "v"(vl), [vw] "v"(vw), [vm] "v"(vm), \
      [ma] "s"(ma), [mb] "s"(mb), [mm] "s"(mm), [mk] "s"(mk), [ms] "s"(ms), [sk] "s"(sk) : "memory", "scc")

__global__ __launch_bounds__(512, 2) void fused_block(const char* __restrict__ x, const char* __restrict__ wq, const char* __restrict__ wk, const char* __restrict__ wv,
                                                      const char* __restrict__ wo, const char* __restrict__ wr, const char* __restrict__ wg, const char* __restrict__ wu,
                                                      const char* __restrict__ wd, char* __restrict__ out, char* __restrict__ ws) {
    extern __shared__ __attribute__((aligned(16))) char lds_raw[];
    const unsigned lds0 = (unsigned)(uintptr_t)lds_raw;
    const int tid = threadIdx.x, lane = tid & 63, w = __builtin_amdgcn_readfirstlane(tid >> 6);
    const int b = blockIdx.x, xcd = b & 7, r = b >> 3, tb = (xcd >> 1) * 8 + (r & 7), hg = (xcd & 1) * 4 + (r >> 3);
    const int isV = xcd & 1, sub = hg & 3, T0 = tb * 64 + sub * 16;
    const int l15 = lane & 15, g4 = lane >> 4, l31 = lane & 31, h2 = lane >> 5;
    const int hd = w & 3, tbk = w >> 2;
    const unsigned vl = lane * 16;
    STAMP(0);
    const int koff = (2 * hg + 4 * (tb & 3) + xcd) & 15;
    f32x16 qa; float csum;
    if (w < 4) {
        u32x2 ev; u32x4 xq[8], wrq[8];
        {
            const char* xp = x + (size_t)(T0 + l15) * 2048 + w * 512 + g4 * 16; const char* rp = wr + w * 512 + g4 * 16;
            asm volatile("global_load_dwordx4 %0, %8, off\n\tglobal_load_dwordx4 %1, %8, off offset:64\n\tglobal_load_dwordx4 %2, %8, off offset:128\n\tglobal_load_dwordx4 %3, %8, off offset:192\n\t"
                         "global_load_dwordx4 %4, %8, off offset:256\n\tglobal_load_dwordx4 %5, %8, off offset:320\n\tglobal_load_dwordx4 %6, %8, off offset:384\n\tglobal_load_dwordx4 %7, %8, off offset:448"
                         : "=&v"(xq[0]), "=&v"(xq[1]), "=&v"(xq[2]), "=&v"(xq[3]), "=&v"(xq[4]), "=&v"(xq[5]), "=&v"(xq[6]), "=&v"(xq[7]) : "v"(xp));
            asm volatile("global_load_dwordx4 %0, %8, off\n\tglobal_load_dwordx4 %1, %8, off offset:64\n\tglobal_load_dwordx4 %2, %8, off offset:128\n\tglobal_load_dwordx4 %3, %8, off offset:192\n\t"
                         "global_load_dwordx4 %4, %8, off offset:256\n\tglobal_load_dwordx4 %5, %8, off offset:320\n\tglobal_load_dwordx4 %6, %8, off offset:384\n\tglobal_load_dwordx4 %7, %8, off offset:448"
                         : "=&v"(wrq[0]), "=&v"(wrq[1]), "=&v"(wrq[2]), "=&v"(wrq[3]), "=&v"(wrq[4]), "=&v"(wrq[5]), "=&v"(wrq[6]), "=&v"(wrq[7]) : "v"(rp));
            asm volatile("global_load_dwordx2 %0, %1, %2 sc1" : "=v"(ev) : "v"(0), "s"(ws));
            const char* wkv = (isV ? wv : wk) + w * 16384;
            for (int i = 0; i < 4; ++i) dma4(wkv + i * 4096, vl, lds0 + L_WKV + w * 16384 + i * 4096);
            asm volatile("s_waitcnt vmcnt(16)" : "+v"(ev), "+v"(xq[0]), "+v"(xq[1]), "+v"(xq[2]), "+v"(xq[3]), "+v"(xq[4]), "+v"(xq[5]), "+v"(xq[6]), "+v"(xq[7]),
                         "+v"(wrq[0]), "+v"(wrq[1]), "+v"(wrq[2]), "+v"(wrq[3]), "+v"(wrq[4]), "+v"(wrq[5]), "+v"(wrq[6]), "+v"(wrq[7]));
        }
        STAMP(8);
        const u32x4 ones = {0x3F803F80u, 0x3F803F80u, 0x3F803F80u, 0x3F803F80u}; const f32x4 z4 = {0, 0, 0, 0};
        f32x4 s1 = z4, gr = z4, sw = z4, a0 = z4, a1 = z4, c0 = z4, c1 = z4;
        const unsigned oth = l15 == 1 ? 0x3F803F80u : 0u;
        for (int s = 0; s < 8; ++s) { u32x4 aw; for (int i = 0; i < 4; ++i) aw[i] = l15 == 0 ? wrq[s][i] : oth;
            gr = mfma16(xq[s], xq[s], gr); s1 = mfma16(aw, xq[s], s1); sw = mfma16(aw, ones, sw); }
        WAIT_VM(0);
        STAMP(9);
        const unsigned wa = lds0 + L_WKV + w * 16384 + (8 * g4 + (l15 >> 2)) * 64 + (lane & 3) * 8;
        if (isV) { for (int s = 0; s < 8; ++s) { const u32x4 w0 = trfrag(wa + s * 2048, 256), w1 = trfrag(wa + s * 2048 + 32, 256);
                       a0 = mfma16(xq[s], w0, a0); a1 = mfma16(xq[s], w1, a1); c0 = mfma16(ones, w0, c0); c1 = mfma16(ones, w1, c1); } }
        else { for (int s = 0; s < 8; ++s) { const u32x4 w0 = trfrag(wa + s * 2048, 256), w1 = trfrag(wa + s * 2048 + 32, 256);
                       a0 = mfma16(w0, xq[s], a0); a1 = mfma16(w1, xq[s], a1); c0 = mfma16(w0, ones, c0); c1 = mfma16(w1, ones, c1); } }
        const unsigned pa = lds0 + L_PART + w * 4096 + vl;
        *(LAS f32x4*)pa = a0; *(LAS f32x4*)(pa + 1024) = c0; *(LAS f32x4*)(pa + 2048) = a1; *(LAS f32x4*)(pa + 3072) = c1;
        if (lane < 16) { *(LAS float*)(lds0 + M_STAT + l15 * 32 + w * 4) = s1[1]; *(LAS f32x2*)(lds0 + M_RP + l15 * 32 + w * 8) = (f32x2){s1[0], sw[0]}; }
        if (g4 == (l15 >> 2)) *(LAS float*)(lds0 + M_STAT + l15 * 32 + 16 + w * 4) = (l15 & 2) ? ((l15 & 1) ? gr[3] : gr[2]) : ((l15 & 1) ? gr[1] : gr[0]);
        if (tid == 0) *(LAS u32x2*)(lds0 + M_CTL) = ev;
        BAR_LDS();
        STAMP(1);
        unsigned key = 0;
        if (w < 3) {
            const f32x4 t0 = *(LAS f32x4*)(lds0 + M_STAT + l15 * 32), t1 = *(LAS f32x4*)(lds0 + M_STAT + l15 * 32 + 16);
            const float mean = ((t0[0] + t0[1]) + (t0[2] + t0[3])) * (1.f / 1024.f), var = fmaxf(((t1[0] + t1[1]) + (t1[2] + t1[3])) * (1.f / 1024.f) - mean * mean, 0.f);
            const float mrow = rb(mean), rsrow = 1.f / rb(__builtin_amdgcn_sqrtf(rb(rb(var) + 1e-5f)));
            if (w < 2) {
                f32x4 a = z4, c = z4; for (int i = 0; i < 4; ++i) { a += *(LAS f32x4*)(lds0 + L_PART + i * 4096 + w * 2048 + vl); c += *(LAS f32x4*)(lds0 + L_PART + i * 4096 + w * 2048 + 1024 + vl); }
                const int kt = T0 >> 5, sh = sub & 1; unsigned off; float o[4];
                if (isV) { for (int i = 0; i < 4; ++i) o[i] = (a[i] - __shfl(mrow, 4 * g4 + i) * c[i]) * __shfl(rsrow, 4 * g4 + i);
                    off = ((kt - 1 + sh) & 63) * 4096 + 2048 + sh * 1024 + ((g4 & 1) * 32 + w * 16 + l15) * 16 + (g4 >> 1) * 8; }
                else { for (int i = 0; i < 4; ++i) o[i] = (a[i] - mrow * c[i]) * rsrow;
                    off = ((kt - 2) & 63) * 4096 + w * 1024 + ((g4 & 1) * 32 + sh * 16 + l15) * 16 + (g4 >> 1) * 8; }
                st8_sc1(ws + WS_KV + off, ((u64)pk2(o[2], o[3]) << 32) | pk2(o[0], o[1]));
            } else if (!isV) {
                const f32x4 p0 = *(LAS f32x4*)(lds0 + M_RP + l15 * 32), p1 = *(LAS f32x4*)(lds0 + M_RP + l15 * 32 + 16);
                const unsigned u = __float_as_uint(rb((((p0[0] + p0[2]) + (p1[0] + p1[2])) - mrow * ((p0[1] + p0[3]) + (p1[1] + p1[3]))) * rsrow)) >> 16;
                key = ((((u & 0x8000u) ? ~u : (u | 0x8000u)) & 0xffffu) << 11) | (2047 - (T0 + l15));
                for (int o = 1; o < 16; o <<= 1) key = max(key, (unsigned)__shfl_xor((int)key, o));
                if (lane < 16) st8_sc1(ws + WS_STATS + (T0 + l15) * 8, ((u64)__float_as_uint(rsrow) << 32) | __float_as_uint(mrow));
            }
        }
        const unsigned sv = (((l15 >> 2) & 1) == (g4 & 1)) ? 0x3F803F80u : 0u; const u32x4 sel = {sv, sv, sv, sv};
        const unsigned xa = lds0 + l31 * 128 + ((h2 ^ ((l31 >> 1) & 7)) << 4);
        const unsigned wq_a = lds0 + 8192 + (8 * h2 + (l15 >> 2)) * 256 + ((hd ^ ((lane >> 2) & 3)) << 6) + (g4 & 1) * 32 + (lane & 3) * 8;
        f32x16 qb; f32x4 cs = z4; for (int i = 0; i < 16; ++i) { qa[i] = 0.f; qb[i] = 0.f; }
        u32x4 af[2][4], b0[2][4], b1[2][4];
#define QRD(p, s) do { const unsigned sl_ = ((s) % 5) * SLOT; for (int ks = 0; ks < 4; ++ks) { \
            af[p][ks] = trfrag(wq_a + sl_ + ks * 4096, 1024); b0[p][ks] = lds16((xa ^ (ks * 32)) + sl_); b1[p][ks] = lds16((xa ^ (ks * 32)) + sl_ + 4096); } } while (0)
        BAR_LDS(); QRD(0, 0);
#pragma unroll
        for (int s = 0; s < 16; ++s) {
            if (s < 15) {
                if (s == 1) WAIT_VM(0);
                BAR_LDS();
                if (s == 1 && tid == 128) { const u32x2 e = *(LAS u32x2*)(lds0 + M_CTL); st8_sc1(ws + WS_FLAG1 + b * 8, (mix(e) & ~0xFFFFFFFull) | key); }
                QRD((s + 1) & 1, s + 1);
            }
            for (int ks = 0; ks < 4; ++ks) { qa = mfma32(af[s & 1][ks], b0[s & 1][ks], qa); qb = mfma32(af[s & 1][ks], b1[s & 1][ks], qb); cs = mfma16(sel, af[s & 1][ks], cs); }
            if (s < 15) for (int i = 0; i < 8; ++i) { __builtin_amdgcn_sched_group_barrier(0x008, 1, 0); __builtin_amdgcn_sched_group_barrier(0x100, 2, 0); }
            __builtin_amdgcn_sched_barrier(0);
        }
        for (int q = 0; q < 4; ++q) *(LAS f32x4*)(lds0 + L_QX + w * 4096 + q * 1024 + vl) = (f32x4){qb[4 * q], qb[4 * q + 1], qb[4 * q + 2], qb[4 * q + 3]};
        csum = cs[0]; *(LAS float*)(lds0 + M_RP + w * 256 + lane * 4) = csum;
        STAMP(2);
    } else {
        const int lw = w - 4;
        unsigned vx[2], vq[4];
        for (int i = 0; i < 2; ++i) { const int row = lw * 16 + i * 8 + (lane >> 3); vx[i] = row * 2048 + (((lane & 7) ^ ((row >> 1) & 7)) << 4); }
        for (int i = 0; i < 4; ++i) vq[i] = (lw * 16 + i * 4 + g4) * 2048 + ((((lane >> 2) & 3) ^ g4) << 6) + ((lane & 3) << 4);
        const char* xb = x + (size_t)tb * 64 * 2048; const char* qb_ = wq + hg * 256;
#define RING_ISSUE(s, sl) do { const int kk_ = ((s) + koff) & 15; \
        asm volatile("s_mov_b32 m0, %8\n\ts_nop 0\n\tglobal_load_lds_dwordx4 %0, %6\n\ts_add_u32 m0, m0, 1024\n\ts_nop 0\n\tglobal_load_lds_dwordx4 %1, %6\n\t" \
                     "s_mov_b32 m0, %9\n\ts_nop 0\n\tglobal_load_lds_dwordx4 %2, %7\n\ts_add_u32 m0, m0, 1024\n\ts_nop 0\n\tglobal_load_lds_dwordx4 %3, %7\n\t" \
                     "s_add_u32 m0, m0, 1024\n\ts_nop 0\n\tglobal_load_lds_dwordx4 %4, %7\n\ts_add_u32 m0, m0, 1024\n\ts_nop 0\n\tglobal_load_lds_dwordx4 %5, %7" \
                     :: "v"(vx[0]), "v"(vx[1]), "v"(vq[0]), "v"(vq[1]), "v"(vq[2]), "v"(vq[3]), "s"(xb + kk_ * 128), "s"(qb_ + (size_t)kk_ * 131072), \
                        "s"(lds0 + (sl) * SLOT + lw * 2048), "s"(lds0 + (sl) * SLOT + 8192 + lw * 4096) : "memory", "scc"); } while (0)
        {
            u64 pc; asm volatile("s_getpc_b64 %0" : "=s"(pc));
            const char* cb = (const char*)(pc & ~127ull) - CODE_BACK;
            for (int i = 0; i < 3; ++i) dma1(cb + min((i * 128 + r * 4 + lw) * 128, CODE_SPAN - 128), (lane & 7) * 16, lds0 + M_JUNK);
        }
        RING_ISSUE(0, 0); RING_ISSUE(1, 1); RING_ISSUE(2, 2);
        BAR_LDS();
        RING_ISSUE(3, 3); RING_ISSUE(4, 4);
        WAIT_VM(24); BAR_LDS();
        int sl = 0;
#pragma unroll 1
        for (int s = 0; s < 11; ++s) { WAIT_VM(18); BAR_LDS(); RING_ISSUE(s + 5, sl); sl = sl == 4 ? 0 : sl + 1; }
        WAIT_VM(18); BAR_LDS(); WAIT_VM(12); BAR_LDS(); WAIT_VM(6); BAR_LDS(); WAIT_VM(0); BAR_LDS();
        if (w == 7) {
            const u32x2 e = *(LAS u32x2*)(lds0 + M_CTL); const u64 tag36 = mix(e) & ~0xFFFFFFFull;
            unsigned key = 0;
            for (unsigned spins = 0; spins < SPIN_CAP; ++spins) {
                u64 f[4]; for (int i = 0; i < 4; ++i) f[i] = ld8_sc1(ws + WS_FLAG1 + (i * 64 + lane) * 8);
                bool ok = true; key = 0; for (int i = 0; i < 4; ++i) { ok &= ((f[i] ^ tag36) >> 28) == 0; key = max(key, (unsigned)f[i] & 0xFFFFFFFu); }
                if (__all(ok)) break;
                __builtin_amdgcn_s_sleep(2);
            }
            for (int o = 1; o < 64; o <<= 1) key = max(key, (unsigned)__shfl_xor((int)key, o));
            if (lane == 0) { *(LAS unsigned*)(lds0 + M_CTL + 8) = key; if (b == 0) st8_sc1(ws + WS_EPOCH, (((u64)e[1] << 32) | e[0]) + 1); }
        }
    }
    BAR_LDS();
    STAMP(3);
    const u32x4 ctl = lds16(lds0 + M_CTL);
    const u64 seed = mix((u32x2){(unsigned)__builtin_amdgcn_readfirstlane(ctl[0]), (unsigned)__builtin_amdgcn_readfirstlane(ctl[1])});
    const unsigned tag32 = (unsigned)seed ^ 0x5bd1e995u, gkey = __builtin_amdgcn_readfirstlane(ctl[2]);
    const int idx = 2047 - (gkey & 2047);
    const int c0 = r & 7; const char* kv = ws + WS_KV;
    const bool moe = r < 28;
    u32x8 Q, K, V, P; f32x16 SA, SB, NEG, O; f32x4 L; u32x4 SEL; unsigned IDX;
    float mtok, rstok;
    {
        dma4_sc1(kv + c0 * 32768 + w * 4096, vl, lds0 + KA + w * 4096);
        if (w < 2) {
            dma1(x + (size_t)idx * 2048 + w * 1024, vl, lds0 + M_XSEL + w * 1024);
            asm volatile("s_mov_b32 m0, %2\n\ts_nop 0\n\tglobal_load_lds_dword %0, %1 sc1" :: "v"((lane & 1) * 4), "s"(ws + WS_STATS + idx * 8), "s"(lds0 + M_JUNK + w * 256) : "memory");
        }
        if (w >= 4) { for (int q = 0; q < 4; ++q) { const f32x4 t = *(LAS f32x4*)(lds0 + L_QX + hd * 4096 + q * 1024 + vl); for (int i = 0; i < 4; ++i) qa[4 * q + i] = t[i]; }
            csum = *(LAS float*)(lds0 + M_RP + hd * 256 + lane * 4); }
        u32x4 k00, k01, k10, k11, v00; u32x2 st;
        asm volatile("global_load_dwordx2 %0, %6, %7 sc1\n\tglobal_load_dwordx4 %1, %8, %9 sc1\n\tglobal_load_dwordx4 %2, %8, %9 offset:1024 sc1\n\t"
                     "global_load_dwordx4 %3, %8, %10 sc1\n\tglobal_load_dwordx4 %4, %8, %10 offset:1024 sc1\n\tglobal_load_dwordx4 %5, %8, %10 offset:2048 sc1\n\ts_waitcnt vmcnt(0)"
                     : "=&v"(st), "=&v"(k00), "=&v"(k01), "=&v"(k10), "=&v"(k11), "=&v"(v00)
                     : "v"((tb * 64 + tbk * 32 + l31) * 8), "s"(ws + WS_STATS), "v"(vl), "s"(kv + ((c0 * 8 + 62) & 63) * 4096), "s"(kv + ((c0 * 8 + 63) & 63) * 4096) : "memory");
        mtok = __uint_as_float(st[0]); rstok = __uint_as_float(st[1]);
        const float c1 = rb(csum), c2 = rb(csum - c1), c3 = csum - c1 - c2;
        const u32x4 ca = {h2 ? 0u : pk2(c1, c2), h2 ? 0u : pk2(c3, 0.f), 0u, 0u}, cb = {h2 ? 0u : pk2(-mtok, -mtok), h2 ? 0u : pk2(-mtok, 0.f), 0u, 0u};
        qa = mfma32(ca, cb, qa);
        const float qs = rstok * (0.17677669529663687f * 1.4426950408889634f);
        for (int i = 0; i < 8; ++i) Q[i] = pk2(qa[2 * i] * qs, qa[2 * i + 1] * qs);
        f32x16 z; for (int i = 0; i < 16; ++i) z[i] = 0.f;
        f32x16 s0 = mfma32(k00, __builtin_shufflevector(Q, Q, 0, 1, 2, 3), z); s0 = mfma32(k01, __builtin_shufflevector(Q, Q, 4, 5, 6, 7), s0);
        float m = s0[0]; for (int i = 1; i < 16; ++i) m = fmaxf(m, s0[i]);
        m = xmax32(m);
        for (int i = 0; i < 16; ++i) { NEG[i] = -m; SA[i] = s0[i] - m; O[i] = 0.f; }
        K = __builtin_shufflevector(k10, k11, 0, 1, 2, 3, 4, 5, 6, 7); const u32x4 z4 = {0, 0, 0, 0}; V = __builtin_shufflevector(v00, z4, 0, 1, 2, 3, 4, 5, 6, 7);
        P = (u32x8){0, 0, 0, 0, 0, 0, 0, 0}; L = (f32x4){0, 0, 0, 0};
        const unsigned one = 0x3F803F80u; SEL = (u32x4){l15 < 2 ? one : 0u, l15 < 2 ? one : 0u, (l15 & 14) == 2 ? one : 0u, (l15 & 14) == 2 ? one : 0u};
        IDX = (lane & 1) ? 0xEEEEEEEEu : 0x44444444u;
        asm volatile("" : "+v"(NEG), "+v"(SEL), "+v"(IDX));
        if (w < 2) {
            const f32x2 sx = *(LAS f32x2*)(lds0 + M_JUNK + w * 256); u32x4 v = lds16(lds0 + M_XSEL + tid * 16);
            for (int i = 0; i < 4; ++i) { const unsigned d = pk2(bf_lo(v[i]) - sx[0], bf_hi(v[i]) - sx[0]); v[i] = pk2(bf_lo(d) * sx[1], bf_hi(d) * sx[1]); }
            *(LAS u32x4*)(lds0 + M_XSEL + tid * 16) = v;
        }
    }
    STAMP(4);
    const int cbk = hd, kh = tbk, rm = min(r, 27);
#define MO() ((8 * g4 + (l15 >> 2)) * 32 + (lane & 3) * 8)
    const unsigned wt = (4 * h2 + (l15 >> 2)) * 64 + (g4 & 1) * 32 + (lane & 3) * 8;
    const unsigned vw = (lane >> 2) * 2048 + (lane & 3) * 16;
    const unsigned ma = lds0 + R4 + w * 2048, mb = lds0 + R3 + w * 4096;
    u64 swo = (u64)(wo + (size_t)kh * 512 * 2048 + hg * 256 + cbk * 64) - 0x8000, smo;
    u32x4 wf[32];
#define WO_RD(j, base) wf[j] = trfrag((base) + wt, 512)
#define SEAM(c) const unsigned ad = lds0 + (((c) & 1) ? KB : KA) + vl, mk = lds0 + (((c) & 1) ? KA : KB) + w * 4096; const char* sk = kv + ((c0 + (c) + 1) & 7) * 32768 + w * 4096
    { BAR_LDS(); SEAM(0); const unsigned vm = (lane >> 1) * 7168 + (lane & 1) * 16, mm = lds0 + R2 + w * 4096, ms = 32 * 7168; smo = (u64)(wg + (xcd * 28 + rm) * 32 + (size_t)w * 128 * 7168) - ms;
      BODY(WOA(0), NO, WOA(1024), NO, KV0, KVN(1024), KVN(2048), KVN(3072), MOE(0), NO, MOE(1024), NO, MOE(2048), NO, MOE(3072), NO); }
    { WAIT_VM(4); WO_RD(0, ma); WO_RD(1, ma + 1024); BAR_LDS(); SEAM(1); RELANE();
      const unsigned vm = (lane >> 1) * 7168 + (lane & 1) * 16, mm = mb, ms = 32 * 7168; smo = (u64)(wu + (xcd * 28 + rm) * 32 + (size_t)w * 128 * 7168) - ms;
      BODY(WOA(0), NO, WOA(1024), NO, KV0, KVN(1024), KVN(2048), KVN(3072), MOE(0), NO, MOE(1024), NO, MOE(2048), NO, MOE(3072), NO); }
    { WAIT_VM(4); WO_RD(2, ma); WO_RD(3, ma + 1024); BAR_LDS(); SEAM(2); const unsigned vm = 0, mm = 0, ms = 0;
      BODY(WOA(0), NO, WOA(1024), NO, KV0, NO, KVN(1024), NO, KVN(2048), NO, KVN(3072), NO, NO, NO, NO, NO); }
    { WAIT_VM(0); WO_RD(4, ma); WO_RD(5, ma + 1024); RELANE(); const unsigned mo = MO();
      f32x4 ga = {0, 0, 0, 0}, ua = ga;
      for (int s = 0; s < 4; ++s) { const u32x4 xf = lds16(lds0 + M_XSEL + w * 256 + s * 64 + g4 * 16);
          ga = mfma16(xf, trfrag(lds0 + R2 + w * 4096 + s * 1024 + mo, 128), ga); ua = mfma16(xf, trfrag(mb + s * 1024 + mo, 128), ua); }
      if (lane < 16) { *(LAS float*)(lds0 + M_GU + l15 * 32 + w * 4) = ga[0]; *(LAS float*)(lds0 + M_GU + 512 + l15 * 32 + w * 4) = ua[0]; }
      BAR_LDS(); SEAM(3);
      if (w == 0 && moe) {
          const f32x4 g0 = *(LAS f32x4*)(lds0 + M_GU + l15 * 32), g1 = *(LAS f32x4*)(lds0 + M_GU + l15 * 32 + 16), u0 = *(LAS f32x4*)(lds0 + M_GU + 512 + l15 * 32), u1 = *(LAS f32x4*)(lds0 + M_GU + 512 + l15 * 32 + 16);
          const float gv = rb(((g0[0] + g0[1]) + (g0[2] + g0[3])) + ((g1[0] + g1[1]) + (g1[2] + g1[3]))), uv = rb(((u0[0] + u0[1]) + (u0[2] + u0[3])) + ((u1[0] + u1[1]) + (u1[2] + u1[3])));
          const float hv = rb(silu(gv)) * uv, hn = __shfl_xor(hv, 1);
          if (lane < 16 && !(lane & 1)) st8_sc1(ws + WS_HG + ((xcd * 28 + r) * 8 + (lane >> 1)) * 8, ((u64)tag32 << 32) | pk2(hv, hn));
      }
      const unsigned vm = (lane >> 1) * 2048 + (lane & 1) * 16, mm = lds0 + R2 + w * 4096, ms = 65536; smo = (u64)(wd + (size_t)rm * 128 * 2048 + xcd * 256 + w * 32) - ms;
      BODY(WOA(0), WOA(1024), WOB(0), WOB(1024), WOB(2048), WOB(3072), KV0, KVN(1024), KVN(2048), KVN(3072), MOE(0), MOE(1024), MOE(2048), NO, MOE(3072), NO); }
    { WAIT_VM(4); WO_RD(6, ma); WO_RD(7, ma + 1024); WO_RD(8, mb); WO_RD(9, mb + 1024); WO_RD(10, mb + 2048); WO_RD(11, mb + 3072); BAR_LDS(); SEAM(4); const unsigned vm = 0, mm = 0, ms = 0;
      BODY(WOA(0), WOA(1024), WOB(0), WOB(1024), WOB(2048), WOB(3072), KV0, KVN(1024), KVN(2048), KVN(3072), NO, NO, NO, NO, NO, NO); }
    { WAIT_VM(0); WO_RD(12, ma); WO_RD(13, ma + 1024); WO_RD(14, mb); WO_RD(15, mb + 1024); WO_RD(16, mb + 2048); WO_RD(17, mb + 3072); BAR_LDS(); SEAM(5); const unsigned vm = 0, mm = 0, ms = 0; RELANE();
      if (w == 0) asm volatile("s_mov_b32 m0, %2\n\ts_nop 0\n\tglobal_load_lds_dwordx4 %0, %1 sc1" :: "v"(l31 * 16), "s"(ws + WS_HG + rm * 512), "s"(lds0 + M_JUNK) : "memory");
      BODY(WOA(0), WOA(1024), WOB(0), WOB(1024), WOB(2048), WOB(3072), KV0, KVN(1024), KVN(2048), KVN(3072), NO, NO, NO, NO, NO, NO); }
    { WAIT_VM(0); WO_RD(18, ma); WO_RD(19, ma + 1024); WO_RD(20, mb); WO_RD(21, mb + 1024); WO_RD(22, mb + 2048); WO_RD(23, mb + 3072); RELANE(); const unsigned mo = MO();
      if (w == 0 && moe) {
          u32x2 hgr = *(LAS u32x2*)(lds0 + M_JUNK + lane * 8);
          for (unsigned spins = 0; spins < SPIN_CAP && !__all(hgr[1] == tag32); ++spins) { __builtin_amdgcn_s_sleep(2); const u64 v = ld8_sc1(ws + WS_HG + (r * 64 + lane) * 8); hgr = (u32x2){(unsigned)v, (unsigned)(v >> 32)}; }
          *(LAS unsigned*)(lds0 + M_H + lane * 4) = hgr[0];
      }
      BAR_LDS(); SEAM(6); const unsigned vm = 0, mm = 0, ms = 0;
      if (moe) {
          f32x4 ya = {0, 0, 0, 0};
          for (int s = 0; s < 4; ++s) ya = mfma16(lds16(lds0 + M_H + s * 64 + g4 * 16), trfrag(lds0 + R2 + w * 4096 + s * 1024 + mo, 128), ya);
          if (lane < 16) st8_sc1(ws + WS_YG + (r * 1024 + xcd * 128 + w * 16 + l15) * 8, ((u64)tag32 << 32) | __float_as_uint(ya[0]));
      }
      BODY(WOA(0), WOA(1024), WOB(0), WOB(1024), WOB(2048), WOB(3072), KV0, KVN(1024), KVN(2048), KVN(3072), NO, NO, NO, NO, NO, NO); }
    { WAIT_VM(0); WO_RD(24, ma); WO_RD(25, ma + 1024); WO_RD(26, mb); WO_RD(27, mb + 1024); WO_RD(28, mb + 2048); WO_RD(29, mb + 3072); BAR_LDS(); SEAM(7);
      const unsigned vm = vw, mm = mb, ms = 0x8000; smo = (u64)(x + (size_t)(tb * 64 + kh * 32) * 2048 + hg * 256 + cbk * 64) - ms;
      BODY(WOA(0), NO, WOA(1024), NO, MOE(0), NO, MOE(1024), NO, NO, NO, NO, NO, NO, NO, NO, NO); }
    STAMP(5);
    {
    RELANE();
    {
        O = mfma32(__builtin_shufflevector(V, V, 4, 5, 6, 7), __builtin_shufflevector(P, P, 4, 5, 6, 7), O);
        asm volatile("s_nop 1\n\tv_smfmac_f32_16x16x64_bf16 %0, %1, %2, %3\n\ts_nop 7\n\ts_nop 7" : "+v"(L) : "v"(SEL), "v"(P), "v"(IDX));
        const float llo = __shfl(L[0] + L[1], l15), lhi = __shfl(L[2] + L[3], l15);
        float lsum = (lane & 16) ? lhi : llo;
        float om = 0.f; for (int i = 0; i < 16; ++i) om = fmaxf(om, fabsf(O[i]));
        if (__builtin_expect(__any(!(lsum <= 1.8446744e19f) || !(om <= 3.0e38f)), 0)) {
            float mr = -INFINITY; lsum = 0.f; for (int i = 0; i < 16; ++i) O[i] = 0.f;
#pragma unroll 1
            for (int t = 0; t < 64; ++t) {
                u32x4 k0, k1, v0, v1;
                asm volatile("global_load_dwordx4 %0, %4, %5 sc1\n\tglobal_load_dwordx4 %1, %4, %5 offset:1024 sc1\n\tglobal_load_dwordx4 %2, %4, %6 offset:2048 sc1\n\tglobal_load_dwordx4 %3, %4, %7 offset:3072 sc1\n\ts_waitcnt vmcnt(0)"
                             : "=&v"(k0), "=&v"(k1), "=&v"(v0), "=&v"(v1) : "v"(vl), "s"(kv + ((t + 62) & 63) * 4096), "s"(kv + ((t + 63) & 63) * 4096), "s"(kv + t * 4096) : "memory");
                f32x16 z; for (int i = 0; i < 16; ++i) z[i] = 0.f;
                f32x16 s = mfma32(k0, __builtin_shufflevector(Q, Q, 0, 1, 2, 3), z); s = mfma32(k1, __builtin_shufflevector(Q, Q, 4, 5, 6, 7), s);
                float m = s[0]; for (int i = 1; i < 16; ++i) m = fmaxf(m, s[i]);
                m = fmaxf(mr, xmax32(m)); const float al = __builtin_amdgcn_exp2f(mr - m); mr = m;
                u32x8 pp; float ps = 0.f;
                for (int i = 0; i < 8; ++i) { pp[i] = pk2(__builtin_amdgcn_exp2f(s[2 * i] - m), __builtin_amdgcn_exp2f(s[2 * i + 1] - m)); ps += bf_lo(pp[i]) + bf_hi(pp[i]); }
                lsum = lsum * al + xsum32(ps); for (int i = 0; i < 16; ++i) O[i] *= al;
                O = mfma32(v0, __builtin_shufflevector(pp, pp, 0, 1, 2, 3), O); O = mfma32(v1, __builtin_shufflevector(pp, pp, 4, 5, 6, 7), O);
            }
        }
        const float rl = __builtin_amdgcn_rcpf(lsum);
        u32x4 o0, o1; for (int i = 0; i < 4; ++i) { o0[i] = pk2(O[2 * i] * rl, O[2 * i + 1] * rl); o1[i] = pk2(O[8 + 2 * i] * rl, O[8 + 2 * i + 1] * rl); }
        asm volatile("global_store_dwordx4 %0, %1, %3 sc1\n\tglobal_store_dwordx4 %0, %2, %3 offset:2048 sc1\n\ts_waitcnt vmcnt(2)"
                     :: "v"(vl), "v"(o0), "v"(o1), "s"(ws + WS_O + (size_t)(tb * 8 + hg) * 16384 + hd * 4096 + tbk * 1024) : "memory");
    }
    WO_RD(30, ma); WO_RD(31, ma + 1024);
    u32x2 xr[4]; for (int q = 0; q < 4; ++q) xr[q] = *(LAS u32x2*)(mb + l31 * 64 + q * 16 + h2 * 8);
    BAR_LDS();
    const unsigned pv = lds0 + w * PRIV;
    unsigned xn2[8], g2[8];
    for (int i = 0; i < 8; ++i) { const unsigned xx = xr[i >> 1][i & 1], d = pk2(bf_lo(xx) - mtok, bf_hi(xx) - mtok); xn2[i] = pk2(bf_lo(d) * rstok, bf_hi(d) * rstok); }
    for (int i = 0; i < 4; ++i) g2[i] = pk2(silu(bf_lo(xn2[i])), silu(bf_hi(xn2[i])));
    for (int i = 0; i < 4; ++i) asm volatile("" : "+v"(g2[i]));
    WAIT_VM(0);
    if (lane == 0) st8_sc1(ws + WS_FLAG4 + b * 128 + w * 8, seed);
    for (int i = 4; i < 8; ++i) g2[i] = pk2(silu(bf_lo(xn2[i])), silu(bf_hi(xn2[i])));
    if (tb == (idx >> 6) && kh == ((idx >> 5) & 1)) {
        float acc = 0.f;
        for (unsigned spins = 0; spins < SPIN_CAP; ++spins) {
            u64 f[14]; for (int i = 0; i < 14; ++i) f[i] = ld8_sc1(ws + WS_YG + ((h2 * 14 + i) * 1024 + hg * 128 + cbk * 32 + l31) * 8);
            bool ok = true; acc = 0.f; for (int i = 0; i < 14; ++i) { ok &= (unsigned)(f[i] >> 32) == tag32; acc += __uint_as_float((unsigned)f[i]); }
            if (__all(ok)) break;
            __builtin_amdgcn_s_sleep(2);
        }
        const unsigned o16 = (gkey >> 11) & 0xffffu, rbits = (o16 & 0x8000u) ? (o16 & 0x7fffu) : (~o16 & 0xffffu);
        const float rsel = rb(__builtin_amdgcn_rcpf(1.f + __builtin_amdgcn_exp2f(-1.44269504089f * bf_lo(rbits))));
        const float y = rb(rb(xsum32(acc)) * rsel);
#pragma unroll 1
        for (int i = 0; i < 8; ++i) {
            const int n = 2 * (i & 1) + 8 * (i >> 1) + 4 * h2; unsigned xx = xn2[0]; for (int q = 1; q < 8; ++q) xx = i == q ? xn2[q] : xx;
            const unsigned t = pk2(silu(rb(bf_lo(xx) + __shfl(y, n))), silu(rb(bf_hi(xx) + __shfl(y, n + 1))));
            for (int q = 0; q < 8; ++q) g2[q] = (i == q && l31 == (idx & 31)) ? t : g2[q];
        }
    }
    for (int i = 0; i < 8; ++i) asm volatile("" : "+v"(g2[i]));
    {
        const int pb = (((w & 3) * 8 + (tb & 7)) << 3) | ((tb >> 3) * 2 + (w >> 2));
        for (unsigned spins = 0; spins < SPIN_CAP; ++spins) { if (__all(ld8_sc1(ws + WS_FLAG4 + pb * 128 + (lane & 7) * 8) == seed)) break; __builtin_amdgcn_s_sleep(1); }
        const char* src = ws + WS_O + (size_t)(tb * 8 + w) * 16384;
        for (int i = 0; i < 4; ++i) dma4_sc1(src + i * 4096, vl, pv + i * 4096);
    }
    BAR_ALL();
    STAMP(6);
    f32x16 ao, at; for (int i = 0; i < 16; ++i) { ao[i] = 0.f; at[i] = 0.f; }
    {
        const unsigned oa = lds0 + kh * 4 * PRIV + vl + kh * 1024, ob = oa ^ 1024;
        u32x4 fo[2][4], ft[2][4];
#define ORD(p, jj) do { for (int i = 0; i < 4; ++i) { const int j_ = (jj) * 4 + i; fo[p][i] = lds16(oa + (j_ >> 3) * PRIV + (j_ & 7) * 2048); ft[p][i] = lds16(ob + (j_ >> 3) * PRIV + (j_ & 7) * 2048); } } while (0)
        ORD(0, 0);
#pragma unroll
        for (int jj = 0; jj < 8; ++jj) {
            if (jj < 7) ORD((jj + 1) & 1, jj + 1);
            for (int i = 0; i < 4; ++i) { at = mfma32(wf[jj * 4 + i], ft[jj & 1][i], at); ao = mfma32(wf[jj * 4 + i], fo[jj & 1][i], ao); }
            if (jj < 7) for (int i = 0; i < 8; ++i) { __builtin_amdgcn_sched_group_barrier(0x008, 1, 0); __builtin_amdgcn_sched_group_barrier(0x100, 1, 0); }
            __builtin_amdgcn_sched_barrier(0);
        }
        for (int q = 0; q < 4; ++q) *(LAS f32x4*)(pv + 16384 + q * 1024 + vl) = (f32x4){at[4 * q], at[4 * q + 1], at[4 * q + 2], at[4 * q + 3]};
        BAR_LDS();
        const unsigned pp = lds0 + (w ^ 4) * PRIV + 16384 + vl;
        char* op = out + (size_t)(tb * 64 + kh * 32 + l31) * 2048 + hg * 256 + cbk * 64 + h2 * 8;
        for (int q = 0; q < 4; ++q) {
            const f32x4 t = *(LAS f32x4*)(pp + q * 1024); u32x2 o;
            for (int i = 0; i < 2; ++i) {
                const unsigned s2 = pk2(ao[4 * q + 2 * i] + t[2 * i], ao[4 * q + 2 * i + 1] + t[2 * i + 1]), gg = g2[2 * q + i];
                const unsigned p2 = pk2(bf_lo(s2) * bf_lo(gg), bf_hi(s2) * bf_hi(gg));
                o[i] = pk2(bf_lo(xr[q][i]) + bf_lo(p2), bf_hi(xr[q][i]) + bf_hi(p2));
            }
            *(u32x2*)(op + q * 16) = o;
        }
    }
    }
    STAMP(7);
}

extern "C" void kernel_launch(void* const* d_in, const int* in_sizes, int n_in, void* d_out, int out_size, void* d_ws, size_t ws_size, hipStream_t stream) {
    (void)hipFuncSetAttribute((const void*)fused_block, hipFuncAttributeMaxDynamicSharedMemorySize, LDS_BYTES);
    fused_block<<<256, 512, LDS_BYTES, stream>>>((const char*)d_in[0], (const char*)d_in[1], (const char*)d_in[2], (const char*)d_in[3], (const char*)d_in[4],
                                                 (const char*)d_in[6], (const char*)d_in[7], (const char*)d_in[8], (const char*)d_in[9], (char*)d_out, (char*)d_ws);
}
```

```cpp
#include <hip/hip_runtime.h>
#include <hip/hip_bf16.h>
#include <stdint.h>
#include <type_traits>

typedef unsigned long long u64;
typedef float f32x16 __attribute__((ext_vector_type(16)));
typedef float f32x4 __attribute__((ext_vector_type(4)));
typedef float f32x2 __attribute__((ext_vector_type(2)));
typedef unsigned u32x2 __attribute__((ext_vector_type(2)));
typedef unsigned u32x4 __attribute__((ext_vector_type(4)));
typedef unsigned u32x8 __attribute__((ext_vector_type(8)));
typedef unsigned u32x16 __attribute__((ext_vector_type(16)));
typedef __bf16 bf16x8 __attribute__((ext_vector_type(8)));
typedef __bf16 bf16x2 __attribute__((ext_vector_type(2)));
typedef short s16x4 __attribute__((ext_vector_type(4)));
#define LAS __attribute__((address_space(3)))
#define GAS __attribute__((address_space(1)))
#define DI __device__ __forceinline__
#define LD(T, a) (*(LAS T*)(unsigned)(a))

constexpr unsigned WS_EPOCH = 0, WS_FLAG1 = 4096, WS_STATS = 8192, WS_FLAG4 = 65536, WS_KV = 131072, WS_YG = 393216, WS_GU = 622592, WS_O = 1u << 20;
constexpr unsigned WS_DBG = 6u << 20;
constexpr unsigned SLOT = 24576, L_X16 = 49152, L_WR = 81920, L_SP = 86016, L_WKV = 98304, L_QX = 0, L_PRO = 16384, L_MISC = 155648, LDS_BYTES = 163840;
constexpr unsigned M_CTL = L_MISC, M_ST64 = L_MISC + 256, M_STX = L_MISC + 1280, M_CS = L_MISC + 1536, M_XSEL = L_MISC + 2560, M_H = L_MISC + 3584, M_GU = L_MISC + 4096;
constexpr unsigned KA = 122880, KB = 32768, R2 = 65536, R3 = 0, R4 = 98304, M_GR = 114688, PRIV = 20480;
constexpr unsigned SPIN_CAP = 1u << 20;
constexpr int CODE_SPAN = 36864;

DI float bf_lo(unsigned v) { return __uint_as_float(v << 16); }
DI float bf_hi(unsigned v) { return __uint_as_float(v & 0xffff0000u); }
DI unsigned pk2(float lo, float hi) { f32x2 v = {lo, hi}; return __builtin_bit_cast(unsigned, __builtin_convertvector(v, bf16x2)); }
DI float rb(float f) { return __uint_as_float((unsigned)__builtin_bit_cast(unsigned short, (__bf16)f) << 16); }
DI float xsum32(float v) { auto r = __builtin_amdgcn_permlane32_swap(__float_as_uint(v), __float_as_uint(v), false, false); return __uint_as_float(r[0]) + __uint_as_float(r[1]); }
DI float xmax32(float v) { auto r = __builtin_amdgcn_permlane32_swap(__float_as_uint(v), __float_as_uint(v), false, false); return fmaxf(__uint_as_float(r[0]), __uint_as_float(r[1])); }
DI f32x16 mfma32(u32x4 a, u32x4 b, f32x16 c) { return __builtin_amdgcn_mfma_f32_32x32x16_bf16(__builtin_bit_cast(bf16x8, a), __builtin_bit_cast(bf16x8, b), c, 0, 0, 0); }
DI f32x4 mfma16(u32x4 a, u32x4 b, f32x4 c) { return __builtin_amdgcn_mfma_f32_16x16x32_bf16(__builtin_bit_cast(bf16x8, a), __builtin_bit_cast(bf16x8, b), c, 0, 0, 0); }
DI u32x2 trd(unsigned a) { return __builtin_bit_cast(u32x2, __builtin_amdgcn_ds_read_tr16_b64_v4i16((LAS s16x4*)a)); }
DI u32x4 trfrag(unsigned a, unsigned step) { u32x2 lo = trd(a), hi = trd(a + step); return (u32x4){lo[0], lo[1], hi[0], hi[1]}; }
DI u32x4 lo4(u32x8 v) { return __builtin_shufflevector(v, v, 0, 1, 2, 3); }
DI u32x4 hi4(u32x8 v) { return __builtin_shufflevector(v, v, 4, 5, 6, 7); }
DI float silu(float v) { return v * __builtin_amdgcn_rcpf(1.f + __builtin_amdgcn_exp2f(-1.44269504089f * v)); }
DI unsigned ln2(unsigned v, float m, float rs) { const unsigned d = pk2(bf_lo(v) - m, bf_hi(v) - m); return pk2(bf_lo(d) * rs, bf_hi(d) * rs); }
DI u64 ld8_sc1(const void* p) { return __hip_atomic_load((GAS const u64*)p, __ATOMIC_RELAXED, __HIP_MEMORY_SCOPE_AGENT); }
DI void st8_sc1(void* p, u64 v) { __hip_atomic_store((GAS u64*)p, v, __ATOMIC_RELAXED, __HIP_MEMORY_SCOPE_AGENT); }
DI int fresh_lane() { int l; asm volatile("v_mbcnt_lo_u32_b32 %0, -1, 0\n\tv_mbcnt_hi_u32_b32 %0, -1, %0" : "=v"(l)); return l; }
#define RELANE() const int lane = fresh_lane(), l15 = lane & 15, g4 = lane >> 4, l31 = lane & 31, h2 = lane >> 5; (void)l15; (void)g4; (void)l31; (void)h2
DI u64 mix(unsigned lo, unsigned hi) { u64 z = (((u64)hi << 32) | lo) + 0x9E3779B97F4A7C15ull; z = (z ^ (z >> 30)) * 0xBF58476D1CE4E5B9ull; z = (z ^ (z >> 27)) * 0x94D049BB133111EBull; return z ^ (z >> 31); }
DI unsigned rowmax16(unsigned k) { k = max(k, (unsigned)__builtin_amdgcn_update_dpp(0, (int)k, 0x128, 0xf, 0xf, false)); k = max(k, (unsigned)__builtin_amdgcn_update_dpp(0, (int)k, 0x124, 0xf, 0xf, false));
    k = max(k, (unsigned)__builtin_amdgcn_update_dpp(0, (int)k, 0x122, 0xf, 0xf, false)); return max(k, (unsigned)__builtin_amdgcn_update_dpp(0, (int)k, 0x121, 0xf, 0xf, false)); }
DI void dma1(const void* sbase, unsigned voff, unsigned m0v) { asm volatile("s_mov_b32 m0, %2\n\ts_nop 0\n\tglobal_load_lds_dwordx4 %0, %1" :: "v"(voff), "s"(sbase), "s"(m0v) : "memory"); }
DI void dma1_sc1(const void* sbase, unsigned voff, unsigned m0v) { asm volatile("s_mov_b32 m0, %2\n\ts_nop 0\n\tglobal_load_lds_dwordx4 %0, %1 sc1" :: "v"(voff), "s"(sbase), "s"(m0v) : "memory"); }
DI void dma4(const void* sbase, unsigned voff, unsigned m0v) {
    asm volatile("s_mov_b32 m0, %2\n\ts_nop 0\n\tglobal_load_lds_dwordx4 %0, %1\n\tglobal_load_lds_dwordx4 %0, %1 offset:1024\n\tglobal_load_lds_dwordx4 %0, %1 offset:2048\n\tglobal_load_lds_dwordx4 %0, %1 offset:3072"
                 :: "v"(voff), "s"(sbase), "s"(m0v) : "memory"); }
DI void dma4_sc1(const void* sbase, unsigned voff, unsigned m0v) {
    asm volatile("s_mov_b32 m0, %2\n\ts_nop 0\n\tglobal_load_lds_dwordx4 %0, %1 sc1\n\tglobal_load_lds_dwordx4 %0, %1 offset:1024 sc1\n\tglobal_load_lds_dwordx4 %0, %1 offset:2048 sc1\n\tglobal_load_lds_dwordx4 %0, %1 offset:3072 sc1"
                 :: "v"(voff), "s"(sbase), "s"(m0v) : "memory"); }
#define BAR_ALL() asm volatile("s_waitcnt vmcnt(0) lgkmcnt(0)\n\ts_barrier" ::: "memory")
#define BAR_LDS() asm volatile("s_waitcnt lgkmcnt(0)\n\ts_barrier" ::: "memory")
#define BAR_VM(n) asm volatile("s_waitcnt vmcnt(" #n ") lgkmcnt(0)\n\ts_barrier" ::: "memory")
#define WAIT_VM(n) asm volatile("s_waitcnt vmcnt(" #n ")" ::: "memory")
#define PIN(v) asm volatile("" : "+v"(v))
#define SBAR() __builtin_amdgcn_sched_barrier(0)
#define SGB(m, n) __builtin_amdgcn_sched_group_barrier(m, n, 0)
#define TS(i) do { } while (0)

#define EX(A,i) "v_exp_f32 v[" #A "+" #i "], v[" #A "+" #i "]\n"
#define E4(A,a,b,c,d) EX(A,a) EX(A,b) EX(A,c) EX(A,d)
#define CV(p,A,i) "v_cvt_pk_bf16_f32 v[104+" #p "], v[" #A "+" #i "], v[" #A "+" #i "+1]\n"
#define RDX(dst, o, k) "ds_read_b128 " dst ", v22 offset:" o "+" #k "\n"
#define M32(x) "v_mfma_f32_32x32x16_bf16 " x "\n"
#define M16(x) "v_mfma_f32_16x16x32_bf16 " x "\n"
#define TILE(A, B, o, X1, X2, Y) \
    M32("v[" #B ":" #B "+15], v[112:115], v[96:99], v[64:79]") RDX("v[112:115]", o, 0) X1 E4(A,0,1,2,3) \
    M32("v[80:95], v[124:127], v[108:111], v[80:95]") RDX("v[124:127]", o, 3072) E4(A,4,5,6,7) \
    "v_smfmac_f32_16x16x64_bf16 v[24:27], v[28:31], v[104:111], v23\n" "s_waitcnt lgkmcnt(2)\n" CV(0,A,0) CV(1,A,2) CV(2,A,4) CV(3,A,6) \
    M32("v[" #B ":" #B "+15], v[116:119], v[100:103], v[" #B ":" #B "+15]") RDX("v[116:119]", o, 1024) X2 E4(A,8,9,10,11) \
    M32("v[80:95], v[120:123], v[104:107], v[80:95]") RDX("v[120:123]", o, 2048) E4(A,12,13,14,15) Y \
    "s_waitcnt lgkmcnt(2)\n" CV(4,A,8) CV(5,A,10) CV(6,A,12) CV(7,A,14)
#define NO ""
#define WOA(k) "s_add_u32 m0, %[ma], " #k "\n s_add_u32 s70, s70, 0x8000\n s_addc_u32 s71, s71, 0\n global_load_lds_dwordx4 v20, s[70:71]\n"
#define WOB(k) "s_add_u32 m0, %[mb], " #k "\n s_add_u32 s70, s70, 0x8000\n s_addc_u32 s71, s71, 0\n global_load_lds_dwordx4 v20, s[70:71]\n"
#define MOE(k, st) "s_add_u32 m0, %[mm], " #k "\n s_add_u32 s72, s72, " #st "\n s_addc_u32 s73, s73, 0\n global_load_lds_dwordx4 v19, s[72:73]\n"
#define KV0 "s_mov_b32 m0, %[mk]\n s_nop 0\n global_load_lds_dwordx4 v21, %[sk] sc1\n"
#define KVN(k) "global_load_lds_dwordx4 v21, %[sk] offset:" #k " sc1\n"
#define GRD "s_mov_b32 m0, %[mm]\n s_nop 0\n global_load_lds_dwordx4 v21, s[72:73] sc1\n"
#define TR2(d, a, k, st) "ds_read_b64_tr_b16 v[" #d ":" #d "+1], " a " offset:" #k "\n ds_read_b64_tr_b16 v[" #d "+2:" #d "+3], " a " offset:" #k "+" #st "\n"
#define TRA(j, k) TR2(128+4*j, "v18", k, 512)
#define TRB(j, k) TR2(128+4*j, "v17", k, 512)
#define B64(d, a, k) "ds_read_b64 v[" #d ":" #d "+1], " a " offset:" #k "\n"
#define VM(n) "s_waitcnt vmcnt(" #n ")\n"
#define PRIOV 0
#define BODY_TXT(T0, T1, T2, T3, T4, T5, T6, T7) "s_nop 1\n s_setprio 3\n" T0 T1 T2 T3 "s_setprio 2\n" T4 T5 "s_setprio 1\n" T6 "s_setprio 0\n" T7 "s_waitcnt lgkmcnt(0)\n s_nop 7\n"
#define PH ""
#define T0(a,b,c) TILE(32,48,"0",a,b,c)
#define T1(a,b,c) TILE(48,32,"4096",a,b,c)
#define T2(a,b,c) TILE(32,48,"8192",a,b,c)
#define T3(a,b,c) TILE(48,32,"12288",a,b,c)
#define T4(a,b,c) TILE(32,48,"16384",a,b,c)
#define T5(a,b,c) TILE(48,32,"20480",a,b,c)
#define T6(a,b,c) TILE(32,48,"24576",a,b,c)
#define T7(a,b,c) TILE(48,32,"28672",a,PH b,c)
#define BODY_OUT "+{v[32:47]}"(SA), "=&{v[48:63]}"(SB), "+{v[80:95]}"(O), "+{v[24:31]}"(LS), "+{v[104:119]}"(PK), "+{v[120:127]}"(V), "+{s[70:71]}"(swo), "+{s[72:73]}"(smo)
#define BODY_IN "{v[64:79]}"(NEG), "{v[96:103]}"(Q), "{v23}"(IDX), "{v22}"(ad), "{v21}"(vl), "{v20}"(vw), "{v19}"(vm), "{v18}"(ta), "{v17}"(tb_), \
      [ma] "s"(ma), [mb] "s"(mb), [mm] "s"(mm), [mk] "s"(mk), [sk] "s"(sk)
#define C8(a,b,c,d,e,f,g,h) "v" #a, "v" #b, "v" #c, "v" #d, "v" #e, "v" #f, "v" #g, "v" #h

__global__ __launch_bounds__(512, 2) void fused_block(const char* __restrict__ x, const char* __restrict__ wq, const char* __restrict__ wk, const char* __restrict__ wv,
                                                      const char* __restrict__ wo, const char* __restrict__ wr, const char* __restrict__ wg, const char* __restrict__ wu,
                                                      const char* __restrict__ wd, char* __restrict__ out, char* __restrict__ ws) {
    const int tid = threadIdx.x, lane = tid & 63, w = __builtin_amdgcn_readfirstlane(tid >> 6);
    const int b = blockIdx.x, xcd = b & 7, r = b >> 3, tb = (xcd >> 1) * 8 + (r & 7), hg = (xcd & 1) * 4 + (r >> 3);
    const int isV = xcd & 1, sub = hg & 3, T0 = tb * 64 + sub * 16;
    const int l15 = lane & 15, g4 = lane >> 4, l31 = lane & 31, h2 = lane >> 5;
    const int hd = w & 3, tbk = w >> 2;
    const unsigned vl = lane * 16;
    TS(0);
    {
        u64 pc; asm volatile("s_getpc_b64 %0" : "=s"(pc));
        asm volatile("s_mov_b32 m0, %2\n\ts_mov_b64 exec, 3\n\tglobal_load_lds_dword %0, %1\n\ts_mov_b64 exec, -1"
                     :: "v"(lane * 128), "s"((pc & ~127ull) + min((r * 8 + w) * 256, CODE_SPAN - 256)), "s"(w < 4 ? L_X16 + w * 8192 : L_WKV + (w - 4) * 16384) : "memory");
    }
    const int c0 = r & 7, rm = min(r, 27), mi = xcd * 28 + rm; const char* kv = ws + WS_KV;
    f32x16 qa; float csum;
    u32x8 LS, PZ; f32x16 O; unsigned IDX, vw, vm0, ta, tb_;
    const unsigned ma = R4 + w * 2048, mb = R3 + w * 4096;
#define SETUP() do { const unsigned one_ = 0x3F803F80u; LS = (u32x8){0u, 0u, 0u, 0u, l15 < 2 ? one_ : 0u, l15 < 2 ? one_ : 0u, (l15 & 14) == 2 ? one_ : 0u, (l15 & 14) == 2 ? one_ : 0u}; \
        IDX = (lane & 1) ? 0xEEEEEEEEu : 0x44444444u; vw = (lane >> 2) * 2048 + (lane & 3) * 16; vm0 = (lane >> 3) * 7168 + (((lane & 7) ^ (g4 << 1)) << 4); \
        ta = ma + (4 * h2 + (l15 >> 2)) * 64 + (g4 & 1) * 32 + (lane & 3) * 8; tb_ = ta + (mb - ma); for (int i = 0; i < 16; ++i) O[i] = 0.f; PZ = (u32x8){0u, 0u, 0u, 0u, 0u, 0u, 0u, 0u}; PIN(O); PIN(PZ); PIN(LS); PIN(IDX); PIN(vw); PIN(vm0); PIN(ta); PIN(tb_); } while (0)
    if (__builtin_expect(w < 4, 1)) {
        __builtin_amdgcn_s_setprio(3);
        u32x2 ev; u32x4 xq[8], aw[8], sel, ones; unsigned wa, wa2, spa, kvo, wb[3], xb_[3][4];
        {
            const char* xs = x + (size_t)T0 * 2048 + w * 512;
            for (int i = 0; i < 8; ++i) dma1(xs, (2 * i + h2) * 2048 + ((l31 ^ (2 * i + h2)) << 4), L_X16 + w * 8192 + i * 1024);
            dma1(wr + w * 512, l31 * 16, L_WR + w * 1024);
            asm volatile("s_mov_b64 exec, 1\n\tglobal_load_dwordx2 %0, %1, %2 sc1\n\ts_mov_b64 exec, -1" : "=v"(ev) : "v"(0), "s"(ws));
            SETUP();
            wa = L_WKV + w * 16384 + (8 * g4 + (l15 >> 2)) * 64 + (g4 & 1) * 32 + (lane & 3) * 8; wa2 = wa ^ 32; spa = L_SP + l15 * 16;
            const int kt = T0 >> 5, sh = sub & 1;
            kvo = isV ? ((kt - 1 + sh) & 63) * 4096 + 2048 + sh * 1024 + ((g4 & 1) * 32 + w * 16 + l15) * 16 + (g4 >> 1) * 8
                      : ((kt - 2) & 63) * 4096 + w * 1024 + ((g4 & 1) * 32 + sh * 16 + l15) * 16 + (g4 >> 1) * 8;
            const unsigned sv = (((l15 >> 2) & 1) == (g4 & 1)) ? 0x3F803F80u : 0u, xq_a = l31 * 128 + ((h2 ^ ((l31 >> 1) & 7)) << 4);
            const unsigned wq_a = 8192 + (8 * h2 + (l15 >> 2)) * 256 + ((hd ^ ((lane >> 2) & 3)) << 6) + (g4 & 1) * 32 + (lane & 3) * 8;
            sel = (u32x4){sv, sv, sv, sv}; ones = (u32x4){0x3F803F80u, 0x3F803F80u, 0x3F803F80u, 0x3F803F80u};
            for (int i = 0; i < 3; ++i) { wb[i] = wq_a + i * 2 * SLOT; PIN(wb[i]); for (int ks = 0; ks < 4; ++ks) { xb_[i][ks] = (xq_a ^ (ks * 32)) + i * 2 * SLOT; PIN(xb_[i][ks]); } }
            const unsigned oth = l15 == 1 ? 0x3F803F80u : 0u;
            for (int s = 0; s < 8; ++s) { aw[s] = (u32x4){oth, oth, oth, oth}; PIN(aw[s]); }
            PIN(kvo); PIN(wa); PIN(wa2); PIN(spa); PIN(ones); PIN(sel);
            asm volatile("s_waitcnt vmcnt(0)" : "+v"(ev) :: "memory");
            const unsigned xa = L_X16 + w * 8192 + l15 * 512 + ((g4 ^ l15) << 4);
            for (int s = 0; s < 8; ++s) xq[s] = LD(u32x4, xa ^ (s * 64));
            if (l15 == 0) for (int s = 0; s < 8; ++s) aw[s] = LD(u32x4, L_WR + w * 1024 + s * 64 + g4 * 16);
        }
        TS(8);
        const f32x4 z4 = {0, 0, 0, 0};
        f32x4 s1 = z4, gr = z4, sw = z4;
        for (int s = 0; s < 8; ++s) { gr = mfma16(xq[s], xq[s], gr); s1 = mfma16(aw[s], xq[s], s1); sw = mfma16(aw[s], ones, sw); }
        if (lane < 16) { LD(float, L_SP + w * 256 + vl) = s1[1]; LD(f32x2, L_SP + w * 256 + vl + 8) = (f32x2){s1[0], sw[0]}; }
        if (g4 == (l15 >> 2)) LD(float, L_SP + w * 256 + l15 * 16 + 4) = (l15 & 2) ? ((l15 & 1) ? gr[3] : gr[2]) : ((l15 & 1) ? gr[1] : gr[0]);
        TS(9);
        BAR_LDS();
        TS(33);
        f32x4 t = z4, r4; float mrow, rsrow;
        auto kvpart = [&](auto V_) {
            constexpr bool V = decltype(V_)::value;
            u32x4 w0[8], w1[8]; f32x4 a0 = z4, a1 = z4, c0_ = z4, c1_ = z4, m4; float var;
            for (int s = 0; s < 2; ++s) { w0[s] = trfrag(wa + s * 2048, 256); w1[s] = trfrag(wa2 + s * 2048, 256); }
            f32x4 p[4]; for (int i = 0; i < 4; ++i) p[i] = LD(f32x4, spa + i * 256);
            SBAR();
#pragma unroll
            for (int s = 0; s < 8; ++s) {
                if (s < 6) { w0[s + 2] = trfrag(wa + (s + 2) * 2048, 256); w1[s + 2] = trfrag(wa2 + (s + 2) * 2048, 256); }
                if (V) { a0 = mfma16(xq[s], w0[s], a0); c0_ = mfma16(ones, w0[s], c0_); a1 = mfma16(xq[s], w1[s], a1); c1_ = mfma16(ones, w1[s], c1_); }
                else { a0 = mfma16(w0[s], xq[s], a0); c0_ = mfma16(w0[s], ones, c0_); a1 = mfma16(w1[s], xq[s], a1); c1_ = mfma16(w1[s], ones, c1_); }
                if (s < 6) for (int i = 0; i < 4; ++i) { SGB(0x008, 1); SGB(0x100, 1); }
                SBAR();
                if (s == 0) { t = (p[0] + p[1]) + (p[2] + p[3]); PIN(t); }
                if (s == 1) { const float mean = t[0] * (1.f / 1024.f); var = rb(fmaxf(t[1] * (1.f / 1024.f) - mean * mean, 0.f)); mrow = rb(mean); PIN(var); PIN(mrow); }
                if (s == 2) { var = __builtin_amdgcn_sqrtf(rb(var + 1e-5f)); PIN(var); }
                if (s == 3) { rsrow = __builtin_amdgcn_rcpf(rb(var)); PIN(rsrow); }
                if (s == 5) for (int i = 0; i < 4; ++i) {
                    m4[i] = V ? __uint_as_float(__builtin_amdgcn_ds_bpermute((4 * g4 + i) * 4, (int)__float_as_uint(mrow))) : mrow;
                    r4[i] = V ? __uint_as_float(__builtin_amdgcn_ds_bpermute((4 * g4 + i) * 4, (int)__float_as_uint(rsrow))) : rsrow; }
                SBAR();
            }
            LD(f32x4, L_WKV + w * 16384 + vl) = a0 - m4 * c0_; LD(f32x4, L_WKV + w * 16384 + 1024 + vl) = a1 - m4 * c1_;
        };
        if (isV) kvpart(std::true_type{}); else kvpart(std::false_type{});
        if (tid == 192) LD(u32x2, M_CTL) = ev;
        TS(35);
        BAR_LDS();
        TS(1);
        unsigned key = 0;
        if (w < 2) {
            f32x4 p[4]; for (int i = 0; i < 4; ++i) p[i] = LD(f32x4, L_WKV + i * 16384 + w * 1024 + vl);
            const f32x4 a = ((p[0] + p[1]) + (p[2] + p[3])) * r4;
            st8_sc1(ws + WS_KV + kvo, ((u64)pk2(a[2], a[3]) << 32) | pk2(a[0], a[1]));
        } else if (w == 2 && !isV) {
            const unsigned u = __float_as_uint(rb((t[2] - mrow * t[3]) * rsrow)) >> 16;
            key = rowmax16(((((u & 0x8000u) ? ~u : (u | 0x8000u)) & 0xffffu) << 11) | (2047 - (T0 + l15)));
            if (lane < 16) st8_sc1(ws + WS_STATS + (T0 + l15) * 8, ((u64)__float_as_uint(rsrow) << 32) | __float_as_uint(mrow));
        }
        f32x16 qb; f32x4 cs = z4; qa = O; qb = O;
        u32x4 af[2][4], b0[2][4], b1[2][4];
#define QRD(p, s) do { const unsigned sl_ = ((s) % 6 & 1) * SLOT; const int bi_ = (s) % 6 >> 1; for (int ks = 0; ks < 4; ++ks) { \
            af[p][ks] = trfrag(wb[bi_] + sl_ + ks * 4096, 1024); b1[p][ks] = LD(u32x4, xb_[bi_][ks] + sl_ + 4096); b0[p][ks] = LD(u32x4, xb_[bi_][ks] + sl_); } } while (0)
#pragma unroll
        for (int s = 0; s < 16; ++s) {
            if (!(s & 1)) TS(36 + s);
            if (s == 4) WAIT_VM(0);
            if (!(s & 1) && s <= 12) { BAR_LDS(); TS(37 + s); }
            if (s == 0) { QRD(0, 0); SBAR(); }
            if (s == 4 && tid == 128) { const u32x2 e = LD(u32x2, M_CTL); st8_sc1(ws + WS_FLAG1 + b * 8, (mix(e[0], e[1]) & ~0xFFFFFFFull) | key); }
            if (s < 15) {
                QRD((s + 1) & 1, s + 1);
                for (int ks = 0; ks < 4; ++ks) { qb = mfma32(af[s & 1][ks], b1[s & 1][ks], qb); qa = mfma32(af[s & 1][ks], b0[s & 1][ks], qa); cs = mfma16(sel, af[s & 1][ks], cs); }
                for (int i = 0; i < 8; ++i) { SGB(0x008, 1); SGB(0x100, 2); }
            } else {
                for (int ks = 0; ks < 4; ++ks) { qb = mfma32(af[1][ks], b1[1][ks], qb); cs = mfma16(sel, af[1][ks], cs); }
                SBAR();
                for (int ks = 0; ks < 4; ++ks) { qa = mfma32(af[1][ks], b0[1][ks], qa); LD(f32x4, L_QX + w * 4096 + ks * 1024 + vl) = (f32x4){qb[4 * ks], qb[4 * ks + 1], qb[4 * ks + 2], qb[4 * ks + 3]}; }
                csum = cs[0]; LD(float, M_CS + w * 256 + lane * 4) = csum;
            }
            SBAR();
        }
        TS(2);
        BAR_LDS();
    } else {
        const int lw = w - 4, koff = (2 * hg + 4 * (tb & 3) + xcd) & 15;
        { const char* wkv = (isV ? wv : wk) + lw * 16384;
          for (int i = 0; i < 4; ++i) dma4(wkv + i * 4096, vl ^ (h2 << 5), L_WKV + lw * 16384 + i * 4096); }
        unsigned vx[2], vq[4];
        for (int i = 0; i < 2; ++i) { const int row = lw * 16 + i * 8 + (lane >> 3); vx[i] = row * 2048 + (((lane & 7) ^ ((row >> 1) & 7)) << 4); }
        for (int i = 0; i < 4; ++i) vq[i] = (lw * 16 + i * 4 + g4) * 2048 + ((((lane >> 2) & 3) ^ g4) << 6) + ((lane & 3) << 4);
        const char* xb = x + (size_t)tb * 64 * 2048; const char* qb_ = wq + hg * 256;
#define RING_ISSUE(s) do { const int kk_ = ((s) + koff) & 15; \
        asm volatile("s_mov_b32 m0, %8\n\ts_nop 0\n\tglobal_load_lds_dwordx4 %0, %6\n\ts_add_u32 m0, m0, 1024\n\ts_nop 0\n\tglobal_load_lds_dwordx4 %1, %6\n\t" \
                     "s_mov_b32 m0, %9\n\ts_nop 0\n\tglobal_load_lds_dwordx4 %2, %7\n\ts_add_u32 m0, m0, 1024\n\ts_nop 0\n\tglobal_load_lds_dwordx4 %3, %7\n\t" \
                     "s_add_u32 m0, m0, 1024\n\ts_nop 0\n\tglobal_load_lds_dwordx4 %4, %7\n\ts_add_u32 m0, m0, 1024\n\ts_nop 0\n\tglobal_load_lds_dwordx4 %5, %7" \
                     :: "v"(vx[0]), "v"(vx[1]), "v"(vq[0]), "v"(vq[1]), "v"(vq[2]), "v"(vq[3]), "s"(xb + kk_ * 128), "s"(qb_ + (size_t)kk_ * 131072), \
                        "s"(((s) % 6) * SLOT + lw * 2048), "s"(((s) % 6) * SLOT + 8192 + lw * 4096) : "memory", "scc"); } while (0)
        TS(32); RING_ISSUE(0); TS(8);
        BAR_VM(6); TS(33); RING_ISSUE(1); RING_ISSUE(2); SETUP(); TS(35);
        BAR_LDS(); TS(1); RING_ISSUE(3); TS(36);
        BAR_VM(6); TS(37); RING_ISSUE(4); RING_ISSUE(5); TS(38);
        BAR_VM(6); TS(39); RING_ISSUE(6); RING_ISSUE(7); RING_ISSUE(8); TS(40);
        BAR_VM(12); TS(41); RING_ISSUE(9); RING_ISSUE(10); TS(42);
        BAR_VM(12); TS(43); RING_ISSUE(11); RING_ISSUE(12); TS(44);
        BAR_VM(12); TS(45);
        if (lw < 2) dma1_sc1(ws + WS_FLAG1 + lw * 1024, vl, M_GU + lw * 1024);
        RING_ISSUE(13); RING_ISSUE(14); TS(46);
        BAR_VM(12); TS(47); RING_ISSUE(15);
        const u32x2 e = LD(u32x2, M_CTL); const u64 tag36 = mix(e[0], e[1]) & ~0xFFFFFFFull;
        u64 f[4]; { const u32x4 f0 = LD(u32x4, M_GU + vl), f1 = LD(u32x4, M_GU + 1024 + vl); f[0] = ((u64)f0[1] << 32) | f0[0]; f[1] = ((u64)f0[3] << 32) | f0[2]; f[2] = ((u64)f1[1] << 32) | f1[0]; f[3] = ((u64)f1[3] << 32) | f1[2]; }
        TS(48);
        BAR_VM(0); TS(49);
#pragma unroll 1
        for (unsigned spins = 0; spins < SPIN_CAP && !__all((((f[0] ^ tag36) | (f[1] ^ tag36) | (f[2] ^ tag36) | (f[3] ^ tag36)) >> 28) == 0); ++spins) {
            if (spins) __builtin_amdgcn_s_sleep(2);
            for (int i = 0; i < 4; ++i) f[i] = ld8_sc1(ws + WS_FLAG1 + (i * 64 + lane) * 8);
        }
        TS(50);
        dma4_sc1(kv + c0 * 32768 + lw * 8192, vl, KA + lw * 8192); dma4_sc1(kv + c0 * 32768 + lw * 8192 + 4096, vl, KA + lw * 8192 + 4096);
        if (lw < 2) dma4_sc1(kv + ((c0 * 8 + 62 + lw) & 63) * 4096, vl, L_PRO + lw * 4096);
        else if (lw == 2) dma1_sc1(ws + WS_STATS + tb * 512, l31 * 16, M_ST64);
        else {
            unsigned key = rowmax16(max(max((unsigned)f[0] & 0xFFFFFFFu, (unsigned)f[1] & 0xFFFFFFFu), max((unsigned)f[2] & 0xFFFFFFFu, (unsigned)f[3] & 0xFFFFFFFu)));
            key = max(max((unsigned)__builtin_amdgcn_readlane(key, 0), (unsigned)__builtin_amdgcn_readlane(key, 16)), max((unsigned)__builtin_amdgcn_readlane(key, 32), (unsigned)__builtin_amdgcn_readlane(key, 48)));
            const int ix = 2047 - (key & 2047);
            dma1(x + (size_t)ix * 2048 + (mi & 3) * 512, l31 * 16, M_XSEL);
            asm volatile("s_mov_b32 m0, %2\n\ts_nop 0\n\tglobal_load_lds_dword %0, %1 sc1" :: "v"((lane & 1) * 4), "s"(ws + WS_STATS + ix * 8), "s"(M_STX) : "memory");
            if (lane == 0) { LD(unsigned, M_CTL + 8) = key; if (b == 0) st8_sc1(ws + WS_EPOCH, (((u64)e[1] << 32) | e[0]) + 1); }
        }
        TS(2);
        __builtin_amdgcn_s_setprio(3);
        BAR_ALL();
        for (int q = 0; q < 4; ++q) { const f32x4 t = LD(f32x4, L_QX + hd * 4096 + q * 1024 + vl); for (int i = 0; i < 4; ++i) qa[4 * q + i] = t[i]; }
        csum = LD(float, M_CS + hd * 256 + lane * 4);
    }
    TS(3);
    u32x8 Q, V; u32x16 PK; f32x16 SA, SB, NEG;
    const u32x4 ctl = LD(u32x4, M_CTL);
    {
        const f32x2 st = LD(f32x2, M_ST64 + (tbk * 32 + l31) * 8);
        const u32x4 k00 = LD(u32x4, L_PRO + vl), k01 = LD(u32x4, L_PRO + 1024 + vl), k10 = LD(u32x4, L_PRO + 4096 + vl), k11 = LD(u32x4, L_PRO + 5120 + vl), v00 = LD(u32x4, L_PRO + 6144 + vl);
        const float c1 = rb(csum), c2 = rb(csum - c1), c3 = csum - c1 - c2;
        const u32x4 ca = {h2 ? 0u : pk2(c1, c2), h2 ? 0u : pk2(c3, 0.f), 0u, 0u}, cb = {pk2(-st[0], -st[0]), pk2(-st[0], 0.f), 0u, 0u};
        qa = mfma32(ca, cb, qa);
        const float qs = st[1] * (0.17677669529663687f * 1.4426950408889634f);
        for (int i = 0; i < 8; ++i) Q[i] = pk2(qa[2 * i] * qs, qa[2 * i + 1] * qs);
        f32x16 s0 = mfma32(k00, lo4(Q), O); s0 = mfma32(k01, hi4(Q), s0);
        for (int i = 0; i < 4; ++i) { PK[i] = PZ[i]; PK[4 + i] = PZ[4 + i]; PK[8 + i] = k10[i]; PK[12 + i] = k11[i]; V[i] = v00[i]; V[4 + i] = k00[i]; }
        float m = fmaxf(fmaxf(fmaxf(fmaxf(s0[0], s0[1]), s0[2]), fmaxf(fmaxf(s0[3], s0[4]), s0[5])), fmaxf(fmaxf(fmaxf(s0[6], s0[7]), s0[8]), fmaxf(fmaxf(s0[9], s0[10]), s0[11])));
        m = xmax32(fmaxf(fmaxf(m, fmaxf(s0[12], s0[13])), fmaxf(s0[14], s0[15])));
        const float aone = h2 ? 0.f : 1.f;
        SA = __builtin_amdgcn_mfma_f32_32x32x2f32(aone, -m, s0, 0, 0, 0); NEG = __builtin_amdgcn_mfma_f32_32x32x2f32(aone, -m, O, 0, 0, 0);
        asm volatile("s_nop 7\n\ts_nop 7" : "+v"(NEG), "+v"(SA));
    }
    TS(4);
    const u64 seed = mix(__builtin_amdgcn_readfirstlane(ctl[0]), __builtin_amdgcn_readfirstlane(ctl[1]));
    const unsigned tag32 = (unsigned)seed ^ 0x5bd1e995u, gkey = __builtin_amdgcn_readfirstlane(ctl[2]);
    const int idx = 2047 - (gkey & 2047), cbk = hd, kh = tbk;
    const bool moe = r < 28;
    u64 swo = (u64)(wo + (size_t)kh * 512 * 2048 + hg * 256 + cbk * 64) - 0x8000, smo;
    u32x8 wf[16];
#define SEAM(c) const unsigned ad = (((c) & 1) ? KB : KA) + vl, mk = (((c) & 1) ? KA : KB) + w * 4096; const char* sk = kv + ((c0 + (c) + 1) & 7) * 32768 + w * 4096
#define MROW() (16 * (g4 >> 1) + 4 * (g4 & 1) + (l15 >> 2))
#define MSWZ() (2 * (g4 & 1) + (l15 >> 3))
    { SEAM(0); const unsigned vm = vm0, mm = R2 + w * 4096; smo = (u64)(wg + (size_t)((mi & 3) * 256 + w * 32) * 7168 + (mi >> 2) * 128); TS(10);
      asm volatile(BODY_TXT(T0(WOA(0), NO, NO), T1(WOA(1024), NO, NO), T2(KV0, KVN(1024), NO), T3(KVN(2048), KVN(3072), NO), T4(MOE(0, 0), MOE(1024, 57344), NO), T5(MOE(2048, 57344), MOE(3072, 57344), NO),
                            T6(NO, NO, NO), T7(VM(8) TRA(0, 0) TRA(1, 1024), NO, NO))
                   : BODY_OUT, "=&{v[128:135]}"(wf[0]) : BODY_IN : "memory", "scc"); TS(18); }
    { BAR_VM(4); SEAM(1); const unsigned vm = vm0, mm = mb; smo = (u64)(wu + (size_t)((mi & 3) * 256 + w * 32) * 7168 + (mi >> 2) * 128);
      TS(11);
      asm volatile(BODY_TXT(T0(WOA(0), NO, NO), T1(WOA(1024), NO, NO), T2(KV0, KVN(1024), NO), T3(KVN(2048), KVN(3072), NO), T4(MOE(0, 0), MOE(1024, 57344), NO), T5(MOE(2048, 57344), MOE(3072, 57344), NO),
                            T6(NO, NO, NO), T7(VM(8) TRA(2, 0) TRA(3, 1024), NO, NO))
                   : BODY_OUT, "=&{v[136:143]}"(wf[1]) : BODY_IN : "memory", "scc"); TS(19); }
    { if (w == 0) { RELANE(); const f32x2 sx = LD(f32x2, M_STX); const u32x2 v = LD(u32x2, M_XSEL + lane * 8); LD(u32x2, M_XSEL + lane * 8) = (u32x2){ln2(v[0], sx[0], sx[1]), ln2(v[1], sx[0], sx[1])}; }
      BAR_VM(4); SEAM(2); const unsigned vm = 0, mm = 0; TS(12);
      asm volatile(BODY_TXT(T0(WOA(0), NO, NO), T1(WOA(1024), NO, NO), T2(KV0, NO, NO), T3(KVN(1024), NO, NO), T4(KVN(2048), NO, NO), T5(KVN(3072), NO, NO), T6(NO, NO, NO), T7(VM(4) TRA(4, 0) TRA(5, 1024), NO, NO))
                   : BODY_OUT, "=&{v[144:151]}"(wf[2]) : BODY_IN : "memory", "scc"); TS(20); }
    { RELANE(); const unsigned vm = R2 + w * 4096 + MROW() * 128 + MSWZ() * 32 + (lane & 3) * 8, mm = 0;
      const unsigned xa = M_XSEL + w * 64 + (g4 >> 1) * 32 + (g4 & 1) * 8, gw = M_GU + w * 256 + lane * 4;
      BAR_VM(0); SEAM(3); TS(13);
#define GM(d, a) M16("v[" #d ":" #d "+3], v[176:179], v[" #a ":" #a "+3], 0")
      asm volatile(BODY_TXT(T0(WOA(0) "v_xor_b32 v244, 32, v19\n v_xor_b32 v245, 64, v19\n v_xor_b32 v246, 0x60, v19\n v_subrev_u32 v247, 0x10000, v19\n" B64(176, "v15", 0) B64(178, "v15", 16) TR2(180, "v19", 0, 1024),
                               WOA(1024) TR2(184, "v244", 0, 1024) TR2(188, "v245", 0, 1024) "v_subrev_u32 v244, 0x10000, v244\n v_subrev_u32 v245, 0x10000, v245\n", NO),
                            T1(TR2(192, "v246", 0, 1024) TR2(196, "v247", 0, 1024) "v_subrev_u32 v246, 0x10000, v246\n", TR2(200, "v244", 0, 1024) TR2(204, "v245", 0, 1024), GM(212, 180) GM(216, 184)),
                            T2(TR2(208, "v246", 0, 1024) KV0, KVN(1024), GM(220, 188) GM(224, 192)), T3(KVN(2048) KVN(3072), WOB(0), GM(228, 196) GM(232, 200)), T4(WOB(1024), WOB(2048), GM(236, 204) GM(240, 208)),
                            T5(WOB(3072), NO, NO),
                            T6("s_mov_b32 vcc_lo, 0xffff0000\n s_mov_b32 vcc_hi, 0xffff0000\n v_cndmask_b32 v212, v212, v216, vcc\n v_cndmask_b32 v220, v220, v224, vcc\n v_cndmask_b32 v228, v228, v232, vcc\n"
                               "v_cndmask_b32 v236, v236, v240, vcc\n s_mov_b32 vcc_lo, 0\n s_mov_b32 vcc_hi, -1\n v_cndmask_b32 v212, v212, v220, vcc\n v_cndmask_b32 v228, v228, v236, vcc\n",
                               "ds_write_b32 v16, v212\n ds_write_b32 v16, v228 offset:2048\n" VM(8) TRA(6, 0) TRA(7, 1024), NO),
                            T7(VM(0) TRB(8, 0) TRB(9, 1024), TRB(10, 2048) TRB(11, 3072), NO))
                   : BODY_OUT, "=&{v[152:159]}"(wf[3]), "=&{v[160:167]}"(wf[4]), "=&{v[168:175]}"(wf[5]) : BODY_IN, "{v15}"(xa), "{v16}"(gw)
                   : "memory", "scc", C8(176,177,178,179,180,181,182,183), C8(184,185,186,187,188,189,190,191), C8(192,193,194,195,196,197,198,199), C8(200,201,202,203,204,205,206,207),
                     C8(208,209,210,211,212,213,214,215), C8(216,217,218,219,220,221,222,223), C8(224,225,226,227,228,229,230,231), C8(232,233,234,235,236,237,238,239), C8(240,241,242,243,244,245,246,247), "vcc"); TS(21); }
    const unsigned ye = moe ? 0xFFFFu : 0u;
    { RELANE(); const unsigned vm = (lane >> 3) * 2048 + (((lane & 7) ^ (g4 << 1)) << 4), mm = R2 + w * 2048; smo = (u64)(wd + (size_t)(rm * 128 + w * 16) * 2048 + xcd * 256);
      const unsigned ga = M_GU + (w >> 2) * 2048 + ((w & 3) * 16 + l15) * 4, go = l15 * 64; const char* gs = ws + WS_GU + ((mi >> 2) * 64 + (w & 3) * 16) * 64 + (w >> 2) * 32 + (mi & 3) * 8;
      BAR_VM(0); SEAM(4); TS(14);
#define R2S(d, a, b) "ds_read2st64_b32 v[" #d ":" #d "+1], v15 offset0:" #a " offset1:" #b "\n"
      asm volatile(BODY_TXT(T0(WOA(0) R2S(224, 0, 1) R2S(226, 2, 3), WOA(1024) R2S(228, 4, 5) R2S(230, 6, 7), NO),
                            T1(WOB(0), WOB(1024) "v_add_f32 v224, v224, v225\n v_add_f32 v226, v226, v227\n v_add_f32 v228, v228, v229\n v_add_f32 v230, v230, v231\n v_add_f32 v224, v224, v226\n v_add_f32 v228, v228, v230\n"
                               "v_mov_b32 v225, %[tg]\n v_add_f32 v224, v224, v228\n", "s_mov_b32 exec_lo, %[ye]\n s_mov_b32 exec_hi, 0\n global_store_dwordx2 v16, v[224:225], %[gs] sc1\n s_mov_b64 exec, -1\n"),
                            T2(WOB(2048), WOB(3072), NO), T3(KV0, KVN(1024), NO), T4(KVN(2048), KVN(3072), NO), T5(MOE(0, 0), MOE(16384, 128), NO),
                            T6(MOE(1024, 16256), MOE(17408, 128) VM(8) TRA(12, 0) TRA(13, 1024), NO), T7(TRB(14, 0) TRB(15, 1024), TRB(16, 2048) TRB(17, 3072), NO))
                   : BODY_OUT, "=&{v[176:183]}"(wf[6]), "=&{v[184:191]}"(wf[7]), "=&{v[192:199]}"(wf[8]) : BODY_IN, "{v15}"(ga), "{v16}"(go), [ye] "s"(ye), [tg] "s"(tag32), [gs] "s"(gs)
                   : "memory", "scc", C8(224,225,226,227,228,229,230,231)); TS(22); }
    u64 bad;
    { RELANE(); const unsigned vm = 0, mm = M_GR + w * 1024, ga = M_GR + w * 1024 + lane * 16, hw = M_H + w * 32 + (lane >> 2) * 2; smo = (u64)(ws + WS_GU + (size_t)(rm * 128 + w * 16) * 64);
      BAR_VM(4); SEAM(5); TS(15);
      asm volatile(BODY_TXT(T0(WOA(0), WOA(1024), NO), T1(WOB(0), WOB(1024), NO), T2(WOB(2048), WOB(3072), GRD), T3(KV0, KVN(1024), NO), T4(KVN(2048), KVN(3072), NO), T5(NO, NO, NO),
                            T6(VM(4) "ds_read_b128 v[224:227], v15\n" TRA(18, 0) TRA(19, 1024), TRB(20, 0) TRB(21, 1024), NO),
                            T7("v_add_f32 v224, v224, v226\n v_cmp_ne_u32 vcc, %[tg], v225\n v_cmp_ne_u32 s[74:75], %[tg], v227\n v_add_f32_dpp v224, v224, v224 quad_perm:[1,0,3,2] row_mask:0xf bank_mask:0xf\n"
                               "v_cvt_pk_bf16_f32 v224, v224, v224\n v_lshlrev_b32 v224, 16, v224\n v_mul_f32 v226, 0xbfb8aa3b, v224\n v_exp_f32 v226, v226\n"
                               "v_mov_b32_dpp v225, v224 quad_perm:[2,3,0,1] row_mask:0xf bank_mask:0xf\n s_or_b64 s[74:75], vcc, s[74:75]\n" TRB(22, 2048),
                               "v_add_f32 v226, 1.0, v226\n v_rcp_f32 v226, v226\n" TRB(23, 3072),
                               "v_mul_f32 v226, v224, v226\n v_cvt_pk_bf16_f32 v226, v226, v226\n v_lshlrev_b32 v226, 16, v226\n v_mul_f32 v226, v226, v225\n v_cvt_pk_bf16_f32 v226, v226, v226\n"
                               "s_mov_b32 exec_lo, 0x11111111\n s_mov_b32 exec_hi, 0x11111111\n ds_write_b16 v16, v226\n s_mov_b64 exec, -1\n"))
                   : BODY_OUT, "=&{v[200:207]}"(wf[9]), "=&{v[208:215]}"(wf[10]), "=&{v[216:223]}"(wf[11]), "=&{s[74:75]}"(bad) : BODY_IN, "{v15}"(ga), "{v16}"(hw), [tg] "s"(tag32)
                   : "memory", "scc", "vcc", "v224", "v225", "v226", "v227"); TS(23); }
    { RELANE();
      if (__builtin_expect(moe && bad != 0, 0)) {
          u32x4 gq;
#pragma unroll 1
          for (unsigned spins = 0; spins < SPIN_CAP; ++spins) {
              const char* p = ws + WS_GU + (size_t)(r * 128 + w * 16) * 64 + lane * 16; const u64 v0 = ld8_sc1(p), v1 = ld8_sc1(p + 8); gq = (u32x4){(unsigned)v0, (unsigned)(v0 >> 32), (unsigned)v1, (unsigned)(v1 >> 32)};
              if (__all(gq[1] == tag32 && gq[3] == tag32)) break;
              __builtin_amdgcn_s_sleep(2); }
          float sm = __uint_as_float(gq[0]) + __uint_as_float(gq[2]);
          sm = rb(sm + __uint_as_float(__builtin_amdgcn_update_dpp(0, (int)__float_as_uint(sm), 0xB1, 0xf, 0xf, false)));
          const float uu = __uint_as_float(__builtin_amdgcn_update_dpp(0, (int)__float_as_uint(sm), 0x4E, 0xf, 0xf, false));
          if (!(lane & 3)) LD(unsigned short, M_H + w * 32 + (lane >> 2) * 2) = (unsigned short)pk2(rb(silu(sm)) * uu, 0.f);
      }
      const unsigned vm = R2 + (w >> 2) * 16384 + MROW() * 128 + ((MSWZ() ^ (w & 3)) * 32) + (lane & 3) * 8, mm = tag32, ha = M_H + (g4 >> 1) * 32 + (g4 & 1) * 8, yo = lane * 8;
      smo = (u64)(ws + WS_YG + (rm * 1024 + xcd * 128 + w * 16) * 8);
      BAR_VM(0); SEAM(6); TS(16);
#define DM(h, d, c) M16("v[248:251], v[" #h ":" #h "+3], v[" #d ":" #d "+3], " c)
      asm volatile(BODY_TXT(T0(WOA(0) B64(224, "v15", 0) B64(226, "v15", 16) TR2(228, "v19", 0, 1024), WOA(1024) B64(232, "v15", 64) B64(234, "v15", 80) TR2(236, "v19", 4096, 1024), NO),
                            T1(WOB(0), WOB(1024), DM(224, 228, "0") DM(232, 236, "v[248:251]")),
                            T2(WOB(2048) B64(224, "v15", 128) B64(226, "v15", 144) TR2(228, "v19", 8192, 1024), WOB(3072) B64(232, "v15", 192) B64(234, "v15", 208) TR2(236, "v19", 12288, 1024), NO),
                            T3(KV0, KVN(1024), DM(224, 228, "v[248:251]") DM(232, 236, "v[248:251]")), T4(KVN(2048), KVN(3072), NO),
                            T5("v_mov_b32 v249, %[mm]\n", "s_mov_b32 exec_lo, %[ye]\n s_mov_b32 exec_hi, 0\n global_store_dwordx2 v16, v[248:249], s[72:73] sc1\n s_mov_b64 exec, -1\n", NO),
                            T6(NO, VM(5) TRA(24, 0) TRA(25, 1024), NO), T7(TRB(26, 0) TRB(27, 1024), TRB(28, 2048) TRB(29, 3072), NO))
                   : BODY_OUT, "=&{v[224:231]}"(wf[12]), "=&{v[232:239]}"(wf[13]), "=&{v[240:247]}"(wf[14]) : BODY_IN, "{v15}"(ha), "{v16}"(yo), [ye] "s"(ye)
                   : "memory", "scc", "v248", "v249", "v250", "v251"); TS(24); }
    { BAR_VM(1); SEAM(7);
      const unsigned vm = vw ^ ((vw >> 9) & 48), mm = mb; smo = (u64)(x + (size_t)(tb * 64 + kh * 32) * 2048 + hg * 256 + cbk * 64); TS(17);
      asm volatile(BODY_TXT(T0(WOA(0), NO, NO), T1(WOA(1024), NO, NO), T2(MOE(0, 0), NO, NO), T3(MOE(1024, 32768), NO, NO), T4(NO, NO, NO), T5(NO, NO, NO), T6(NO, NO, NO), T7(VM(0) TRA(30, 0) TRA(31, 1024), NO, NO))
                   : BODY_OUT, "=&{v[248:255]}"(wf[15]) : BODY_IN : "memory", "scc"); TS(25); }
    {
    RELANE(); const unsigned vl = lane * 16;
    u32x4 o0, o1;
    const bool routed = tb == (idx >> 6) && kh == ((idx >> 5) & 1);
    u64 yg[14];
    if (routed) for (int i = 0; i < 14; ++i) yg[i] = ld8_sc1(ws + WS_YG + ((h2 * 14 + i) * 1024 + hg * 128 + cbk * 32 + l31) * 8);
    u32x2 xr[4]; for (int q = 0; q < 4; ++q) xr[q] = LD(u32x2, mb + l31 * 64 + ((q ^ ((l31 >> 2) & 3)) * 16) + h2 * 8);
    const f32x2 stt = LD(f32x2, M_ST64 + (tbk * 32 + l31) * 8);
    {
        const u32x8 P = __builtin_shufflevector(PK, PK, 0, 1, 2, 3, 4, 5, 6, 7); f32x4 L = __builtin_bit_cast(f32x4, lo4(LS));
        asm volatile("s_nop 1\n\tv_smfmac_f32_16x16x64_bf16 %0, %1, %2, %3\n\ts_nop 11" : "+v"(L) : "v"(hi4(LS)), "v"(P), "v"(IDX));
        O = mfma32(hi4(V), hi4(P), O);
        const auto l16 = __builtin_amdgcn_permlane16_swap(__float_as_uint(L[0] + L[1]), __float_as_uint(L[2] + L[3]), false, false);
        float lsum = __uint_as_float(__builtin_amdgcn_permlane32_swap(l16[0], l16[0], false, false)[0]);
        const float om = fmaxf(fmaxf(fmaxf(fmaxf(fabsf(O[0]), fabsf(O[1])), fabsf(O[2])), fmaxf(fmaxf(fabsf(O[3]), fabsf(O[4])), fabsf(O[5]))), fmaxf(fmaxf(fmaxf(fabsf(O[6]), fabsf(O[7])), fabsf(O[8])),
                         fmaxf(fmaxf(fmaxf(fabsf(O[9]), fabsf(O[10])), fabsf(O[11])), fmaxf(fmaxf(fmaxf(fabsf(O[12]), fabsf(O[13])), fabsf(O[14])), fabsf(O[15])))));
        if (__builtin_expect(__any(!(lsum <= 1.8446744e19f) || !(om <= 3.0e38f)), 0)) {
            float mr = -INFINITY; lsum = 0.f; for (int i = 0; i < 16; ++i) O[i] = 0.f;
#pragma unroll 1
            for (int t = 0; t < 64; ++t) {
                u32x4 k0, k1, v0, v1;
                asm volatile("global_load_dwordx4 %0, %4, %5 sc1\n\tglobal_load_dwordx4 %1, %4, %5 offset:1024 sc1\n\tglobal_load_dwordx4 %2, %4, %6 offset:2048 sc1\n\tglobal_load_dwordx4 %3, %4, %7 offset:3072 sc1\n\ts_waitcnt vmcnt(0)"
                             : "=&v"(k0), "=&v"(k1), "=&v"(v0), "=&v"(v1) : "v"(vl), "s"(kv + ((t + 62) & 63) * 4096), "s"(kv + ((t + 63) & 63) * 4096), "s"(kv + t * 4096) : "memory");
                f32x16 z; for (int i = 0; i < 16; ++i) z[i] = 0.f;
                f32x16 s = mfma32(k0, lo4(Q), z); s = mfma32(k1, hi4(Q), s);
                float m = s[0]; for (int i = 1; i < 16; ++i) m = fmaxf(m, s[i]);
                m = fmaxf(mr, xmax32(m)); const float al = __builtin_amdgcn_exp2f(mr - m); mr = m;
                u32x8 pp; float ps = 0.f;
                for (int i = 0; i < 8; ++i) { pp[i] = pk2(__builtin_amdgcn_exp2f(s[2 * i] - m), __builtin_amdgcn_exp2f(s[2 * i + 1] - m)); ps += bf_lo(pp[i]) + bf_hi(pp[i]); }
                lsum = lsum * al + xsum32(ps); for (int i = 0; i < 16; ++i) O[i] *= al;
                O = mfma32(v0, lo4(pp), O); O = mfma32(v1, hi4(pp), O);
            }
        }
        const float rl = __builtin_amdgcn_rcpf(lsum);
        for (int i = 0; i < 4; ++i) { o0[i] = pk2(O[2 * i] * rl, O[2 * i + 1] * rl); o1[i] = pk2(O[8 + 2 * i] * rl, O[8 + 2 * i + 1] * rl); }
        asm volatile("global_store_dwordx4 %0, %1, %3 sc1\n\tglobal_store_dwordx4 %0, %2, %3 offset:2048 sc1\n\ts_nop 1"
                     :: "v"(vl), "v"(o0), "v"(o1), "s"(ws + WS_O + (size_t)(tb * 8 + hg) * 16384 + hd * 4096 + tbk * 1024) : "memory");
    }
    TS(26);
    BAR_LDS();
    TS(27);
    const unsigned pv = w * PRIV;
    LD(u32x4, hg * PRIV + hd * 4096 + tbk * 1024 + vl) = o0; LD(u32x4, hg * PRIV + hd * 4096 + 2048 + tbk * 1024 + vl) = o1;
    unsigned xn2[8], g2[8];
    for (int i = 0; i < 8; ++i) { xn2[i] = ln2(xr[i >> 1][i & 1], stt[0], stt[1]); PIN(xn2[i]); }
    WAIT_VM(0);
    if (lane == 0) st8_sc1(ws + WS_FLAG4 + b * 128 + w * 8, seed);
    TS(28);
    if (routed) {
#pragma unroll 1
        for (unsigned spins = 0; spins < SPIN_CAP; ++spins) {
            bool ok = true; for (int i = 0; i < 14; ++i) ok &= (unsigned)(yg[i] >> 32) == tag32;
            if (__all(ok)) break;
            __builtin_amdgcn_s_sleep(2);
            for (int i = 0; i < 14; ++i) yg[i] = ld8_sc1(ws + WS_YG + ((h2 * 14 + i) * 1024 + hg * 128 + cbk * 32 + l31) * 8);
        }
        float acc = 0.f; for (int i = 0; i < 14; ++i) acc += __uint_as_float((unsigned)yg[i]);
        const unsigned o16 = (gkey >> 11) & 0xffffu, rbits = (o16 & 0x8000u) ? (o16 & 0x7fffu) : (~o16 & 0xffffu);
        const float rsel = rb(__builtin_amdgcn_rcpf(1.f + __builtin_amdgcn_exp2f(-1.44269504089f * bf_lo(rbits))));
        LD(float, pv + 16384 + lane * 4) = rb(rb(xsum32(acc)) * rsel);
        for (int q = 0; q < 4; ++q) { const f32x4 y = LD(f32x4, pv + 16384 + q * 32 + h2 * 16);
            for (int i = 0; i < 2; ++i) { const unsigned t = pk2(bf_lo(xn2[2 * q + i]) + y[2 * i], bf_hi(xn2[2 * q + i]) + y[2 * i + 1]); xn2[2 * q + i] = l31 == (idx & 31) ? t : xn2[2 * q + i]; } }
    }
    for (int i = 0; i < 8; ++i) { g2[i] = pk2(silu(bf_lo(xn2[i])), silu(bf_hi(xn2[i]))); PIN(g2[i]); }
    TS(31);
    if (w != hg) {
        const int pb = (((w & 3) * 8 + (tb & 7)) << 3) | ((tb >> 3) * 2 + (w >> 2));
#pragma unroll 1
        for (unsigned spins = 0; spins < SPIN_CAP; ++spins) { if (__all(ld8_sc1(ws + WS_FLAG4 + pb * 128 + (lane & 7) * 8) == seed)) break; __builtin_amdgcn_s_sleep(1); }
        TS(29);
        const char* src = ws + WS_O + (size_t)(tb * 8 + w) * 16384;
        for (int i = 0; i < 4; ++i) dma4_sc1(src + i * 4096, vl, pv + i * 4096);
    }
    BAR_VM(12);
    TS(6);
    f32x16 ao, at; for (int i = 0; i < 16; ++i) { ao[i] = 0.f; at[i] = 0.f; }
    {
        const unsigned oa = kh * 4 * PRIV + vl + kh * 1024, ob = oa ^ 1024;
        u32x4 fr[2][8]; f32x4 pt[4];
#define OAD(g, i) ((g) < 6 ? (((i) & 1) ? oa : ob) + (((g) & 1) * 2 + ((i) >> 2)) * PRIV + (((g) >> 1) * 2 + (((i) >> 1) & 1)) * 2048 : ((g) == 6 ? ob : oa) + ((i) >> 1) * PRIV + (6 + ((i) & 1)) * 2048)
#define OWJ(g, i) ((g) < 6 ? (((g) & 1) * 2 + ((i) >> 2)) * 8 + ((g) >> 1) * 2 + (((i) >> 1) & 1) : ((i) >> 1) * 8 + 6 + ((i) & 1))
#define ORD(p, g) do { for (int i = 0; i < 8; ++i) fr[p][i] = LD(u32x4, OAD(g, i)); } while (0)
        __builtin_amdgcn_s_setprio(1);
        ORD(0, 0); SBAR();
#pragma unroll
        for (int g = 0; g < 8; ++g) {
            if (g == 1) asm volatile("s_waitcnt vmcnt(8)\n\ts_barrier" ::: "memory");
            if (g == 3) asm volatile("s_waitcnt vmcnt(4)\n\ts_barrier" ::: "memory");
            if (g == 5) asm volatile("s_waitcnt vmcnt(0)\n\ts_barrier" ::: "memory");
            if (g == 7) { for (int q = 0; q < 4; ++q) LD(f32x4, pv + 16384 + q * 1024 + vl) = (f32x4){at[4 * q], at[4 * q + 1], at[4 * q + 2], at[4 * q + 3]}; __builtin_amdgcn_s_setprio(0); SBAR(); }
            if (g < 7) ORD((g + 1) & 1, g + 1);
            for (int i = 0; i < 8; ++i) { const int j_ = OWJ(g, i); const u32x4 a = (j_ & 1) ? hi4(wf[j_ >> 1]) : lo4(wf[j_ >> 1]);
                if (g < 6 ? (i & 1) : g == 7) ao = mfma32(a, fr[g & 1][i], ao); else at = mfma32(a, fr[g & 1][i], at);
                if (g == 7 && i == 3) { SBAR(); BAR_LDS(); for (int q = 0; q < 4; ++q) pt[q] = LD(f32x4, (w ^ 4) * PRIV + 16384 + q * 1024 + vl); SBAR(); } }
            if (g < 7) for (int i = 0; i < 8; ++i) { SGB(0x008, 1); SGB(0x100, 1); }
            SBAR();
        }
        TS(30);
        char* op = out + (size_t)(tb * 64 + kh * 32 + l31) * 2048 + hg * 256 + cbk * 64 + h2 * 8;
        for (int q = 0; q < 4; ++q) {
            u32x2 o;
            for (int i = 0; i < 2; ++i) {
                const unsigned s2 = pk2(ao[4 * q + 2 * i] + pt[q][2 * i], ao[4 * q + 2 * i + 1] + pt[q][2 * i + 1]), gg = g2[2 * q + i];
                const unsigned p2 = pk2(bf_lo(s2) * bf_lo(gg), bf_hi(s2) * bf_hi(gg));
                o[i] = pk2(bf_lo(xr[q][i]) + bf_lo(p2), bf_hi(xr[q][i]) + bf_hi(p2));
            }
            *(u32x2*)(op + q * 16) = o;
        }
    }
    }
    TS(7);
}

extern "C" void kernel_launch(void* const* d_in, const int* in_sizes, int n_in, void* d_out, int out_size, void* d_ws, size_t ws_size, hipStream_t stream) {
    (void)hipFuncSetAttribute((const void*)fused_block, hipFuncAttributeMaxDynamicSharedMemorySize, LDS_BYTES);
    fused_block<<<256, 512, LDS_BYTES, stream>>>((const char*)d_in[0], (const char*)d_in[1], (const char*)d_in[2], (const char*)d_in[3], (const char*)d_in[4],
                                                 (const char*)d_in[6], (const char*)d_in[7], (const char*)d_in[8], (const char*)d_in[9], (char*)d_out, (char*)d_ws);
}
```

```cpp
#define PRIOV 1
#define FSTAGE 4
#include <hip/hip_runtime.h>
#include <hip/hip_bf16.h>
#include <stdint.h>
#include <type_traits>

typedef unsigned long long u64;
typedef float f32x16 __attribute__((ext_vector_type(16)));
typedef float f32x4 __attribute__((ext_vector_type(4)));
typedef float f32x2 __attribute__((ext_vector_type(2)));
typedef unsigned u32x2 __attribute__((ext_vector_type(2)));
typedef unsigned u32x4 __attribute__((ext_vector_type(4)));
typedef unsigned u32x8 __attribute__((ext_vector_type(8)));
typedef unsigned u32x16 __attribute__((ext_vector_type(16)));
typedef __bf16 bf16x8 __attribute__((ext_vector_type(8)));
typedef __bf16 bf16x2 __attribute__((ext_vector_type(2)));
typedef short s16x4 __attribute__((ext_vector_type(4)));
#define LAS __attribute__((address_space(3)))
#define GAS __attribute__((address_space(1)))
#define DI __device__ __forceinline__
#define LD(T, a) (*(LAS T*)(unsigned)(a))

constexpr unsigned WS_EPOCH = 0, WS_FLAG1 = 4096, WS_STATS = 8192, WS_FLAG4 = 65536, WS_KV = 131072, WS_YG = 393216, WS_GU = 622592, WS_CS = 851968, WS_O = 1u << 20;
constexpr unsigned WS_DBG = 6u << 20;
constexpr unsigned SLOT = 24576, L_X16 = 49152, L_WR = 81920, L_SP = 86016, L_FIN = 87040, L_PA = 73728, L_PC = 88064, L_WKV = 98304, L_QX = 0, L_PRO = 16384, L_CSP = 73728, L_MISC = 155648, LDS_BYTES = 163840;
constexpr unsigned M_CTL = L_MISC, M_ST64 = L_MISC + 256, M_STX = L_MISC + 1280, M_CS = L_MISC + 1536, M_XSEL = L_MISC + 2560, M_H = L_MISC + 3584, M_GU = L_MISC + 4096;
constexpr unsigned KA = 122880, KB = 32768, R2 = 65536, R3 = 0, R4 = 98304, M_GR = 114688, PRIV = 20480;
constexpr int slot_of(int s) { return s % 6 < 3 ? s % 6 : s % 6 == 5 ? 3 : s % 6 + 1; }
constexpr unsigned SPIN_CAP = 1u << 20;
constexpr int CODE_SPAN = 38912;

DI float bf_lo(unsigned v) { return __uint_as_float(v << 16); }
DI float bf_hi(unsigned v) { return __uint_as_float(v & 0xffff0000u); }
DI unsigned pk2(float lo, float hi) { f32x2 v = {lo, hi}; return __builtin_bit_cast(unsigned, __builtin_convertvector(v, bf16x2)); }
DI float rb(float f) { return __uint_as_float((unsigned)__builtin_bit_cast(unsigned short, (__bf16)f) << 16); }
DI float xsum32(float v) { auto r = __builtin_amdgcn_permlane32_swap(__float_as_uint(v), __float_as_uint(v), false, false); return __uint_as_float(r[0]) + __uint_as_float(r[1]); }
DI float xmax32(float v) { auto r = __builtin_amdgcn_permlane32_swap(__float_as_uint(v), __float_as_uint(v), false, false); return fmaxf(__uint_as_float(r[0]), __uint_as_float(r[1])); }
DI f32x16 mfma32(u32x4 a, u32x4 b, f32x16 c) { return __builtin_amdgcn_mfma_f32_32x32x16_bf16(__builtin_bit_cast(bf16x8, a), __builtin_bit_cast(bf16x8, b), c, 0, 0, 0); }
DI f32x4 mfma16(u32x4 a, u32x4 b, f32x4 c) { return __builtin_amdgcn_mfma_f32_16x16x32_bf16(__builtin_bit_cast(bf16x8, a), __builtin_bit_cast(bf16x8, b), c, 0, 0, 0); }
DI u32x2 trd(unsigned a) { return __builtin_bit_cast(u32x2, __builtin_amdgcn_ds_read_tr16_b64_v4i16((LAS s16x4*)a)); }
DI u32x4 trfrag(unsigned a, unsigned step) { u32x2 lo = trd(a), hi = trd(a + step); return (u32x4){lo[0], lo[1], hi[0], hi[1]}; }
DI u32x4 lo4(u32x8 v) { return __builtin_shufflevector(v, v, 0, 1, 2, 3); }
DI u32x4 hi4(u32x8 v) { return __builtin_shufflevector(v, v, 4, 5, 6, 7); }
DI float silu(float v) { return v * __builtin_amdgcn_rcpf(1.f + __builtin_amdgcn_exp2f(-1.44269504089f * v)); }
DI unsigned ln2(unsigned v, float m, float rs) { const unsigned d = pk2(bf_lo(v) - m, bf_hi(v) - m); return pk2(bf_lo(d) * rs, bf_hi(d) * rs); }
DI u64 ld8_sc1(const void* p) { return __hip_atomic_load((GAS const u64*)p, __ATOMIC_RELAXED, __HIP_MEMORY_SCOPE_AGENT); }
DI void st8_sc1(void* p, u64 v) { __hip_atomic_store((GAS u64*)p, v, __ATOMIC_RELAXED, __HIP_MEMORY_SCOPE_AGENT); }
DI int fresh_lane() { int l; asm volatile("v_mbcnt_lo_u32_b32 %0, -1, 0\n\tv_mbcnt_hi_u32_b32 %0, -1, %0" : "=v"(l)); return l; }
#define RELANE() const int lane = fresh_lane(), l15 = lane & 15, g4 = lane >> 4, l31 = lane & 31, h2 = lane >> 5; (void)l15; (void)g4; (void)l31; (void)h2
DI u64 mix(unsigned lo, unsigned hi) { u64 z = (((u64)hi << 32) | lo) + 0x9E3779B97F4A7C15ull; z = (z ^ (z >> 30)) * 0xBF58476D1CE4E5B9ull; z = (z ^ (z >> 27)) * 0x94D049BB133111EBull; return z ^ (z >> 31); }
DI unsigned rowmax16(unsigned k) { k = max(k, (unsigned)__builtin_amdgcn_update_dpp(0, (int)k, 0x128, 0xf, 0xf, false)); k = max(k, (unsigned)__builtin_amdgcn_update_dpp(0, (int)k, 0x124, 0xf, 0xf, false));
    k = max(k, (unsigned)__builtin_amdgcn_update_dpp(0, (int)k, 0x122, 0xf, 0xf, false)); return max(k, (unsigned)__builtin_amdgcn_update_dpp(0, (int)k, 0x121, 0xf, 0xf, false)); }
DI void dma1(const void* sbase, unsigned voff, unsigned m0v) { asm volatile("s_mov_b32 m0, %2\n\ts_nop 0\n\tglobal_load_lds_dwordx4 %0, %1" :: "v"(voff), "s"(sbase), "s"(m0v) : "memory"); }
DI void dma1_sc1(const void* sbase, unsigned voff, unsigned m0v) { asm volatile("s_mov_b32 m0, %2\n\ts_nop 0\n\tglobal_load_lds_dwordx4 %0, %1 sc1" :: "v"(voff), "s"(sbase), "s"(m0v) : "memory"); }
DI void dma4(const void* sbase, unsigned voff, unsigned m0v) {
    asm volatile("s_mov_b32 m0, %2\n\ts_nop 0\n\tglobal_load_lds_dwordx4 %0, %1\n\tglobal_load_lds_dwordx4 %0, %1 offset:1024\n\tglobal_load_lds_dwordx4 %0, %1 offset:2048\n\tglobal_load_lds_dwordx4 %0, %1 offset:3072"
                 :: "v"(voff), "s"(sbase), "s"(m0v) : "memory"); }
DI void dma4_sc1(const void* sbase, unsigned voff, unsigned m0v) {
    asm volatile("s_mov_b32 m0, %2\n\ts_nop 0\n\tglobal_load_lds_dwordx4 %0, %1 sc1\n\tglobal_load_lds_dwordx4 %0, %1 offset:1024 sc1\n\tglobal_load_lds_dwordx4 %0, %1 offset:2048 sc1\n\tglobal_load_lds_dwordx4 %0, %1 offset:3072 sc1"
                 :: "v"(voff), "s"(sbase), "s"(m0v) : "memory"); }
#define BAR_ALL() asm volatile("s_waitcnt vmcnt(0) lgkmcnt(0)\n\ts_barrier" ::: "memory")
#define BAR_LDS() asm volatile("s_waitcnt lgkmcnt(0)\n\ts_barrier" ::: "memory")
#define BAR_VM(n) asm volatile("s_waitcnt vmcnt(" #n ") lgkmcnt(0)\n\ts_barrier" ::: "memory")
#define WAIT_VM(n) asm volatile("s_waitcnt vmcnt(" #n ")" ::: "memory")
#define PIN(v) asm volatile("" : "+v"(v))
#define SBAR() __builtin_amdgcn_sched_barrier(0)
#define SGB(m, n) __builtin_amdgcn_sched_group_barrier(m, n, 0)
#define TS(i) do { } while (0)

#define EX(A,i) "v_exp_f32 v[" #A "+" #i "], v[" #A "+" #i "]\n"
#define E4(A,a,b,c,d) EX(A,a) EX(A,b) EX(A,c) EX(A,d)
#define CV(p,A,i) "v_cvt_pk_bf16_f32 v[104+" #p "], v[" #A "+" #i "], v[" #A "+" #i "+1]\n"
#define RDX(dst, o, k) "ds_read_b128 " dst ", v22 offset:" o "+" #k "\n"
#define M32(x) "v_mfma_f32_32x32x16_bf16 " x "\n"
#define M16(x) "v_mfma_f32_16x16x32_bf16 " x "\n"
#define TILE(A, B, o, X1, X2, Y) \
    M32("v[" #B ":" #B "+15], v[112:115], v[96:99], v[64:79]") RDX("v[112:115]", o, 0) X1 E4(A,0,1,2,3) \
    M32("v[80:95], v[124:127], v[108:111], v[80:95]") RDX("v[124:127]", o, 3072) E4(A,4,5,6,7) \
    "v_smfmac_f32_16x16x64_bf16 v[24:27], v[28:31], v[104:111], v23\n" "s_waitcnt lgkmcnt(2)\n" CV(0,A,0) CV(1,A,2) CV(2,A,4) CV(3,A,6) \
    M32("v[" #B ":" #B "+15], v[116:119], v[100:103], v[" #B ":" #B "+15]") RDX("v[116:119]", o, 1024) X2 E4(A,8,9,10,11) \
    M32("v[80:95], v[120:123], v[104:107], v[80:95]") RDX("v[120:123]", o, 2048) E4(A,12,13,14,15) Y \
    "s_waitcnt lgkmcnt(2)\n" CV(4,A,8) CV(5,A,10) CV(6,A,12) CV(7,A,14)
#define NO ""
#define WOA(k) "s_add_u32 m0, %[ma], " #k "\n s_add_u32 s70, s70, 0x8000\n s_addc_u32 s71, s71, 0\n global_load_lds_dwordx4 v20, s[70:71]\n"
#define WOB(k) "s_add_u32 m0, %[mb], " #k "\n s_add_u32 s70, s70, 0x8000\n s_addc_u32 s71, s71, 0\n global_load_lds_dwordx4 v20, s[70:71]\n"
#define MOE(k, st) "s_add_u32 m0, %[mm], " #k "\n s_add_u32 s72, s72, " #st "\n s_addc_u32 s73, s73, 0\n global_load_lds_dwordx4 v19, s[72:73]\n"
#define KV0 "s_mov_b32 m0, %[mk]\n s_nop 0\n global_load_lds_dwordx4 v21, %[sk] sc1\n"
#define KVN(k) "global_load_lds_dwordx4 v21, %[sk] offset:" #k " sc1\n"
#define GRD "s_mov_b32 m0, %[mm]\n s_nop 0\n global_load_lds_dwordx4 v21, s[72:73] sc1\n"
#define TR2(d, a, k, st) "ds_read_b64_tr_b16 v[" #d ":" #d "+1], " a " offset:" #k "\n ds_read_b64_tr_b16 v[" #d "+2:" #d "+3], " a " offset:" #k "+" #st "\n"
#define TRA(j, k) TR2(128+4*j, "v18", k, 512)
#define TRB(j, k) TR2(128+4*j, "v17", k, 512)
#define B64(d, a, k) "ds_read_b64 v[" #d ":" #d "+1], " a " offset:" #k "\n"
#define VM(n) "s_waitcnt vmcnt(" #n ")\n"
#define BODY_TXT(T0, T1, T2, T3, T4, T5, T6, T7) "s_nop 1\n s_setprio 3\n" T0 T1 T2 T3 T4 "s_setprio 2\n" T5 T6 "s_setprio 1\n" T7 "s_waitcnt lgkmcnt(0)\n s_nop 7\n"
#define PH "s_setprio 0\n"
#define T0(a,b,c) TILE(32,48,"0",a,b,c)
#define T1(a,b,c) TILE(48,32,"4096",a,b,c)
#define T2(a,b,c) TILE(32,48,"8192",a,b,c)
#define T3(a,b,c) TILE(48,32,"12288",a,b,c)
#define T4(a,b,c) TILE(32,48,"16384",a,b,c)
#define T5(a,b,c) TILE(48,32,"20480",a,b,c)
#define T6(a,b,c) TILE(32,48,"24576",a,b,c)
#define T7(a,b,c) TILE(48,32,"28672",a,PH b,c)
#define BODY_OUT "+{v[32:47]}"(SA), "=&{v[48:63]}"(SB), "+{v[80:95]}"(O), "+{v[24:31]}"(LS), "+{v[104:119]}"(PK), "+{v[120:127]}"(V), "+{s[70:71]}"(swo), "+{s[72:73]}"(smo)
#define BODY_IN "{v[64:79]}"(NEG), "{v[96:103]}"(Q), "{v23}"(IDX), "{v22}"(ad), "{v21}"(vl), "{v20}"(vw), "{v19}"(vm), "{v18}"(ta), "{v17}"(tb_), \
      [ma] "s"(ma), [mb] "s"(mb), [mm] "s"(mm), [mk] "s"(mk), [sk] "s"(sk)
#define C8(a,b,c,d,e,f,g,h) "v" #a, "v" #b, "v" #c, "v" #d, "v" #e, "v" #f, "v" #g, "v" #h

__global__ __launch_bounds__(512, 2) void fused_block(const char* __restrict__ x, const char* __restrict__ wq, const char* __restrict__ wk, const char* __restrict__ wv,
                                                      const char* __restrict__ wo, const char* __restrict__ wr, const char* __restrict__ wg, const char* __restrict__ wu,
                                                      const char* __restrict__ wd, char* __restrict__ out, char* __restrict__ ws) {
    const int tid = threadIdx.x, lane = tid & 63, w = __builtin_amdgcn_readfirstlane(tid >> 6);
    const int b = blockIdx.x, xcd = b & 7, r = b >> 3, tb = (xcd >> 1) * 8 + (r & 7), hg = (xcd & 1) * 4 + (r >> 3);
    const int isV = r >> 4, sub = (r >> 2) & 3, T0 = (xcd * 4 + (r & 3)) * 64 + sub * 16;
    const int l15 = lane & 15, g4 = lane >> 4, l31 = lane & 31, h2 = lane >> 5;
    const int hd = w & 3, tbk = w >> 2;
    const unsigned vl = lane * 16;
    TS(0);
    {
        u64 pc; asm volatile("s_getpc_b64 %0" : "=s"(pc));
        asm volatile("s_mov_b32 m0, %2\n\ts_mov_b64 exec, 3\n\tglobal_load_lds_dword %0, %1\n\ts_mov_b64 exec, -1"
                     :: "v"(lane * 128), "s"((pc & ~127ull) + min((r * 8 + w) * 256, CODE_SPAN - 256)), "s"(w < 4 ? L_X16 + w * 8192 : L_WKV + (w - 4) * 16384) : "memory");
    }
    const int c0 = r & 7, rm = min(r, 27), mi = xcd * 28 + rm; const char* kv = ws + WS_KV;
    f32x16 qa; float csum;
    u32x8 LS, PZ; f32x16 O; unsigned IDX, vw, vm0, ta, tb_;
    const unsigned ma = R4 + w * 2048, mb = R3 + w * 4096;
#define SETUP() do { const unsigned one_ = 0x3F803F80u; LS = (u32x8){0u, 0u, 0u, 0u, l15 < 2 ? one_ : 0u, l15 < 2 ? one_ : 0u, (l15 & 14) == 2 ? one_ : 0u, (l15 & 14) == 2 ? one_ : 0u}; \
        IDX = (lane & 1) ? 0xEEEEEEEEu : 0x44444444u; vw = (lane >> 2) * 2048 + (lane & 3) * 16; vm0 = (lane >> 3) * 7168 + (((lane & 7) ^ (g4 << 1)) << 4); \
        ta = ma + (4 * h2 + (l15 >> 2)) * 64 + (g4 & 1) * 32 + (lane & 3) * 8; tb_ = ta + (mb - ma); for (int i = 0; i < 16; ++i) O[i] = 0.f; PZ = (u32x8){0u, 0u, 0u, 0u, 0u, 0u, 0u, 0u}; PIN(O); PIN(PZ); PIN(LS); PIN(IDX); PIN(vw); PIN(vm0); PIN(ta); PIN(tb_); } while (0)
    if (__builtin_expect(w < 4, 1)) {
        __builtin_amdgcn_s_setprio(3);
        u32x2 ev; u32x4 xq[8], aw[8], sel, ones; unsigned wa, wa2, spa, kvo, wb[3][2], xb_[3][4];
        {
            const char* xs = x + (size_t)T0 * 2048 + w * 512;
            for (int i = 0; i < 8; ++i) dma1(xs, (2 * i + h2) * 2048 + ((l31 ^ (2 * i + h2)) << 4), L_X16 + w * 8192 + i * 1024);
            dma1(wr + w * 512, l31 * 16, L_WR + w * 1024);
            asm volatile("s_mov_b64 exec, 1\n\tglobal_load_dwordx2 %0, %1, %2 sc1\n\ts_mov_b64 exec, -1" : "=v"(ev) : "v"(0), "s"(ws));
            SETUP();
            wa = L_WKV + w * 16384 + (8 * g4 + (l15 >> 2)) * 64 + (g4 & 1) * 32 + (lane & 3) * 8; wa2 = wa ^ 32; spa = L_SP + ((l15 + (isV ? 4 * g4 : 0)) & 15) * 16;
            const int kt = T0 >> 5, sh = sub & 1;
            kvo = isV ? ((kt - 1 + sh) & 63) * 4096 + 2048 + sh * 1024 + ((g4 & 1) * 32 + w * 16 + l15) * 16 + (g4 >> 1) * 8
                      : ((kt - 2) & 63) * 4096 + w * 1024 + ((g4 & 1) * 32 + sh * 16 + l15) * 16 + (g4 >> 1) * 8;
            const unsigned sv = (((l15 >> 2) & 1) == (g4 & 1)) ? 0x3F803F80u : 0u, xq_a = l31 * 128 + ((h2 ^ ((l31 >> 1) & 7)) << 4);
            const unsigned wq_a = 8192 + (8 * h2 + (l15 >> 2)) * 256 + ((hd ^ ((lane >> 2) & 3)) << 6) + (g4 & 1) * 32 + (lane & 3) * 8;
            sel = (u32x4){sv, sv, sv, sv}; ones = (u32x4){0x3F803F80u, 0x3F803F80u, 0x3F803F80u, 0x3F803F80u};
            for (int i = 0; i < 3; ++i) {
                wb[i][0] = wq_a + i * 2 * SLOT + (tb & 1) * 8192; wb[i][1] = wq_a + i * 2 * SLOT + (~tb & 1) * 8192; PIN(wb[i][0]); PIN(wb[i][1]);
                for (int ks = 0; ks < 4; ++ks) { xb_[i][ks] = (xq_a ^ (ks * 32) ^ ((tb & 1) * 64)) + i * 2 * SLOT; PIN(xb_[i][ks]); } }
            const unsigned oth = l15 == 1 ? 0x3F803F80u : 0u;
            for (int s = 0; s < 8; ++s) { aw[s] = (u32x4){oth, oth, oth, oth}; PIN(aw[s]); }
            PIN(kvo); PIN(wa); PIN(wa2); PIN(spa); PIN(ones); PIN(sel);
            WAIT_VM(1);
            const unsigned xa = L_X16 + w * 8192 + l15 * 512 + ((g4 ^ l15) << 4);
            for (int s = 0; s < 8; ++s) xq[s] = LD(u32x4, xa ^ (s * 64));
            if (l15 == 0) for (int s = 0; s < 8; ++s) aw[s] = LD(u32x4, L_WR + w * 1024 + s * 64 + g4 * 16);
        }
        TS(8);
        const f32x4 z4 = {0, 0, 0, 0};
        f32x4 s1 = z4, gr = z4, sw = z4;
        for (int s = 0; s < 8; ++s) { gr = mfma16(xq[s], xq[s], gr); s1 = mfma16(aw[s], xq[s], s1); sw = mfma16(aw[s], ones, sw); }
        { float d = (l15 & 2) ? ((l15 & 1) ? gr[3] : gr[2]) : ((l15 & 1) ? gr[1] : gr[0]); d = g4 == (l15 >> 2) ? d : 0.f;
          const auto e16 = __builtin_amdgcn_permlane16_swap(__float_as_uint(d), __float_as_uint(d), false, false); d = xsum32(__uint_as_float(e16[0]) + __uint_as_float(e16[1]));
          if (lane < 16) LD(f32x4, L_SP + w * 256 + vl) = (f32x4){s1[1], d, s1[0], sw[0]}; }
        TS(9);
        BAR_LDS();
        f32x4 m4, r4; unsigned key = 0;
        {
            f32x4 p[4]; for (int q = 0; q < 4; ++q) p[q] = LD(f32x4, spa + q * 256);
            const f32x4 t = (p[0] + p[1]) + (p[2] + p[3]);
            const float mean = t[0] * (1.f / 1024.f), var = fmaxf(t[1] * (1.f / 1024.f) - mean * mean, 0.f);
            const float mrow = rb(mean), rsrow = __builtin_amdgcn_rcpf(rb(__builtin_amdgcn_sqrtf(rb(rb(var) + 1e-5f))));
            const int mi_ = (int)__float_as_uint(mrow), ri_ = (int)__float_as_uint(rsrow);
#define BC(v, n) __uint_as_float((unsigned)__builtin_amdgcn_update_dpp(0, v, 0x150 + (n), 0xf, 0xf, false))
            m4 = (f32x4){BC(mi_, 0), BC(mi_, 1), BC(mi_, 2), BC(mi_, 3)}; r4 = (f32x4){BC(ri_, 0), BC(ri_, 1), BC(ri_, 2), BC(ri_, 3)};
            if (!isV) { m4 = (f32x4){mrow, mrow, mrow, mrow}; r4 = (f32x4){rsrow, rsrow, rsrow, rsrow};
                if (w == 2) { const unsigned u = __float_as_uint(rb((t[2] - mrow * t[3]) * rsrow)) >> 16;
                    key = rowmax16(((((u & 0x8000u) ? ~u : (u | 0x8000u)) & 0xffffu) << 11) | (2047 - (T0 + l15))); } }
        }
        PIN(m4); PIN(r4);
        TS(34);
        BAR_LDS();
        TS(33);
        auto kvpart = [&](auto V_) {
            constexpr bool V = decltype(V_)::value;
            u32x4 w0[8], w1[8]; f32x4 a0 = z4, a1 = z4, c0_ = z4, c1_ = z4;
            for (int s = 0; s < 3; ++s) { w0[s] = trfrag(wa + s * 2048, 256); w1[s] = trfrag(wa2 + s * 2048, 256); }
            SBAR();
#pragma unroll
            for (int s = 0; s < 8; ++s) {
                if (s < 5) { w0[s + 3] = trfrag(wa + (s + 3) * 2048, 256); w1[s + 3] = trfrag(wa2 + (s + 3) * 2048, 256); }
                if (V) { a0 = mfma16(xq[s], w0[s], a0); c0_ = mfma16(ones, w0[s], c0_); a1 = mfma16(xq[s], w1[s], a1); c1_ = mfma16(ones, w1[s], c1_); }
                else { a0 = mfma16(w0[s], xq[s], a0); c0_ = mfma16(w0[s], ones, c0_); a1 = mfma16(w1[s], xq[s], a1); c1_ = mfma16(w1[s], ones, c1_); }
                if (s < 5) for (int i = 0; i < 4; ++i) { SGB(0x008, 1); SGB(0x100, 1); }
                SBAR();
            }
            LD(f32x4, L_PA + w * 2048 + vl) = (a0 - m4 * c0_) * r4; LD(f32x4, L_PA + w * 2048 + 1024 + vl) = (a1 - m4 * c1_) * r4;
        };
        if (isV) kvpart(std::true_type{}); else kvpart(std::false_type{});
        asm volatile("s_waitcnt vmcnt(0)" : "+v"(ev) :: "memory");
        if (tid == 192) LD(u32x2, M_CTL) = ev;
        TS(35);
        BAR_LDS();
        TS(1);
        if (w < 2) {
            f32x4 p[4]; for (int i = 0; i < 4; ++i) p[i] = LD(f32x4, L_PA + i * 2048 + w * 1024 + vl);
            const f32x4 a = (p[0] + p[1]) + (p[2] + p[3]);
            st8_sc1(ws + WS_KV + kvo, ((u64)pk2(a[2], a[3]) << 32) | pk2(a[0], a[1]));
        } else if (w == 2 && !isV && lane < 16) st8_sc1(ws + WS_STATS + (T0 + l15) * 8, ((u64)__float_as_uint(r4[0]) << 32) | __float_as_uint(m4[0]));
        f32x16 qb; qa = O; qb = O;
        u32x4 af[2][4], b0[2][4], b1[2][4];
#define QRD(p, s) do { const unsigned sl_ = (slot_of(s) & 1) * SLOT; const int bi_ = slot_of(s) >> 1; for (int ks = 0; ks < 4; ++ks) { \
            af[p][ks] = trfrag(wb[bi_][ks >> 1] + sl_ + (ks & 1) * 4096, 1024); b1[p][ks] = LD(u32x4, xb_[bi_][ks] + sl_ + 4096); b0[p][ks] = LD(u32x4, xb_[bi_][ks] + sl_); } } while (0)
#pragma unroll
        for (int s = 0; s < 16; ++s) {

            if (s == FSTAGE || s == 10) WAIT_VM(0);
            if (!(s & 1)) { BAR_LDS(); if (s == 0) TS(37); if (s == 2) TS(39); if (s == 4) TS(41); if (s == 6) TS(43); if (s == 8) TS(45); if (s == 10) TS(47); if (s == 12) TS(49); if (s == 14) TS(51); }
            if (s == 0) { QRD(0, 0); BAR_LDS();
                const f32x4 cs = mfma16(sel, af[0][1], mfma16(sel, af[0][0], z4));
                if (lane < 32) __hip_atomic_store((GAS unsigned*)(ws + WS_CS + (hg * 32 + tb) * 512 + w * 128 + lane * 4), __float_as_uint(cs[0]), __ATOMIC_RELAXED, __HIP_MEMORY_SCOPE_AGENT);
                SBAR(); }
            if (s == FSTAGE && tid == 128) { const u32x2 e = LD(u32x2, M_CTL); st8_sc1(ws + WS_FLAG1 + b * 8, (mix(e[0], e[1]) & ~0xFFFFFFFull) | key); }
            if (s == 8 && w >= 2) dma1_sc1(ws + WS_FLAG1 + (w - 2) * 1024, vl, M_GU + (w - 2) * 1024);
            if (s < 15) {
                QRD((s + 1) & 1, s + 1);
                for (int ks = 0; ks < 4; ++ks) { qb = mfma32(af[s & 1][ks], b1[s & 1][ks], qb); qa = mfma32(af[s & 1][ks], b0[s & 1][ks], qa); }
                for (int i = 0; i < 8; ++i) { SGB(0x008, 1); SGB(0x100, 2); }
            } else {
                for (int ks = 0; ks < 4; ++ks) qb = mfma32(af[1][ks], b1[1][ks], qb);
                SBAR();
                for (int ks = 0; ks < 4; ++ks) { qa = mfma32(af[1][ks], b0[1][ks], qa); LD(f32x4, L_QX + w * 4096 + ks * 1024 + vl) = (f32x4){qb[4 * ks], qb[4 * ks + 1], qb[4 * ks + 2], qb[4 * ks + 3]}; }
            }
            SBAR();
        }
        TS(2);
        BAR_LDS();
        csum = LD(float, M_CS + w * 256 + lane * 4);
    } else {
        const int lw = w - 4, koff = tb >> 1;
        { const char* wkv = (isV ? wv : wk) + lw * 16384;
          for (int i = 0; i < 4; ++i) dma4(wkv + i * 4096, vl ^ (h2 << 5), L_WKV + lw * 16384 + i * 4096); }
        unsigned vx[2], vq[4];
        for (int i = 0; i < 2; ++i) { const int row = lw * 16 + i * 8 + (lane >> 3); vx[i] = row * 2048 + (((lane & 7) ^ ((row >> 1) & 7)) << 4); }
        for (int i = 0; i < 4; ++i) vq[i] = (lw * 16 + i * 4 + g4) * 2048 + ((((lane >> 2) & 3) ^ g4) << 6) + ((lane & 3) << 4);
        const char* xb = x + (size_t)tb * 64 * 2048; const char* qb_ = wq + hg * 256;
#define RING_ISSUE(s) do { const int kk_ = ((s) + koff) & 15; \
        asm volatile("s_mov_b32 m0, %8\n\ts_nop 0\n\tglobal_load_lds_dwordx4 %0, %6\n\ts_add_u32 m0, m0, 1024\n\ts_nop 0\n\tglobal_load_lds_dwordx4 %1, %6\n\t" \
                     "s_mov_b32 m0, %9\n\ts_nop 0\n\tglobal_load_lds_dwordx4 %2, %7\n\ts_add_u32 m0, m0, 1024\n\ts_nop 0\n\tglobal_load_lds_dwordx4 %3, %7\n\t" \
                     "s_add_u32 m0, m0, 1024\n\ts_nop 0\n\tglobal_load_lds_dwordx4 %4, %7\n\ts_add_u32 m0, m0, 1024\n\ts_nop 0\n\tglobal_load_lds_dwordx4 %5, %7" \
                     :: "v"(vx[0]), "v"(vx[1]), "v"(vq[0]), "v"(vq[1]), "v"(vq[2]), "v"(vq[3]), "s"(xb + kk_ * 128), "s"(qb_ + (size_t)kk_ * 131072), \
                        "s"(slot_of(s) * SLOT + lw * 2048), "s"(slot_of(s) * SLOT + 8192 + lw * 4096) : "memory", "scc"); } while (0)
        TS(32); RING_ISSUE(0); TS(8);
        BAR_LDS();
        BAR_VM(6); TS(33);
        RING_ISSUE(1); RING_ISSUE(2); TS(52);
        TS(35);
        BAR_LDS(); TS(1); RING_ISSUE(3); RING_ISSUE(4); TS(36);
        BAR_VM(12); TS(37); RING_ISSUE(5);
        BAR_LDS(); RING_ISSUE(6); TS(38);
        BAR_VM(12); TS(39); RING_ISSUE(7); RING_ISSUE(8); TS(40);
        BAR_VM(12); TS(41); RING_ISSUE(9); RING_ISSUE(10); TS(42);
        BAR_VM(12); TS(43); RING_ISSUE(11); RING_ISSUE(12); TS(44);
        BAR_VM(12); TS(45);
        RING_ISSUE(13); RING_ISSUE(14); TS(46);
        const u32x2 e = LD(u32x2, M_CTL); const u64 tag36 = mix(e[0], e[1]) & ~0xFFFFFFFull;
        BAR_VM(12); TS(47); RING_ISSUE(15);
        u64 f[4]; { const u32x4 f0 = LD(u32x4, M_GU + vl), f1 = LD(u32x4, M_GU + 1024 + vl); f[0] = ((u64)f0[1] << 32) | f0[0]; f[1] = ((u64)f0[3] << 32) | f0[2]; f[2] = ((u64)f1[1] << 32) | f1[0]; f[3] = ((u64)f1[3] << 32) | f1[2]; }
#define FLAGS_OK() __all((((f[0] ^ tag36) | (f[1] ^ tag36) | (f[2] ^ tag36) | (f[3] ^ tag36)) >> 28) == 0)
        const bool early = FLAGS_OK();
        BAR_VM(6); TS(49);
        if (!early) {
            BAR_VM(0);
#pragma unroll 1
            for (unsigned spins = 0; spins < SPIN_CAP; ++spins) {
                for (int i = 0; i < 4; ++i) f[i] = ld8_sc1(ws + WS_FLAG1 + (i * 64 + lane) * 8);
                if (FLAGS_OK()) break;
                __builtin_amdgcn_s_sleep(2);
            }
            PIN(f[0]); PIN(f[1]); PIN(f[2]); PIN(f[3]);
        }
        TS(50);
        for (int i = 0; i < 4; ++i) dma1_sc1(ws + WS_CS + hg * 16384 + i * 4096 + lw * 128, (lane >> 3) * 512 + (lane & 7) * 16, L_CSP + lw * 4096 + i * 1024);
        if (lw < 2) { dma4_sc1(kv + ((c0 * 8 + 62 + lw) & 63) * 4096, vl, L_PRO + lw * 4096); if (early) BAR_VM(8); }
        else if (lw == 2) { dma1_sc1(ws + WS_STATS + tb * 512, l31 * 16, M_ST64); if (early) BAR_VM(5); }
        else {
            unsigned key = rowmax16(max(max((unsigned)f[0] & 0xFFFFFFFu, (unsigned)f[1] & 0xFFFFFFFu), max((unsigned)f[2] & 0xFFFFFFFu, (unsigned)f[3] & 0xFFFFFFFu)));
            key = max(max((unsigned)__builtin_amdgcn_readlane(key, 0), (unsigned)__builtin_amdgcn_readlane(key, 16)), max((unsigned)__builtin_amdgcn_readlane(key, 32), (unsigned)__builtin_amdgcn_readlane(key, 48)));
            const int ix = 2047 - (key & 2047);
            dma1(x + (size_t)ix * 2048 + (mi & 3) * 512, l31 * 16, M_XSEL);
            asm volatile("s_mov_b32 m0, %2\n\ts_nop 0\n\tglobal_load_lds_dword %0, %1 sc1" :: "v"((lane & 1) * 4), "s"(ws + WS_STATS + ix * 8), "s"(M_STX) : "memory");
            if (early) BAR_VM(6);
            if (lane == 0) { LD(unsigned, M_CTL + 8) = key; if (b == 0) st8_sc1(ws + WS_EPOCH, (((u64)e[1] << 32) | e[0]) + 1); }
        }
        TS(51);
        dma4_sc1(kv + c0 * 32768 + lw * 8192, vl, KA + lw * 8192); dma4_sc1(kv + c0 * 32768 + lw * 8192 + 4096, vl, KA + lw * 8192 + 4096);
        SETUP();
        WAIT_VM(8);
        { float p[16]; for (int i = 0; i < 16; ++i) p[i] = LD(float, L_CSP + lw * 4096 + (h2 * 16 + i) * 128 + l31 * 4);
          csum = xsum32((((p[0] + p[1]) + (p[2] + p[3])) + ((p[4] + p[5]) + (p[6] + p[7]))) + (((p[8] + p[9]) + (p[10] + p[11])) + ((p[12] + p[13]) + (p[14] + p[15]))));
          LD(float, M_CS + lw * 256 + lane * 4) = csum; }
        TS(2);
        __builtin_amdgcn_s_setprio(3);
        BAR_LDS();
        for (int q = 0; q < 4; ++q) { const f32x4 t = LD(f32x4, L_QX + hd * 4096 + q * 1024 + vl); for (int i = 0; i < 4; ++i) qa[4 * q + i] = t[i]; }
    }
    TS(3);
    u32x8 Q, V; u32x16 PK; f32x16 SA, SB, NEG;
    const u32x4 ctl = LD(u32x4, M_CTL);
    {
        const f32x2 st = LD(f32x2, M_ST64 + (tbk * 32 + l31) * 8);
        const u32x4 k00 = LD(u32x4, L_PRO + vl), k01 = LD(u32x4, L_PRO + 1024 + vl), k10 = LD(u32x4, L_PRO + 4096 + vl), k11 = LD(u32x4, L_PRO + 5120 + vl), v00 = LD(u32x4, L_PRO + 6144 + vl);
        const float c1 = rb(csum), c2 = rb(csum - c1), c3 = csum - c1 - c2;
        const u32x4 ca = {h2 ? 0u : pk2(c1, c2), h2 ? 0u : pk2(c3, 0.f), 0u, 0u}, cb = {pk2(-st[0], -st[0]), pk2(-st[0], 0.f), 0u, 0u};
        qa = mfma32(ca, cb, qa);
        const float qs = st[1] * (0.17677669529663687f * 1.4426950408889634f);
        for (int i = 0; i < 8; ++i) Q[i] = pk2(qa[2 * i] * qs, qa[2 * i + 1] * qs);
        f32x16 s0 = mfma32(k00, lo4(Q), O); s0 = mfma32(k01, hi4(Q), s0);
        for (int i = 0; i < 4; ++i) { PK[i] = PZ[i]; PK[4 + i] = PZ[4 + i]; PK[8 + i] = k10[i]; PK[12 + i] = k11[i]; V[i] = v00[i]; V[4 + i] = k00[i]; }
        float m = fmaxf(fmaxf(fmaxf(fmaxf(s0[0], s0[1]), s0[2]), fmaxf(fmaxf(s0[3], s0[4]), s0[5])), fmaxf(fmaxf(fmaxf(s0[6], s0[7]), s0[8]), fmaxf(fmaxf(s0[9], s0[10]), s0[11])));
        m = xmax32(fmaxf(fmaxf(m, fmaxf(s0[12], s0[13])), fmaxf(s0[14], s0[15])));
        NEG = __builtin_amdgcn_mfma_f32_32x32x2f32(h2 ? 0.f : 1.f, -m, O, 0, 0, 0);
        for (int i = 0; i < 16; ++i) SA[i] = s0[i] - m;
        PIN(NEG); PIN(SA);
    }
    TS(4);
    const u64 seed = mix(__builtin_amdgcn_readfirstlane(ctl[0]), __builtin_amdgcn_readfirstlane(ctl[1]));
    const unsigned tag32 = (unsigned)seed ^ 0x5bd1e995u, gkey = __builtin_amdgcn_readfirstlane(ctl[2]);
    const int idx = 2047 - (gkey & 2047), cbk = hd, kh = tbk;
    const bool moe = r < 28;
    u64 swo = (u64)(wo + (size_t)kh * 512 * 2048 + hg * 256 + cbk * 64) - 0x8000, smo;
    u32x8 wf[16];
#define SEAM(c) const unsigned ad = (((c) & 1) ? KB : KA) + vl, mk = (((c) & 1) ? KA : KB) + w * 4096; const char* sk = kv + ((c0 + (c) + 1) & 7) * 32768 + w * 4096
#define MROW() (16 * (g4 >> 1) + 4 * (g4 & 1) + (l15 >> 2))
#define MSWZ() (2 * (g4 & 1) + (l15 >> 3))
    { BAR_VM(0); SEAM(0); const unsigned vm = vm0, mm = R2 + w * 4096; smo = (u64)(wg + (size_t)((mi & 3) * 256 + w * 32) * 7168 + (mi >> 2) * 128); TS(10);
      asm volatile(BODY_TXT(T0(WOA(0), NO, NO), T1(WOA(1024), NO, NO), T2(KV0, KVN(1024), NO), T3(KVN(2048), KVN(3072), NO), T4(MOE(0, 0), MOE(1024, 57344), NO), T5(MOE(2048, 57344), MOE(3072, 57344), NO),
                            T6(NO, NO, NO), T7(VM(8) TRA(0, 0) TRA(1, 1024), NO, NO))
                   : BODY_OUT, "=&{v[128:135]}"(wf[0]) : BODY_IN : "memory", "scc"); TS(18); }
    { BAR_VM(4); SEAM(1); const unsigned vm = vm0, mm = mb; smo = (u64)(wu + (size_t)((mi & 3) * 256 + w * 32) * 7168 + (mi >> 2) * 128);
      TS(11);
      asm volatile(BODY_TXT(T0(WOA(0), NO, NO), T1(WOA(1024), NO, NO), T2(KV0, KVN(1024), NO), T3(KVN(2048), KVN(3072), NO), T4(MOE(0, 0), MOE(1024, 57344), NO), T5(MOE(2048, 57344), MOE(3072, 57344), NO),
                            T6(NO, NO, NO), T7(VM(8) TRA(2, 0) TRA(3, 1024), NO, NO))
                   : BODY_OUT, "=&{v[136:143]}"(wf[1]) : BODY_IN : "memory", "scc"); TS(19); }
    { if (w == 0) { RELANE(); const f32x2 sx = LD(f32x2, M_STX); const u32x2 v = LD(u32x2, M_XSEL + lane * 8); LD(u32x2, M_XSEL + lane * 8) = (u32x2){ln2(v[0], sx[0], sx[1]), ln2(v[1], sx[0], sx[1])}; }
      BAR_VM(4); SEAM(2); const unsigned vm = 0, mm = 0; TS(12);
      asm volatile(BODY_TXT(T0(WOA(0), NO, NO), T1(WOA(1024), NO, NO), T2(KV0, NO, NO), T3(KVN(1024), NO, NO), T4(KVN(2048), NO, NO), T5(KVN(3072), NO, NO), T6(NO, NO, NO), T7(VM(4) TRA(4, 0) TRA(5, 1024), NO, NO))
                   : BODY_OUT, "=&{v[144:151]}"(wf[2]) : BODY_IN : "memory", "scc"); TS(20); }
    { RELANE(); const unsigned vm = R2 + w * 4096 + MROW() * 128 + MSWZ() * 32 + (lane & 3) * 8, mm = 0;
      const unsigned xa = M_XSEL + w * 64 + (g4 >> 1) * 32 + (g4 & 1) * 8, gw = M_GU + w * 256 + lane * 4;
      BAR_VM(0); SEAM(3); TS(13);
#define GM(d, a) M16("v[" #d ":" #d "+3], v[176:179], v[" #a ":" #a "+3], 0")
      asm volatile(BODY_TXT(T0(WOA(0) "v_xor_b32 v244, 32, v19\n v_xor_b32 v245, 64, v19\n v_xor_b32 v246, 0x60, v19\n v_subrev_u32 v247, 0x10000, v19\n" B64(176, "v15", 0) B64(178, "v15", 16) TR2(180, "v19", 0, 1024),
                               WOA(1024) TR2(184, "v244", 0, 1024) TR2(188, "v245", 0, 1024) "v_subrev_u32 v244, 0x10000, v244\n v_subrev_u32 v245, 0x10000, v245\n", NO),
                            T1(TR2(192, "v246", 0, 1024) TR2(196, "v247", 0, 1024) "v_subrev_u32 v246, 0x10000, v246\n", TR2(200, "v244", 0, 1024) TR2(204, "v245", 0, 1024), GM(212, 180) GM(216, 184)),
                            T2(TR2(208, "v246", 0, 1024) KV0, KVN(1024), GM(220, 188) GM(224, 192)), T3(KVN(2048) KVN(3072), WOB(0), GM(228, 196) GM(232, 200)), T4(WOB(1024), WOB(2048), GM(236, 204) GM(240, 208)),
                            T5(WOB(3072), NO, NO),
                            T6("s_mov_b32 vcc_lo, 0xffff0000\n s_mov_b32 vcc_hi, 0xffff0000\n v_cndmask_b32 v212, v212, v216, vcc\n v_cndmask_b32 v220, v220, v224, vcc\n v_cndmask_b32 v228, v228, v232, vcc\n"
                               "v_cndmask_b32 v236, v236, v240, vcc\n s_mov_b32 vcc_lo, 0\n s_mov_b32 vcc_hi, -1\n v_cndmask_b32 v212, v212, v220, vcc\n v_cndmask_b32 v228, v228, v236, vcc\n",
                               "ds_write_b32 v16, v212\n ds_write_b32 v16, v228 offset:2048\n" VM(8) TRA(6, 0) TRA(7, 1024), NO),
                            T7(VM(0) TRB(8, 0) TRB(9, 1024), TRB(10, 2048) TRB(11, 3072), NO))
                   : BODY_OUT, "=&{v[152:159]}"(wf[3]), "=&{v[160:167]}"(wf[4]), "=&{v[168:175]}"(wf[5]) : BODY_IN, "{v15}"(xa), "{v16}"(gw)
                   : "memory", "scc", C8(176,177,178,179,180,181,182,183), C8(184,185,186,187,188,189,190,191), C8(192,193,194,195,196,197,198,199), C8(200,201,202,203,204,205,206,207),
                     C8(208,209,210,211,212,213,214,215), C8(216,217,218,219,220,221,222,223), C8(224,225,226,227,228,229,230,231), C8(232,233,234,235,236,237,238,239), C8(240,241,242,243,244,245,246,247), "vcc"); TS(21); }
    const unsigned ye = moe ? 0xFFFFu : 0u;
    { RELANE(); const unsigned vm = (lane >> 3) * 2048 + (((lane & 7) ^ (g4 << 1)) << 4), mm = R2 + w * 2048; smo = (u64)(wd + (size_t)(rm * 128 + w * 16) * 2048 + xcd * 256);
      const unsigned ga = M_GU + (w >> 2) * 2048 + ((w & 3) * 16 + l15) * 4, go = l15 * 64; const char* gs = ws + WS_GU + ((mi >> 2) * 64 + (w & 3) * 16) * 64 + (w >> 2) * 32 + (mi & 3) * 8;
      BAR_VM(0); SEAM(4); TS(14);
#define R2S(d, a, b) "ds_read2st64_b32 v[" #d ":" #d "+1], v15 offset0:" #a " offset1:" #b "\n"
      asm volatile(BODY_TXT(T0(WOA(0) R2S(224, 0, 1) R2S(226, 2, 3), WOA(1024) R2S(228, 4, 5) R2S(230, 6, 7), NO),
                            T1(WOB(0), WOB(1024) "v_add_f32 v224, v224, v225\n v_add_f32 v226, v226, v227\n v_add_f32 v228, v228, v229\n v_add_f32 v230, v230, v231\n v_add_f32 v224, v224, v226\n v_add_f32 v228, v228, v230\n"
                               "v_mov_b32 v225, %[tg]\n v_add_f32 v224, v224, v228\n", "s_mov_b32 exec_lo, %[ye]\n s_mov_b32 exec_hi, 0\n global_store_dwordx2 v16, v[224:225], %[gs] sc1\n s_mov_b64 exec, -1\n"),
                            T2(WOB(2048), WOB(3072), NO), T3(KV0, KVN(1024), NO), T4(KVN(2048), KVN(3072), NO), T5(MOE(0, 0), MOE(16384, 128), NO),
                            T6(MOE(1024, 16256), MOE(17408, 128) VM(8) TRA(12, 0) TRA(13, 1024), NO), T7(TRB(14, 0) TRB(15, 1024), TRB(16, 2048) TRB(17, 3072), NO))
                   : BODY_OUT, "=&{v[176:183]}"(wf[6]), "=&{v[184:191]}"(wf[7]), "=&{v[192:199]}"(wf[8]) : BODY_IN, "{v15}"(ga), "{v16}"(go), [ye] "s"(ye), [tg] "s"(tag32), [gs] "s"(gs)
                   : "memory", "scc", C8(224,225,226,227,228,229,230,231)); TS(22); }
    u64 bad;
    { RELANE(); const unsigned vm = 0, mm = M_GR + w * 1024, ga = M_GR + w * 1024 + lane * 16, hw = M_H + w * 32 + (lane >> 2) * 2; smo = (u64)(ws + WS_GU + (size_t)(rm * 128 + w * 16) * 64);
      BAR_VM(4); SEAM(5); TS(15);
      asm volatile(BODY_TXT(T0(WOA(0), WOA(1024), NO), T1(WOB(0), WOB(1024), NO), T2(WOB(2048), WOB(3072), GRD), T3(KV0, KVN(1024), NO), T4(KVN(2048), KVN(3072), NO), T5(NO, NO, NO),
                            T6(VM(4) "ds_read_b128 v[224:227], v15\n" TRA(18, 0) TRA(19, 1024), TRB(20, 0) TRB(21, 1024), NO),
                            T7("v_add_f32 v224, v224, v226\n v_cmp_ne_u32 vcc, %[tg], v225\n v_cmp_ne_u32 s[74:75], %[tg], v227\n v_add_f32_dpp v224, v224, v224 quad_perm:[1,0,3,2] row_mask:0xf bank_mask:0xf\n"
                               "v_cvt_pk_bf16_f32 v224, v224, v224\n v_lshlrev_b32 v224, 16, v224\n v_mul_f32 v226, 0xbfb8aa3b, v224\n v_exp_f32 v226, v226\n"
                               "v_mov_b32_dpp v225, v224 quad_perm:[2,3,0,1] row_mask:0xf bank_mask:0xf\n s_or_b64 s[74:75], vcc, s[74:75]\n" TRB(22, 2048),
                               "v_add_f32 v226, 1.0, v226\n v_rcp_f32 v226, v226\n" TRB(23, 3072),
                               "v_mul_f32 v226, v224, v226\n v_cvt_pk_bf16_f32 v226, v226, v226\n v_lshlrev_b32 v226, 16, v226\n v_mul_f32 v226, v226, v225\n v_cvt_pk_bf16_f32 v226, v226, v226\n"
                               "s_mov_b32 exec_lo, 0x11111111\n s_mov_b32 exec_hi, 0x11111111\n ds_write_b16 v16, v226\n s_mov_b64 exec, -1\n"))
                   : BODY_OUT, "=&{v[200:207]}"(wf[9]), "=&{v[208:215]}"(wf[10]), "=&{v[216:223]}"(wf[11]), "=&{s[74:75]}"(bad) : BODY_IN, "{v15}"(ga), "{v16}"(hw), [tg] "s"(tag32)
                   : "memory", "scc", "vcc", "v224", "v225", "v226", "v227"); TS(23); }
    { RELANE();
      if (__builtin_expect(moe && bad != 0, 0)) {
          u32x4 gq;
#pragma unroll 1
          for (unsigned spins = 0; spins < SPIN_CAP; ++spins) {
              const char* p = ws + WS_GU + (size_t)(r * 128 + w * 16) * 64 + lane * 16; const u64 v0 = ld8_sc1(p), v1 = ld8_sc1(p + 8); gq = (u32x4){(unsigned)v0, (unsigned)(v0 >> 32), (unsigned)v1, (unsigned)(v1 >> 32)};
              if (__all(gq[1] == tag32 && gq[3] == tag32)) break;
              __builtin_amdgcn_s_sleep(2); }
          float sm = __uint_as_float(gq[0]) + __uint_as_float(gq[2]);
          sm = rb(sm + __uint_as_float(__builtin_amdgcn_update_dpp(0, (int)__float_as_uint(sm), 0xB1, 0xf, 0xf, false)));
          const float uu = __uint_as_float(__builtin_amdgcn_update_dpp(0, (int)__float_as_uint(sm), 0x4E, 0xf, 0xf, false));
          if (!(lane & 3)) LD(unsigned short, M_H + w * 32 + (lane >> 2) * 2) = (unsigned short)pk2(rb(silu(sm)) * uu, 0.f);
      }
      const unsigned vm = R2 + (w >> 2) * 16384 + MROW() * 128 + ((MSWZ() ^ (w & 3)) * 32) + (lane & 3) * 8, mm = tag32, ha = M_H + (g4 >> 1) * 32 + (g4 & 1) * 8, yo = lane * 8;
      smo = (u64)(ws + WS_YG + (rm * 1024 + xcd * 128 + w * 16) * 8);
      BAR_VM(0); SEAM(6); TS(16);
#define DM(h, d, c) M16("v[248:251], v[" #h ":" #h "+3], v[" #d ":" #d "+3], " c)
      asm volatile(BODY_TXT(T0(WOA(0) B64(224, "v15", 0) B64(226, "v15", 16) TR2(228, "v19", 0, 1024), WOA(1024) B64(232, "v15", 64) B64(234, "v15", 80) TR2(236, "v19", 4096, 1024), NO),
                            T1(WOB(0), WOB(1024), DM(224, 228, "0") DM(232, 236, "v[248:251]")),
                            T2(WOB(2048) B64(224, "v15", 128) B64(226, "v15", 144) TR2(228, "v19", 8192, 1024), WOB(3072) B64(232, "v15", 192) B64(234, "v15", 208) TR2(236, "v19", 12288, 1024), NO),
                            T3(KV0, KVN(1024), DM(224, 228, "v[248:251]") DM(232, 236, "v[248:251]")), T4(KVN(2048), KVN(3072), NO),
                            T5("v_mov_b32 v249, %[mm]\n", "s_mov_b32 exec_lo, %[ye]\n s_mov_b32 exec_hi, 0\n global_store_dwordx2 v16, v[248:249], s[72:73] sc1\n s_mov_b64 exec, -1\n", NO),
                            T6(NO, VM(5) TRA(24, 0) TRA(25, 1024), NO), T7(TRB(26, 0) TRB(27, 1024), TRB(28, 2048) TRB(29, 3072), NO))
                   : BODY_OUT, "=&{v[224:231]}"(wf[12]), "=&{v[232:239]}"(wf[13]), "=&{v[240:247]}"(wf[14]) : BODY_IN, "{v15}"(ha), "{v16}"(yo), [ye] "s"(ye)
                   : "memory", "scc", "v248", "v249", "v250", "v251"); TS(24); }
    { BAR_VM(1); SEAM(7);
      const unsigned vm = vw ^ ((vw >> 9) & 48), mm = mb; smo = (u64)(x + (size_t)(tb * 64 + kh * 32) * 2048 + hg * 256 + cbk * 64); TS(17);
      asm volatile(BODY_TXT(T0(WOA(0), NO, NO), T1(WOA(1024), NO, NO), T2(MOE(0, 0), NO, NO), T3(MOE(1024, 32768), NO, NO), T4(NO, NO, NO), T5(NO, NO, NO), T6(NO, NO, NO), T7(VM(0) TRA(30, 0) TRA(31, 1024), NO, NO))
                   : BODY_OUT, "=&{v[248:255]}"(wf[15]) : BODY_IN : "memory", "scc"); TS(25); }
    {
    RELANE(); const unsigned vl = lane * 16;
    u32x4 o0, o1;
    const bool routed = tb == (idx >> 6) && kh == ((idx >> 5) & 1);
    u64 yg[14];
    if (routed) for (int i = 0; i < 14; ++i) yg[i] = ld8_sc1(ws + WS_YG + ((h2 * 14 + i) * 1024 + hg * 128 + cbk * 32 + l31) * 8);
    u32x2 xr[4]; for (int q = 0; q < 4; ++q) xr[q] = LD(u32x2, mb + l31 * 64 + ((q ^ ((l31 >> 2) & 3)) * 16) + h2 * 8);
    const f32x2 stt = LD(f32x2, M_ST64 + (tbk * 32 + l31) * 8);
    {
        const u32x8 P = __builtin_shufflevector(PK, PK, 0, 1, 2, 3, 4, 5, 6, 7); f32x4 L = __builtin_bit_cast(f32x4, lo4(LS));
        asm volatile("s_nop 1\n\tv_smfmac_f32_16x16x64_bf16 %0, %1, %2, %3\n\ts_nop 11" : "+v"(L) : "v"(hi4(LS)), "v"(P), "v"(IDX));
        O = mfma32(hi4(V), hi4(P), O);
        const auto l16 = __builtin_amdgcn_permlane16_swap(__float_as_uint(L[0] + L[1]), __float_as_uint(L[2] + L[3]), false, false);
        float lsum = __uint_as_float(__builtin_amdgcn_permlane32_swap(l16[0], l16[0], false, false)[0]);
        const float om = fmaxf(fmaxf(fmaxf(fmaxf(fabsf(O[0]), fabsf(O[1])), fabsf(O[2])), fmaxf(fmaxf(fabsf(O[3]), fabsf(O[4])), fabsf(O[5]))), fmaxf(fmaxf(fmaxf(fabsf(O[6]), fabsf(O[7])), fabsf(O[8])),
                         fmaxf(fmaxf(fmaxf(fabsf(O[9]), fabsf(O[10])), fabsf(O[11])), fmaxf(fmaxf(fmaxf(fabsf(O[12]), fabsf(O[13])), fabsf(O[14])), fabsf(O[15])))));
        if (__builtin_expect(__any(!(lsum <= 1.8446744e19f) || !(om <= 3.0e38f)), 0)) {
            float mr = -INFINITY; lsum = 0.f; for (int i = 0; i < 16; ++i) O[i] = 0.f;
#pragma unroll 1
            for (int t = 0; t < 64; ++t) {
                u32x4 k0, k1, v0, v1;
                asm volatile("global_load_dwordx4 %0, %4, %5 sc1\n\tglobal_load_dwordx4 %1, %4, %5 offset:1024 sc1\n\tglobal_load_dwordx4 %2, %4, %6 offset:2048 sc1\n\tglobal_load_dwordx4 %3, %4, %7 offset:3072 sc1\n\ts_waitcnt vmcnt(0)"
                             : "=&v"(k0), "=&v"(k1), "=&v"(v0), "=&v"(v1) : "v"(vl), "s"(kv + ((t + 62) & 63) * 4096), "s"(kv + ((t + 63) & 63) * 4096), "s"(kv + t * 4096) : "memory");
                f32x16 z; for (int i = 0; i < 16; ++i) z[i] = 0.f;
                f32x16 s = mfma32(k0, lo4(Q), z); s = mfma32(k1, hi4(Q), s);
                float m = s[0]; for (int i = 1; i < 16; ++i) m = fmaxf(m, s[i]);
                m = fmaxf(mr, xmax32(m)); const float al = __builtin_amdgcn_exp2f(mr - m); mr = m;
                u32x8 pp; float ps = 0.f;
                for (int i = 0; i < 8; ++i) { pp[i] = pk2(__builtin_amdgcn_exp2f(s[2 * i] - m), __builtin_amdgcn_exp2f(s[2 * i + 1] - m)); ps += bf_lo(pp[i]) + bf_hi(pp[i]); }
                lsum = lsum * al + xsum32(ps); for (int i = 0; i < 16; ++i) O[i] *= al;
                O = mfma32(v0, lo4(pp), O); O = mfma32(v1, hi4(pp), O);
            }
        }
        const float rl = __builtin_amdgcn_rcpf(lsum);
        for (int i = 0; i < 4; ++i) { o0[i] = pk2(O[2 * i] * rl, O[2 * i + 1] * rl); o1[i] = pk2(O[8 + 2 * i] * rl, O[8 + 2 * i + 1] * rl); }
        asm volatile("global_store_dwordx4 %0, %1, %3 sc1\n\tglobal_store_dwordx4 %0, %2, %3 offset:2048 sc1\n\ts_nop 1"
                     :: "v"(vl), "v"(o0), "v"(o1), "s"(ws + WS_O + (size_t)(tb * 8 + hg) * 16384 + hd * 4096 + tbk * 1024) : "memory");
    }
    TS(26);
    BAR_LDS();
    TS(27);
    const unsigned pv = w * PRIV;
    LD(u32x4, hg * PRIV + hd * 4096 + tbk * 1024 + vl) = o0; LD(u32x4, hg * PRIV + hd * 4096 + 2048 + tbk * 1024 + vl) = o1;
    unsigned xn2[8], g2[8];
    for (int i = 0; i < 8; ++i) { xn2[i] = ln2(xr[i >> 1][i & 1], stt[0], stt[1]); PIN(xn2[i]); }
    WAIT_VM(0);
    if (lane == 0) st8_sc1(ws + WS_FLAG4 + b * 128 + w * 8, seed);
    TS(28);
    if (routed) {
#pragma unroll 1
        for (unsigned spins = 0; spins < SPIN_CAP; ++spins) {
            bool ok = true; for (int i = 0; i < 14; ++i) ok &= (unsigned)(yg[i] >> 32) == tag32;
            if (__all(ok)) break;
            __builtin_amdgcn_s_sleep(2);
            for (int i = 0; i < 14; ++i) yg[i] = ld8_sc1(ws + WS_YG + ((h2 * 14 + i) * 1024 + hg * 128 + cbk * 32 + l31) * 8);
        }
        float acc = 0.f; for (int i = 0; i < 14; ++i) acc += __uint_as_float((unsigned)yg[i]);
        const unsigned o16 = (gkey >> 11) & 0xffffu, rbits = (o16 & 0x8000u) ? (o16 & 0x7fffu) : (~o16 & 0xffffu);
        const float rsel = rb(__builtin_amdgcn_rcpf(1.f + __builtin_amdgcn_exp2f(-1.44269504089f * bf_lo(rbits))));
        LD(float, pv + 16384 + lane * 4) = rb(rb(xsum32(acc)) * rsel);
        for (int q = 0; q < 4; ++q) { const f32x4 y = LD(f32x4, pv + 16384 + q * 32 + h2 * 16);
            for (int i = 0; i < 2; ++i) { const unsigned t = pk2(bf_lo(xn2[2 * q + i]) + y[2 * i], bf_hi(xn2[2 * q + i]) + y[2 * i + 1]); xn2[2 * q + i] = l31 == (idx & 31) ? t : xn2[2 * q + i]; } }
    }
    for (int i = 0; i < 8; ++i) { g2[i] = pk2(silu(bf_lo(xn2[i])), silu(bf_hi(xn2[i]))); PIN(g2[i]); }
    TS(31);
    if (w != hg) {
        const int pb = (((w & 3) * 8 + (tb & 7)) << 3) | ((tb >> 3) * 2 + (w >> 2));
#pragma unroll 1
        for (unsigned spins = 0; spins < SPIN_CAP; ++spins) { if (__all(ld8_sc1(ws + WS_FLAG4 + pb * 128 + (lane & 7) * 8) == seed)) break; __builtin_amdgcn_s_sleep(1); }
        TS(29);
        const char* src = ws + WS_O + (size_t)(tb * 8 + w) * 16384;
        for (int i = 0; i < 4; ++i) dma4_sc1(src + i * 4096, vl, pv + i * 4096);
    }
    BAR_VM(12);
    TS(6);
    f32x16 ao, at; for (int i = 0; i < 16; ++i) { ao[i] = 0.f; at[i] = 0.f; }
    {
        const unsigned oa = kh * 4 * PRIV + vl + kh * 1024, ob = oa ^ 1024;
        u32x4 fr[2][8]; f32x4 pt[4];
#define OAD(g, i) ((g) < 4 ? (((i) & 1) ? oa : ob) + (((g) & 1) * 2 + ((i) >> 2)) * PRIV + (((g) >> 1) * 2 + (((i) >> 1) & 1)) * 2048 : ((g) < 6 ? ob : oa) + ((i) >> 1) * PRIV + (4 + ((g) & 1) * 2 + ((i) & 1)) * 2048)
#define OWJ(g, i) ((g) < 4 ? (((g) & 1) * 2 + ((i) >> 2)) * 8 + ((g) >> 1) * 2 + (((i) >> 1) & 1) : ((i) >> 1) * 8 + 4 + ((g) & 1) * 2 + ((i) & 1))
#define ORD(p, g) do { for (int i = 0; i < 8; ++i) fr[p][i] = LD(u32x4, OAD(g, i)); } while (0)
#define WFJ(j) (((j) & 1) ? hi4(wf[(j) >> 1]) : lo4(wf[(j) >> 1]))
        __builtin_amdgcn_s_setprio(1);
        ORD(0, 0); SBAR();
#pragma unroll
        for (int g = 0; g < 8; ++g) {
            if (g == 1) asm volatile("s_waitcnt vmcnt(8)\n\ts_barrier" ::: "memory");
            if (g == 3) asm volatile("s_waitcnt vmcnt(4)\n\ts_barrier" ::: "memory");
            if (g == 4) asm volatile("s_waitcnt vmcnt(0)\n\ts_barrier" ::: "memory");
            if (g < 7) ORD((g + 1) & 1, g + 1);
            for (int i = 0; i < 8; ++i) {
                if (g < 4 ? (i & 1) : g >= 6) ao = mfma32(WFJ(OWJ(g, i)), fr[g & 1][i], ao); else at = mfma32(WFJ(OWJ(g, i)), fr[g & 1][i], at);
                if (g == 6 && i == 1) { SBAR(); for (int q = 0; q < 4; ++q) LD(f32x4, pv + 16384 + q * 1024 + vl) = (f32x4){at[4 * q], at[4 * q + 1], at[4 * q + 2], at[4 * q + 3]}; __builtin_amdgcn_s_setprio(0); SBAR(); }
                if (g == 7 && i == 3) { SBAR(); BAR_LDS(); for (int q = 0; q < 4; ++q) pt[q] = LD(f32x4, (w ^ 4) * PRIV + 16384 + q * 1024 + vl); SBAR(); } }
            if (g < 6) for (int i = 0; i < 8; ++i) { SGB(0x008, 1); SGB(0x100, 1); }
            SBAR();
        }
        TS(30);
        char* op = out + (size_t)(tb * 64 + kh * 32 + l31) * 2048 + hg * 256 + cbk * 64 + h2 * 8;
        for (int q = 0; q < 4; ++q) {
            u32x2 o;
            for (int i = 0; i < 2; ++i) {
                const unsigned s2 = pk2(ao[4 * q + 2 * i] + pt[q][2 * i], ao[4 * q + 2 * i + 1] + pt[q][2 * i + 1]), gg = g2[2 * q + i];
                const unsigned p2 = pk2(bf_lo(s2) * bf_lo(gg), bf_hi(s2) * bf_hi(gg));
                o[i] = pk2(bf_lo(xr[q][i]) + bf_lo(p2), bf_hi(xr[q][i]) + bf_hi(p2));
            }
            *(u32x2*)(op + q * 16) = o;
        }
    }
    }
    TS(7);
}

extern "C" void kernel_launch(void* const* d_in, const int* in_sizes, int n_in, void* d_out, int out_size, void* d_ws, size_t ws_size, hipStream_t stream) {
    (void)hipFuncSetAttribute((const void*)fused_block, hipFuncAttributeMaxDynamicSharedMemorySize, LDS_BYTES);
    fused_block<<<256, 512, LDS_BYTES, stream>>>((const char*)d_in[0], (const char*)d_in[1], (const char*)d_in[2], (const char*)d_in[3], (const char*)d_in[4],
                                                 (const char*)d_in[6], (const char*)d_in[7], (const char*)d_in[8], (const char*)d_in[9], (char*)d_out, (char*)d_ws);
}
```

```cpp
#define PRIOV 1
#define FSTAGE 4
#include <hip/hip_runtime.h>
#include <hip/hip_bf16.h>
#include <stdint.h>
#include <type_traits>

typedef unsigned long long u64;
typedef float f32x16 __attribute__((ext_vector_type(16)));
typedef float f32x4 __attribute__((ext_vector_type(4)));
typedef float f32x2 __attribute__((ext_vector_type(2)));
typedef unsigned u32x2 __attribute__((ext_vector_type(2)));
typedef unsigned u32x4 __attribute__((ext_vector_type(4)));
typedef unsigned u32x8 __attribute__((ext_vector_type(8)));
typedef unsigned u32x16 __attribute__((ext_vector_type(16)));
typedef __bf16 bf16x8 __attribute__((ext_vector_type(8)));
typedef __bf16 bf16x2 __attribute__((ext_vector_type(2)));
typedef short s16x4 __attribute__((ext_vector_type(4)));
#define LAS __attribute__((address_space(3)))
#define GAS __attribute__((address_space(1)))
#define DI __device__ __forceinline__
#define LD(T, a) (*(LAS T*)(unsigned)(a))

constexpr unsigned WS_EPOCH = 0, WS_FLAG1 = 4096, WS_STATS = 8192, WS_FLAG4 = 65536, WS_KV = 131072, WS_YG = 393216, WS_GU = 622592, WS_CS = 851968, WS_O = 1u << 20;
constexpr unsigned WS_DBG = 6u << 20;
constexpr unsigned SLOT = 24576, L_X16 = 49152, L_WR = 81920, L_SP = 86016, L_FIN = 87040, L_PA = 73728, L_PC = 88064, L_WKV = 98304, L_QX = 0, L_PRO = 16384, L_CSP = 73728, L_MISC = 155648, LDS_BYTES = 163840;
constexpr unsigned M_CTL = L_MISC, M_ST64 = L_MISC + 256, M_STX = L_MISC + 1280, M_CS = L_MISC + 1536, M_XSEL = L_MISC + 2560, M_H = L_MISC + 3584, M_GU = L_MISC + 4096;
constexpr unsigned KA = 122880, KB = 32768, R2 = 65536, R3 = 0, R4 = 98304, M_GR = 114688, PRIV = 20480;
constexpr int slot_of(int s) { return s % 6 < 3 ? s % 6 : s % 6 == 5 ? 3 : s % 6 + 1; }
constexpr unsigned SPIN_CAP = 1u << 20;
constexpr int CODE_SPAN = 38912;

DI float bf_lo(unsigned v) { return __uint_as_float(v << 16); }
DI float bf_hi(unsigned v) { return __uint_as_float(v & 0xffff0000u); }
DI unsigned pk2(float lo, float hi) { f32x2 v = {lo, hi}; return __builtin_bit_cast(unsigned, __builtin_convertvector(v, bf16x2)); }
DI float rb(float f) { return __uint_as_float((unsigned)__builtin_bit_cast(unsigned short, (__bf16)f) << 16); }
DI float xsum32(float v) { auto r = __builtin_amdgcn_permlane32_swap(__float_as_uint(v), __float_as_uint(v), false, false); return __uint_as_float(r[0]) + __uint_as_float(r[1]); }
DI float xmax32(float v) { auto r = __builtin_amdgcn_permlane32_swap(__float_as_uint(v), __float_as_uint(v), false, false); return fmaxf(__uint_as_float(r[0]), __uint_as_float(r[1])); }
DI f32x16 mfma32(u32x4 a, u32x4 b, f32x16 c) { return __builtin_amdgcn_mfma_f32_32x32x16_bf16(__builtin_bit_cast(bf16x8, a), __builtin_bit_cast(bf16x8, b), c, 0, 0, 0); }
DI f32x4 mfma16(u32x4 a, u32x4 b, f32x4 c) { return __builtin_amdgcn_mfma_f32_16x16x32_bf16(__builtin_bit_cast(bf16x8, a), __builtin_bit_cast(bf16x8, b), c, 0, 0, 0); }
DI u32x2 trd(unsigned a) { return __builtin_bit_cast(u32x2, __builtin_amdgcn_ds_read_tr16_b64_v4i16((LAS s16x4*)a)); }
DI u32x4 trfrag(unsigned a, unsigned step) { u32x2 lo = trd(a), hi = trd(a + step); return (u32x4){lo[0], lo[1], hi[0], hi[1]}; }
DI u32x4 lo4(u32x8 v) { return __builtin_shufflevector(v, v, 0, 1, 2, 3); }
DI u32x4 hi4(u32x8 v) { return __builtin_shufflevector(v, v, 4, 5, 6, 7); }
DI float silu(float v) { return v * __builtin_amdgcn_rcpf(1.f + __builtin_amdgcn_exp2f(-1.44269504089f * v)); }
DI unsigned ln2(unsigned v, float m, float rs) { const unsigned d = pk2(bf_lo(v) - m, bf_hi(v) - m); return pk2(bf_lo(d) * rs, bf_hi(d) * rs); }
DI u64 ld8_sc1(const void* p) { return __hip_atomic_load((GAS const u64*)p, __ATOMIC_RELAXED, __HIP_MEMORY_SCOPE_AGENT); }
DI void st8_sc1(void* p, u64 v) { __hip_atomic_store((GAS u64*)p, v, __ATOMIC_RELAXED, __HIP_MEMORY_SCOPE_AGENT); }
DI int fresh_lane() { int l; asm volatile("v_mbcnt_lo_u32_b32 %0, -1, 0\n\tv_mbcnt_hi_u32_b32 %0, -1, %0" : "=v"(l)); return l; }
#define RELANE() const int lane = fresh_lane(), l15 = lane & 15, g4 = lane >> 4, l31 = lane & 31, h2 = lane >> 5; (void)l15; (void)g4; (void)l31; (void)h2
DI u64 mix(unsigned lo, unsigned hi) { u64 z = (((u64)hi << 32) | lo) + 0x9E3779B97F4A7C15ull; z = (z ^ (z >> 30)) * 0xBF58476D1CE4E5B9ull; z = (z ^ (z >> 27)) * 0x94D049BB133111EBull; return z ^ (z >> 31); }
DI unsigned rowmax16(unsigned k) { k = max(k, (unsigned)__builtin_amdgcn_update_dpp(0, (int)k, 0x128, 0xf, 0xf, false)); k = max(k, (unsigned)__builtin_amdgcn_update_dpp(0, (int)k, 0x124, 0xf, 0xf, false));
    k = max(k, (unsigned)__builtin_amdgcn_update_dpp(0, (int)k, 0x122, 0xf, 0xf, false)); return max(k, (unsigned)__builtin_amdgcn_update_dpp(0, (int)k, 0x121, 0xf, 0xf, false)); }
DI void dma1(const void* sbase, unsigned voff, unsigned m0v) { asm volatile("s_mov_b32 m0, %2\n\ts_nop 0\n\tglobal_load_lds_dwordx4 %0, %1" :: "v"(voff), "s"(sbase), "s"(m0v) : "memory"); }
DI void dma1_sc1(const void* sbase, unsigned voff, unsigned m0v) { asm volatile("s_mov_b32 m0, %2\n\ts_nop 0\n\tglobal_load_lds_dwordx4 %0, %1 sc1" :: "v"(voff), "s"(sbase), "s"(m0v) : "memory"); }
DI void dma4(const void* sbase, unsigned voff, unsigned m0v) {
    asm volatile("s_mov_b32 m0, %2\n\ts_nop 0\n\tglobal_load_lds_dwordx4 %0, %1\n\tglobal_load_lds_dwordx4 %0, %1 offset:1024\n\tglobal_load_lds_dwordx4 %0, %1 offset:2048\n\tglobal_load_lds_dwordx4 %0, %1 offset:3072"
                 :: "v"(voff), "s"(sbase), "s"(m0v) : "memory"); }
DI void dma4_sc1(const void* sbase, unsigned voff, unsigned m0v) {
    asm volatile("s_mov_b32 m0, %2\n\ts_nop 0\n\tglobal_load_lds_dwordx4 %0, %1 sc1\n\tglobal_load_lds_dwordx4 %0, %1 offset:1024 sc1\n\tglobal_load_lds_dwordx4 %0, %1 offset:2048 sc1\n\tglobal_load_lds_dwordx4 %0, %1 offset:3072 sc1"
                 :: "v"(voff), "s"(sbase), "s"(m0v) : "memory"); }
#define BAR_ALL() asm volatile("s_waitcnt vmcnt(0) lgkmcnt(0)\n\ts_barrier" ::: "memory")
#define BAR_LDS() asm volatile("s_waitcnt lgkmcnt(0)\n\ts_barrier" ::: "memory")
#define BAR_VM(n) asm volatile("s_waitcnt vmcnt(" #n ") lgkmcnt(0)\n\ts_barrier" ::: "memory")
#define WAIT_VM(n) asm volatile("s_waitcnt vmcnt(" #n ")" ::: "memory")
#define PIN(v) asm volatile("" : "+v"(v))
#define SBAR() __builtin_amdgcn_sched_barrier(0)
#define SGB(m, n) __builtin_amdgcn_sched_group_barrier(m, n, 0)
#define TS(i) do { } while (0)

#define EX(A,i) "v_exp_f32 v[" #A "+" #i "], v[" #A "+" #i "]\n"
#define E4(A,a,b,c,d) EX(A,a) EX(A,b) EX(A,c) EX(A,d)
#define CV(p,A,i) "v_cvt_pk_bf16_f32 v[104+" #p "], v[" #A "+" #i "], v[" #A "+" #i "+1]\n"
#define RDX(dst, o, k) "ds_read_b128 " dst ", v22 offset:" o "+" #k "\n"
#define M32(x) "v_mfma_f32_32x32x16_bf16 " x "\n"
#define M16(x) "v_mfma_f32_16x16x32_bf16 " x "\n"
#define TILE(A, B, o, X1, X2, Y) \
    M32("v[" #B ":" #B "+15], v[112:115], v[96:99], v[64:79]") RDX("v[112:115]", o, 0) X1 E4(A,0,1,2,3) \
    M32("v[80:95], v[124:127], v[108:111], v[80:95]") RDX("v[124:127]", o, 3072) E4(A,4,5,6,7) \
    "v_smfmac_f32_16x16x64_bf16 v[24:27], v[28:31], v[104:111], v23\n" "s_waitcnt lgkmcnt(2)\n" CV(0,A,0) CV(1,A,2) CV(2,A,4) CV(3,A,6) \
    M32("v[" #B ":" #B "+15], v[116:119], v[100:103], v[" #B ":" #B "+15]") RDX("v[116:119]", o, 1024) X2 E4(A,8,9,10,11) \
    M32("v[80:95], v[120:123], v[104:107], v[80:95]") RDX("v[120:123]", o, 2048) E4(A,12,13,14,15) Y \
    "s_waitcnt lgkmcnt(2)\n" CV(4,A,8) CV(5,A,10) CV(6,A,12) CV(7,A,14)
#define NO ""
#define WOA(k) "s_add_u32 m0, %[ma], " #k "\n s_add_u32 s70, s70, 0x8000\n s_addc_u32 s71, s71, 0\n global_load_lds_dwordx4 v20, s[70:71]\n"
#define WOB(k) "s_add_u32 m0, %[mb], " #k "\n s_add_u32 s70, s70, 0x8000\n s_addc_u32 s71, s71, 0\n global_load_lds_dwordx4 v20, s[70:71]\n"
#define MOE(k, st) "s_add_u32 m0, %[mm], " #k "\n s_add_u32 s72, s72, " #st "\n s_addc_u32 s73, s73, 0\n global_load_lds_dwordx4 v19, s[72:73]\n"
#define KV0 "s_mov_b32 m0, %[mk]\n s_nop 0\n global_load_lds_dwordx4 v21, %[sk] sc1\n"
#define KVN(k) "global_load_lds_dwordx4 v21, %[sk] offset:" #k " sc1\n"
#define GRD "s_mov_b32 m0, %[mm]\n s_nop 0\n global_load_lds_dwordx4 v21, s[72:73] sc1\n"
#define TR2(d, a, k, st) "ds_read_b64_tr_b16 v[" #d ":" #d "+1], " a " offset:" #k "\n ds_read_b64_tr_b16 v[" #d "+2:" #d "+3], " a " offset:" #k "+" #st "\n"
#define TRA(j, k) TR2(128+4*j, "v18", k, 512)
#define TRB(j, k) TR2(128+4*j, "v17", k, 512)
#define B64(d, a, k) "ds_read_b64 v[" #d ":" #d "+1], " a " offset:" #k "\n"
#define VM(n) "s_waitcnt vmcnt(" #n ")\n"
#define BODY_TXT(T0, T1, T2, T3, T4, T5, T6, T7) "s_nop 1\n s_setprio 3\n" T0 T1 T2 T3 T4 "s_setprio 2\n" T5 T6 "s_setprio 1\n" T7 "s_waitcnt lgkmcnt(0)\n s_nop 7\n"
#define PH "s_setprio 0\n"
#define T0(a,b,c) TILE(32,48,"0",a,b,c)
#define T1(a,b,c) TILE(48,32,"4096",a,b,c)
#define T2(a,b,c) TILE(32,48,"8192",a,b,c)
#define T3(a,b,c) TILE(48,32,"12288",a,b,c)
#define T4(a,b,c) TILE(32,48,"16384",a,b,c)
#define T5(a,b,c) TILE(48,32,"20480",a,b,c)
#define T6(a,b,c) TILE(32,48,"24576",a,b,c)
#define T7(a,b,c) TILE(48,32,"28672",a,PH b,c)
#define BODY_OUT "+{v[32:47]}"(SA), "=&{v[48:63]}"(SB), "+{v[80:95]}"(O), "+{v[24:31]}"(LS), "+{v[104:119]}"(PK), "+{v[120:127]}"(V), "+{s[70:71]}"(swo), "+{s[72:73]}"(smo)
#define BODY_IN "{v[64:79]}"(NEG), "{v[96:103]}"(Q), "{v23}"(IDX), "{v22}"(ad), "{v21}"(vl), "{v20}"(vw), "{v19}"(vm), "{v18}"(ta), "{v17}"(tb_), \
      [ma] "s"(ma), [mb] "s"(mb), [mm] "s"(mm), [mk] "s"(mk), [sk] "s"(sk)
#define C8(a,b,c,d,e,f,g,h) "v" #a, "v" #b, "v" #c, "v" #d, "v" #e, "v" #f, "v" #g, "v" #h

__global__ __launch_bounds__(512, 2) void fused_block(const char* __restrict__ x, const char* __restrict__ wq, const char* __restrict__ wk, const char* __restrict__ wv,
                                                      const char* __restrict__ wo, const char* __restrict__ wr, const char* __restrict__ wg, const char* __restrict__ wu,
                                                      const char* __restrict__ wd, char* __restrict__ out, char* __restrict__ ws) {
    const int tid = threadIdx.x, lane = tid & 63, w = __builtin_amdgcn_readfirstlane(tid >> 6);
    const int b = blockIdx.x, xcd = b & 7, r = b >> 3, tb = (xcd >> 1) * 8 + (r & 7), hg = (xcd & 1) * 4 + (r >> 3);
    const int isV = r >> 4, sub = (r >> 2) & 3, T0 = (xcd * 4 + (r & 3)) * 64 + sub * 16;
    const int l15 = lane & 15, g4 = lane >> 4, l31 = lane & 31, h2 = lane >> 5;
    const int hd = w & 3, tbk = w >> 2;
    const unsigned vl = lane * 16;
    TS(0);
    {
        u64 pc; asm volatile("s_getpc_b64 %0" : "=s"(pc));
        asm volatile("s_mov_b32 m0, %2\n\ts_mov_b64 exec, 3\n\tglobal_load_lds_dword %0, %1\n\ts_mov_b64 exec, -1"
                     :: "v"(lane * 128), "s"((pc & ~127ull) + min((r * 8 + w) * 256, CODE_SPAN - 256)), "s"(w < 4 ? L_X16 + w * 8192 : L_WKV + (w - 4) * 16384) : "memory");
    }
    const int c0 = r & 7, rm = min(r, 27), mi = xcd * 28 + rm; const char* kv = ws + WS_KV;
    f32x16 qa; float csum;
    u32x8 LS, PZ; f32x16 O; unsigned IDX, vw, vm0, ta, tb_;
    const unsigned ma = R4 + w * 2048, mb = R3 + w * 4096;
#define SETUP() do { const unsigned one_ = 0x3F803F80u; LS = (u32x8){0u, 0u, 0u, 0u, l15 < 2 ? one_ : 0u, l15 < 2 ? one_ : 0u, (l15 & 14) == 2 ? one_ : 0u, (l15 & 14) == 2 ? one_ : 0u}; \
        IDX = (lane & 1) ? 0xEEEEEEEEu : 0x44444444u; vw = (lane >> 2) * 2048 + (lane & 3) * 16; vm0 = (lane >> 3) * 7168 + (((lane & 7) ^ (g4 << 1)) << 4); \
        ta = ma + (4 * h2 + (l15 >> 2)) * 64 + (g4 & 1) * 32 + (lane & 3) * 8; tb_ = ta + (mb - ma); for (int i = 0; i < 16; ++i) O[i] = 0.f; PZ = (u32x8){0u, 0u, 0u, 0u, 0u, 0u, 0u, 0u}; PIN(O); PIN(PZ); PIN(LS); PIN(IDX); PIN(vw); PIN(vm0); PIN(ta); PIN(tb_); } while (0)
    if (__builtin_expect(w < 4, 1)) {
        __builtin_amdgcn_s_setprio(3);
        u32x2 ev; u32x4 xq[8], aw[8], sel, ones; unsigned wa, wa2, spa, kvo, wb[3][2], xb_[3][4];
        {
            const char* xs = x + (size_t)T0 * 2048 + w * 512;
            for (int i = 0; i < 8; ++i) dma1(xs, (2 * i + h2) * 2048 + ((l31 ^ (2 * i + h2)) << 4), L_X16 + w * 8192 + i * 1024);
            dma1(wr + w * 512, l31 * 16, L_WR + w * 1024);
            asm volatile("s_mov_b64 exec, 1\n\tglobal_load_dwordx2 %0, %1, %2 sc1\n\ts_mov_b64 exec, -1" : "=v"(ev) : "v"(0), "s"(ws));
            SETUP();
            wa = L_WKV + w * 16384 + (8 * g4 + (l15 >> 2)) * 64 + (g4 & 1) * 32 + (lane & 3) * 8; wa2 = wa ^ 32; spa = L_SP + ((l15 + (isV ? 4 * g4 : 0)) & 15) * 16;
            const int kt = T0 >> 5, sh = sub & 1;
            kvo = isV ? ((kt - 1 + sh) & 63) * 4096 + 2048 + sh * 1024 + ((g4 & 1) * 32 + w * 16 + l15) * 16 + (g4 >> 1) * 8
                      : ((kt - 2) & 63) * 4096 + w * 1024 + ((g4 & 1) * 32 + sh * 16 + l15) * 16 + (g4 >> 1) * 8;
            const unsigned sv = (((l15 >> 2) & 1) == (g4 & 1)) ? 0x3F803F80u : 0u, xq_a = l31 * 128 + ((h2 ^ ((l31 >> 1) & 7)) << 4);
            const unsigned wq_a = 8192 + (8 * h2 + (l15 >> 2)) * 256 + ((hd ^ ((lane >> 2) & 3)) << 6) + (g4 & 1) * 32 + (lane & 3) * 8;
            sel = (u32x4){sv, sv, sv, sv}; ones = (u32x4){0x3F803F80u, 0x3F803F80u, 0x3F803F80u, 0x3F803F80u};
            for (int i = 0; i < 3; ++i) {
                wb[i][0] = wq_a + i * 2 * SLOT + (tb & 1) * 8192; wb[i][1] = wq_a + i * 2 * SLOT + (~tb & 1) * 8192; PIN(wb[i][0]); PIN(wb[i][1]);
                for (int ks = 0; ks < 4; ++ks) { xb_[i][ks] = (xq_a ^ (ks * 32) ^ ((tb & 1) * 64)) + i * 2 * SLOT; PIN(xb_[i][ks]); } }
            const unsigned oth = l15 == 1 ? 0x3F803F80u : 0u;
            for (int s = 0; s < 8; ++s) { aw[s] = (u32x4){oth, oth, oth, oth}; PIN(aw[s]); }
            PIN(kvo); PIN(wa); PIN(wa2); PIN(spa); PIN(ones); PIN(sel);
            WAIT_VM(1);
            const unsigned xa = L_X16 + w * 8192 + l15 * 512 + ((g4 ^ l15) << 4);
            for (int s = 0; s < 8; ++s) xq[s] = LD(u32x4, xa ^ (s * 64));
            if (l15 == 0) for (int s = 0; s < 8; ++s) aw[s] = LD(u32x4, L_WR + w * 1024 + s * 64 + g4 * 16);
        }
        TS(8);
        const f32x4 z4 = {0, 0, 0, 0};
        f32x4 s1 = z4, gr = z4, sw = z4;
        for (int s = 0; s < 8; ++s) { gr = mfma16(xq[s], xq[s], gr); s1 = mfma16(aw[s], xq[s], s1); sw = mfma16(aw[s], ones, sw); }
        { float d = (l15 & 2) ? ((l15 & 1) ? gr[3] : gr[2]) : ((l15 & 1) ? gr[1] : gr[0]); d = g4 == (l15 >> 2) ? d : 0.f;
          const auto e16 = __builtin_amdgcn_permlane16_swap(__float_as_uint(d), __float_as_uint(d), false, false); d = xsum32(__uint_as_float(e16[0]) + __uint_as_float(e16[1]));
          if (lane < 16) LD(f32x4, L_SP + w * 256 + vl) = (f32x4){s1[1], d, s1[0], sw[0]}; }
        TS(9);
        BAR_LDS();
        f32x4 m4, r4; unsigned key = 0;
        {
            f32x4 p[4]; for (int q = 0; q < 4; ++q) p[q] = LD(f32x4, spa + q * 256);
            const f32x4 t = (p[0] + p[1]) + (p[2] + p[3]);
            const float mean = t[0] * (1.f / 1024.f), var = fmaxf(t[1] * (1.f / 1024.f) - mean * mean, 0.f);
            const float mrow = rb(mean), rsrow = __builtin_amdgcn_rcpf(rb(__builtin_amdgcn_sqrtf(rb(rb(var) + 1e-5f))));
            const int mi_ = (int)__float_as_uint(mrow), ri_ = (int)__float_as_uint(rsrow);
#define BC(v, n) __uint_as_float((unsigned)__builtin_amdgcn_update_dpp(0, v, 0x150 + (n), 0xf, 0xf, false))
            m4 = (f32x4){BC(mi_, 0), BC(mi_, 1), BC(mi_, 2), BC(mi_, 3)}; r4 = (f32x4){BC(ri_, 0), BC(ri_, 1), BC(ri_, 2), BC(ri_, 3)};
            if (!isV) { m4 = (f32x4){mrow, mrow, mrow, mrow}; r4 = (f32x4){rsrow, rsrow, rsrow, rsrow};
                if (w == 2) { const unsigned u = __float_as_uint(rb((t[2] - mrow * t[3]) * rsrow)) >> 16;
                    key = rowmax16(((((u & 0x8000u) ? ~u : (u | 0x8000u)) & 0xffffu) << 11) | (2047 - (T0 + l15))); } }
        }
        PIN(m4); PIN(r4);
        TS(34);
        BAR_LDS();
        TS(33);
        auto kvpart = [&](auto V_) {
            constexpr bool V = decltype(V_)::value;
            u32x4 w0[8], w1[8]; f32x4 a0 = z4, a1 = z4, c0_ = z4, c1_ = z4;
            for (int s = 0; s < 3; ++s) { w0[s] = trfrag(wa + s * 2048, 256); w1[s] = trfrag(wa2 + s * 2048, 256); }
            SBAR();
#pragma unroll
            for (int s = 0; s < 8; ++s) {
                if (s < 5) { w0[s + 3] = trfrag(wa + (s + 3) * 2048, 256); w1[s + 3] = trfrag(wa2 + (s + 3) * 2048, 256); }
                if (V) { a0 = mfma16(xq[s], w0[s], a0); c0_ = mfma16(ones, w0[s], c0_); a1 = mfma16(xq[s], w1[s], a1); c1_ = mfma16(ones, w1[s], c1_); }
                else { a0 = mfma16(w0[s], xq[s], a0); c0_ = mfma16(w0[s], ones, c0_); a1 = mfma16(w1[s], xq[s], a1); c1_ = mfma16(w1[s], ones, c1_); }
                if (s < 5) for (int i = 0; i < 4; ++i) { SGB(0x008, 1); SGB(0x100, 1); }
                SBAR();
            }
            LD(f32x4, L_PA + w * 2048 + vl) = (a0 - m4 * c0_) * r4; LD(f32x4, L_PA + w * 2048 + 1024 + vl) = (a1 - m4 * c1_) * r4;
        };
        if (isV) kvpart(std::true_type{}); else kvpart(std::false_type{});
        asm volatile("s_waitcnt vmcnt(0)" : "+v"(ev) :: "memory");
        if (tid == 192) LD(u32x2, M_CTL) = ev;
        TS(35);
        BAR_LDS();
        TS(1);
        if (w < 2) {
            f32x4 p[4]; for (int i = 0; i < 4; ++i) p[i] = LD(f32x4, L_PA + i * 2048 + w * 1024 + vl);
            const f32x4 a = (p[0] + p[1]) + (p[2] + p[3]);
            st8_sc1(ws + WS_KV + kvo, ((u64)pk2(a[2], a[3]) << 32) | pk2(a[0], a[1]));
        } else if (w == 2 && !isV && lane < 16) st8_sc1(ws + WS_STATS + (T0 + l15) * 8, ((u64)__float_as_uint(r4[0]) << 32) | __float_as_uint(m4[0]));
        f32x16 qb; qa = O; qb = O;
        u32x4 af[2][4], b0[2][4], b1[2][4];
#define QRD(p, s) do { const unsigned sl_ = (slot_of(s) & 1) * SLOT; const int bi_ = slot_of(s) >> 1; for (int ks = 0; ks < 4; ++ks) { \
            af[p][ks] = trfrag(wb[bi_][ks >> 1] + sl_ + (ks & 1) * 4096, 1024); b1[p][ks] = LD(u32x4, xb_[bi_][ks] + sl_ + 4096); b0[p][ks] = LD(u32x4, xb_[bi_][ks] + sl_); } } while (0)
#pragma unroll
        for (int s = 0; s < 16; ++s) {

            if (s == FSTAGE || s == 10) WAIT_VM(0);
            if (!(s & 1)) { BAR_LDS(); if (s == 0) TS(37); if (s == 2) TS(39); if (s == 4) TS(41); if (s == 6) TS(43); if (s == 8) TS(45); if (s == 10) TS(47); if (s == 12) TS(49); if (s == 14) TS(51); }
            if (s == 0) { QRD(0, 0); BAR_LDS();
                const f32x4 cs = mfma16(sel, af[0][1], mfma16(sel, af[0][0], z4));
                if (lane < 32) __hip_atomic_store((GAS unsigned*)(ws + WS_CS + (hg * 32 + tb) * 512 + w * 128 + lane * 4), __float_as_uint(cs[0]), __ATOMIC_RELAXED, __HIP_MEMORY_SCOPE_AGENT);
                SBAR(); }
            if (s == FSTAGE && tid == 128) { const u32x2 e = LD(u32x2, M_CTL); st8_sc1(ws + WS_FLAG1 + b * 8, (mix(e[0], e[1]) & ~0xFFFFFFFull) | key); }
            if (s == 8 && w >= 2) dma1_sc1(ws + WS_FLAG1 + (w - 2) * 1024, vl, M_GU + (w - 2) * 1024);
            if (s < 15) {
                QRD((s + 1) & 1, s + 1);
                for (int ks = 0; ks < 4; ++ks) { qb = mfma32(af[s & 1][ks], b1[s & 1][ks], qb); qa = mfma32(af[s & 1][ks], b0[s & 1][ks], qa); }
                for (int i = 0; i < 8; ++i) { SGB(0x008, 1); SGB(0x100, 2); }
            } else {
                for (int ks = 0; ks < 4; ++ks) qb = mfma32(af[1][ks], b1[1][ks], qb);
                SBAR();
                for (int ks = 0; ks < 4; ++ks) { qa = mfma32(af[1][ks], b0[1][ks], qa); LD(f32x4, L_QX + w * 4096 + ks * 1024 + vl) = (f32x4){qb[4 * ks], qb[4 * ks + 1], qb[4 * ks + 2], qb[4 * ks + 3]}; }
            }
            SBAR();
        }
        TS(2);
        BAR_LDS();
        csum = LD(float, M_CS + w * 256 + lane * 4);
    } else {
        const int lw = w - 4, koff = tb >> 1;
        { const char* wkv = (isV ? wv : wk) + lw * 16384;
          for (int i = 0; i < 4; ++i) dma4(wkv + i * 4096, vl ^ (h2 << 5), L_WKV + lw * 16384 + i * 4096); }
        unsigned vx[2], vq[4];
        for (int i = 0; i < 2; ++i) { const int row = lw * 16 + i * 8 + (lane >> 3); vx[i] = row * 2048 + (((lane & 7) ^ ((row >> 1) & 7)) << 4); }
        for (int i = 0; i < 4; ++i) vq[i] = (lw * 16 + i * 4 + g4) * 2048 + ((((lane >> 2) & 3) ^ g4) << 6) + ((lane & 3) << 4);
        const char* xb = x + (size_t)tb * 64 * 2048; const char* qb_ = wq + hg * 256;
#define RING_ISSUE(s) do { const int kk_ = ((s) + koff) & 15; \
        asm volatile("s_mov_b32 m0, %8\n\ts_nop 0\n\tglobal_load_lds_dwordx4 %0, %6\n\ts_add_u32 m0, m0, 1024\n\ts_nop 0\n\tglobal_load_lds_dwordx4 %1, %6\n\t" \
                     "s_mov_b32 m0, %9\n\ts_nop 0\n\tglobal_load_lds_dwordx4 %2, %7\n\ts_add_u32 m0, m0, 1024\n\ts_nop 0\n\tglobal_load_lds_dwordx4 %3, %7\n\t" \
                     "s_add_u32 m0, m0, 1024\n\ts_nop 0\n\tglobal_load_lds_dwordx4 %4, %7\n\ts_add_u32 m0, m0, 1024\n\ts_nop 0\n\tglobal_load_lds_dwordx4 %5, %7" \
                     :: "v"(vx[0]), "v"(vx[1]), "v"(vq[0]), "v"(vq[1]), "v"(vq[2]), "v"(vq[3]), "s"(xb + kk_ * 128), "s"(qb_ + (size_t)kk_ * 131072), \
                        "s"(slot_of(s) * SLOT + lw * 2048), "s"(slot_of(s) * SLOT + 8192 + lw * 4096) : "memory", "scc"); } while (0)
        TS(32); RING_ISSUE(0); TS(8);
        BAR_LDS();
        BAR_VM(6); TS(33);
        RING_ISSUE(1); RING_ISSUE(2); TS(52);
        TS(35);
        BAR_LDS(); TS(1); RING_ISSUE(3); RING_ISSUE(4); TS(36);
        BAR_VM(12); TS(37); RING_ISSUE(5);
        BAR_LDS(); RING_ISSUE(6); TS(38);
        BAR_VM(12); TS(39); RING_ISSUE(7); RING_ISSUE(8); TS(40);
        BAR_VM(12); TS(41); RING_ISSUE(9); RING_ISSUE(10); TS(42);
        BAR_VM(12); TS(43); RING_ISSUE(11); RING_ISSUE(12); TS(44);
        BAR_VM(12); TS(45);
        RING_ISSUE(13); RING_ISSUE(14); TS(46);
        const u32x2 e = LD(u32x2, M_CTL); const u64 tag36 = mix(e[0], e[1]) & ~0xFFFFFFFull;
        BAR_VM(12); TS(47); RING_ISSUE(15);
        u64 f[4]; { const u32x4 f0 = LD(u32x4, M_GU + vl), f1 = LD(u32x4, M_GU + 1024 + vl); f[0] = ((u64)f0[1] << 32) | f0[0]; f[1] = ((u64)f0[3] << 32) | f0[2]; f[2] = ((u64)f1[1] << 32) | f1[0]; f[3] = ((u64)f1[3] << 32) | f1[2]; }
#define FLAGS_OK() __all((((f[0] ^ tag36) | (f[1] ^ tag36) | (f[2] ^ tag36) | (f[3] ^ tag36)) >> 28) == 0)
        const bool early = FLAGS_OK();
        BAR_VM(6); TS(49);
        if (!early) {
            BAR_VM(0);
#pragma unroll 1
            for (unsigned spins = 0; spins < SPIN_CAP; ++spins) {
                for (int i = 0; i < 4; ++i) f[i] = ld8_sc1(ws + WS_FLAG1 + (i * 64 + lane) * 8);
                if (FLAGS_OK()) break;
                __builtin_amdgcn_s_sleep(2);
            }
            PIN(f[0]); PIN(f[1]); PIN(f[2]); PIN(f[3]);
        }
        TS(50);
        for (int i = 0; i < 4; ++i) dma1_sc1(ws + WS_CS + hg * 16384 + i * 4096 + lw * 128, (lane >> 3) * 512 + (lane & 7) * 16, L_CSP + lw * 4096 + i * 1024);
        if (lw < 2) { dma4_sc1(kv + ((c0 * 8 + 62 + lw) & 63) * 4096, vl, L_PRO + lw * 4096); if (early) BAR_VM(8); }
        else if (lw == 2) { dma1_sc1(ws + WS_STATS + tb * 512, l31 * 16, M_ST64); if (early) BAR_VM(5); }
        else {
            unsigned key = rowmax16(max(max((unsigned)f[0] & 0xFFFFFFFu, (unsigned)f[1] & 0xFFFFFFFu), max((unsigned)f[2] & 0xFFFFFFFu, (unsigned)f[3] & 0xFFFFFFFu)));
            key = max(max((unsigned)__builtin_amdgcn_readlane(key, 0), (unsigned)__builtin_amdgcn_readlane(key, 16)), max((unsigned)__builtin_amdgcn_readlane(key, 32), (unsigned)__builtin_amdgcn_readlane(key, 48)));
            const int ix = 2047 - (key & 2047);
            dma1(x + (size_t)ix * 2048 + (mi & 3) * 512, l31 * 16, M_XSEL);
            asm volatile("s_mov_b32 m0, %2\n\ts_nop 0\n\tglobal_load_lds_dword %0, %1 sc1" :: "v"((lane & 1) * 4), "s"(ws + WS_STATS + ix * 8), "s"(M_STX) : "memory");
            if (early) BAR_VM(6);
            if (lane == 0) { LD(unsigned, M_CTL + 8) = key; if (b == 0) st8_sc1(ws + WS_EPOCH, (((u64)e[1] << 32) | e[0]) + 1); }
        }
        TS(51);
        dma4_sc1(kv + c0 * 32768 + lw * 8192, vl, KA + lw * 8192); dma4_sc1(kv + c0 * 32768 + lw * 8192 + 4096, vl, KA + lw * 8192 + 4096);
        SETUP();
        WAIT_VM(8);
        { float p[16]; for (int i = 0; i < 16; ++i) p[i] = LD(float, L_CSP + lw * 4096 + (h2 * 16 + i) * 128 + l31 * 4);
          csum = xsum32((((p[0] + p[1]) + (p[2] + p[3])) + ((p[4] + p[5]) + (p[6] + p[7]))) + (((p[8] + p[9]) + (p[10] + p[11])) + ((p[12] + p[13]) + (p[14] + p[15]))));
          LD(float, M_CS + lw * 256 + lane * 4) = csum; }
        TS(2);
        __builtin_amdgcn_s_setprio(3);
        BAR_LDS();
        for (int q = 0; q < 4; ++q) { const f32x4 t = LD(f32x4, L_QX + hd * 4096 + q * 1024 + vl); for (int i = 0; i < 4; ++i) qa[4 * q + i] = t[i]; }
    }
    TS(3);
    u32x8 Q, V; u32x16 PK; f32x16 SA, SB, NEG;
    const u32x4 ctl = LD(u32x4, M_CTL);
    {
        const f32x2 st = LD(f32x2, M_ST64 + (tbk * 32 + l31) * 8);
        const u32x4 k00 = LD(u32x4, L_PRO + vl), k01 = LD(u32x4, L_PRO + 1024 + vl), k10 = LD(u32x4, L_PRO + 4096 + vl), k11 = LD(u32x4, L_PRO + 5120 + vl), v00 = LD(u32x4, L_PRO + 6144 + vl);
        const float c1 = rb(csum), c2 = rb(csum - c1), c3 = csum - c1 - c2;
        const u32x4 ca = {h2 ? 0u : pk2(c1, c2), h2 ? 0u : pk2(c3, 0.f), 0u, 0u}, cb = {pk2(-st[0], -st[0]), pk2(-st[0], 0.f), 0u, 0u};
        qa = mfma32(ca, cb, qa);
        const float qs = st[1] * (0.17677669529663687f * 1.4426950408889634f);
        for (int i = 0; i < 8; ++i) Q[i] = pk2(qa[2 * i] * qs, qa[2 * i + 1] * qs);
        f32x16 s0 = mfma32(k00, lo4(Q), O); s0 = mfma32(k01, hi4(Q), s0);
        for (int i = 0; i < 4; ++i) { PK[i] = PZ[i]; PK[4 + i] = PZ[4 + i]; PK[8 + i] = k10[i]; PK[12 + i] = k11[i]; V[i] = v00[i]; V[4 + i] = k00[i]; }
        float m = fmaxf(fmaxf(fmaxf(fmaxf(s0[0], s0[1]), s0[2]), fmaxf(fmaxf(s0[3], s0[4]), s0[5])), fmaxf(fmaxf(fmaxf(s0[6], s0[7]), s0[8]), fmaxf(fmaxf(s0[9], s0[10]), s0[11])));
        m = xmax32(fmaxf(fmaxf(m, fmaxf(s0[12], s0[13])), fmaxf(s0[14], s0[15])));
        NEG = __builtin_amdgcn_mfma_f32_32x32x2f32(h2 ? 0.f : 1.f, -m, O, 0, 0, 0);
        for (int i = 0; i < 16; ++i) SA[i] = s0[i] - m;
        PIN(NEG); PIN(SA);
    }
    TS(4);
    const u64 seed = mix(__builtin_amdgcn_readfirstlane(ctl[0]), __builtin_amdgcn_readfirstlane(ctl[1]));
    const unsigned tag32 = (unsigned)seed ^ 0x5bd1e995u, gkey = __builtin_amdgcn_readfirstlane(ctl[2]);
    const int idx = 2047 - (gkey & 2047), cbk = hd, kh = tbk;
    const bool moe = r < 28;
    u64 swo = (u64)(wo + (size_t)kh * 512 * 2048 + hg * 256 + cbk * 64) - 0x8000, smo;
    u32x8 wf[16];
#define SEAM(c) const unsigned ad = (((c) & 1) ? KB : KA) + vl, mk = (((c) & 1) ? KA : KB) + w * 4096; const char* sk = kv + ((c0 + (c) + 1) & 7) * 32768 + w * 4096
#define MROW() (16 * (g4 >> 1) + 4 * (g4 & 1) + (l15 >> 2))
#define MSWZ() (2 * (g4 & 1) + (l15 >> 3))
    { BAR_VM(0); SEAM(0); const unsigned vm = vm0, mm = R2 + w * 4096; smo = (u64)(wg + (size_t)((mi & 3) * 256 + w * 32) * 7168 + (mi >> 2) * 128); TS(10);
      asm volatile(BODY_TXT(T0(WOA(0), NO, NO), T1(WOA(1024), NO, NO), T2(KV0, KVN(1024), NO), T3(KVN(2048), KVN(3072), NO), T4(MOE(0, 0), MOE(1024, 57344), NO), T5(MOE(2048, 57344), MOE(3072, 57344), NO),
                            T6(NO, NO, NO), T7(VM(8) TRA(0, 0) TRA(1, 1024), NO, NO))
                   : BODY_OUT, "=&{v[128:135]}"(wf[0]) : BODY_IN : "memory", "scc"); TS(18); }
    { BAR_VM(4); SEAM(1); const unsigned vm = vm0, mm = mb; smo = (u64)(wu + (size_t)((mi & 3) * 256 + w * 32) * 7168 + (mi >> 2) * 128);
      TS(11);
      asm volatile(BODY_TXT(T0(WOA(0), NO, NO), T1(WOA(1024), NO, NO), T2(KV0, KVN(1024), NO), T3(KVN(2048), KVN(3072), NO), T4(MOE(0, 0), MOE(1024, 57344), NO), T5(MOE(2048, 57344), MOE(3072, 57344), NO),
                            T6(NO, NO, NO), T7(VM(8) TRA(2, 0) TRA(3, 1024), NO, NO))
                   : BODY_OUT, "=&{v[136:143]}"(wf[1]) : BODY_IN : "memory", "scc"); TS(19); }
    { if (w == 0) { RELANE(); const f32x2 sx = LD(f32x2, M_STX); const u32x2 v = LD(u32x2, M_XSEL + lane * 8); LD(u32x2, M_XSEL + lane * 8) = (u32x2){ln2(v[0], sx[0], sx[1]), ln2(v[1], sx[0], sx[1])}; }
      BAR_VM(4); SEAM(2); const unsigned vm = 0, mm = 0; TS(12);
      asm volatile(BODY_TXT(T0(WOA(0), NO, NO), T1(WOA(1024), NO, NO), T2(KV0, NO, NO), T3(KVN(1024), NO, NO), T4(KVN(2048), NO, NO), T5(KVN(3072), NO, NO), T6(NO, NO, NO), T7(VM(4) TRA(4, 0) TRA(5, 1024), NO, NO))
                   : BODY_OUT, "=&{v[144:151]}"(wf[2]) : BODY_IN : "memory", "scc"); TS(20); }
    { RELANE(); const unsigned vm = R2 + w * 4096 + MROW() * 128 + MSWZ() * 32 + (lane & 3) * 8, mm = 0;
      const unsigned xa = M_XSEL + w * 64 + (g4 >> 1) * 32 + (g4 & 1) * 8, gw = M_GU + w * 256 + lane * 4;
      BAR_VM(0); SEAM(3); TS(13);
#define GM(d, a) M16("v[" #d ":" #d "+3], v[176:179], v[" #a ":" #a "+3], 0")
      asm volatile(BODY_TXT(T0(WOA(0) "v_xor_b32 v244, 32, v19\n v_xor_b32 v245, 64, v19\n v_xor_b32 v246, 0x60, v19\n v_subrev_u32 v247, 0x10000, v19\n" B64(176, "v15", 0) B64(178, "v15", 16) TR2(180, "v19", 0, 1024),
                               WOA(1024) TR2(184, "v244", 0, 1024) TR2(188, "v245", 0, 1024) "v_subrev_u32 v244, 0x10000, v244\n v_subrev_u32 v245, 0x10000, v245\n", NO),
                            T1(TR2(192, "v246", 0, 1024) TR2(196, "v247", 0, 1024) "v_subrev_u32 v246, 0x10000, v246\n", TR2(200, "v244", 0, 1024) TR2(204, "v245", 0, 1024), GM(212, 180) GM(216, 184)),
                            T2(TR2(208, "v246", 0, 1024) KV0, KVN(1024), GM(220, 188) GM(224, 192)), T3(KVN(2048) KVN(3072), WOB(0), GM(228, 196) GM(232, 200)), T4(WOB(1024), WOB(2048), GM(236, 204) GM(240, 208)),
                            T5(WOB(3072), NO, NO),
                            T6("s_mov_b32 vcc_lo, 0xffff0000\n s_mov_b32 vcc_hi, 0xffff0000\n v_cndmask_b32 v212, v212, v216, vcc\n v_cndmask_b32 v220, v220, v224, vcc\n v_cndmask_b32 v228, v228, v232, vcc\n"
                               "v_cndmask_b32 v236, v236, v240, vcc\n s_mov_b32 vcc_lo, 0\n s_mov_b32 vcc_hi, -1\n v_cndmask_b32 v212, v212, v220, vcc\n v_cndmask_b32 v228, v228, v236, vcc\n",
                               "ds_write_b32 v16, v212\n ds_write_b32 v16, v228 offset:2048\n" VM(8) TRA(6, 0) TRA(7, 1024), NO),
                            T7(VM(0) TRB(8, 0) TRB(9, 1024), TRB(10, 2048) TRB(11, 3072), NO))
                   : BODY_OUT, "=&{v[152:159]}"(wf[3]), "=&{v[160:167]}"(wf[4]), "=&{v[168:175]}"(wf[5]) : BODY_IN, "{v15}"(xa), "{v16}"(gw)
                   : "memory", "scc", C8(176,177,178,179,180,181,182,183), C8(184,185,186,187,188,189,190,191), C8(192,193,194,195,196,197,198,199), C8(200,201,202,203,204,205,206,207),
                     C8(208,209,210,211,212,213,214,215), C8(216,217,218,219,220,221,222,223), C8(224,225,226,227,228,229,230,231), C8(232,233,234,235,236,237,238,239), C8(240,241,242,243,244,245,246,247), "vcc"); TS(21); }
    const unsigned ye = moe ? 0xFFFFu : 0u;
    { RELANE(); const unsigned vm = (lane >> 3) * 2048 + (((lane & 7) ^ (g4 << 1)) << 4), mm = R2 + w * 2048; smo = (u64)(wd + (size_t)(rm * 128 + w * 16) * 2048 + xcd * 256);
      const unsigned ga = M_GU + (w >> 2) * 2048 + ((w & 3) * 16 + l15) * 4, go = l15 * 64; const char* gs = ws + WS_GU + ((mi >> 2) * 64 + (w & 3) * 16) * 64 + (w >> 2) * 32 + (mi & 3) * 8;
      BAR_VM(0); SEAM(4); TS(14);
#define R2S(d, a, b) "ds_read2st64_b32 v[" #d ":" #d "+1], v15 offset0:" #a " offset1:" #b "\n"
      asm volatile(BODY_TXT(T0(WOA(0) R2S(224, 0, 1) R2S(226, 2, 3), WOA(1024) R2S(228, 4, 5) R2S(230, 6, 7), NO),
                            T1(WOB(0), WOB(1024) "v_add_f32 v224, v224, v225\n v_add_f32 v226, v226, v227\n v_add_f32 v228, v228, v229\n v_add_f32 v230, v230, v231\n v_add_f32 v224, v224, v226\n v_add_f32 v228, v228, v230\n"
                               "v_mov_b32 v225, %[tg]\n v_add_f32 v224, v224, v228\n", "s_mov_b32 exec_lo, %[ye]\n s_mov_b32 exec_hi, 0\n global_store_dwordx2 v16, v[224:225], %[gs] sc1\n s_mov_b64 exec, -1\n"),
                            T2(WOB(2048), WOB(3072), NO), T3(KV0, KVN(1024), NO), T4(KVN(2048), KVN(3072), NO), T5(MOE(0, 0), MOE(16384, 128), NO),
                            T6(MOE(1024, 16256), MOE(17408, 128) VM(8) TRA(12, 0) TRA(13, 1024), NO), T7(TRB(14, 0) TRB(15, 1024), TRB(16, 2048) TRB(17, 3072), NO))
                   : BODY_OUT, "=&{v[176:183]}"(wf[6]), "=&{v[184:191]}"(wf[7]), "=&{v[192:199]}"(wf[8]) : BODY_IN, "{v15}"(ga), "{v16}"(go), [ye] "s"(ye), [tg] "s"(tag32), [gs] "s"(gs)
                   : "memory", "scc", C8(224,225,226,227,228,229,230,231)); TS(22); }
    u64 bad;
    { RELANE(); const unsigned vm = 0, mm = M_GR + w * 1024, ga = M_GR + w * 1024 + lane * 16, hw = M_H + w * 32 + (lane >> 2) * 2; smo = (u64)(ws + WS_GU + (size_t)(rm * 128 + w * 16) * 64);
      BAR_VM(4); SEAM(5); TS(15);
      asm volatile(BODY_TXT(T0(WOA(0), WOA(1024), NO), T1(WOB(0), WOB(1024), NO), T2(WOB(2048), WOB(3072), GRD), T3(KV0, KVN(1024), NO), T4(KVN(2048), KVN(3072), NO), T5(NO, NO, NO),
                            T6(VM(4) "ds_read_b128 v[224:227], v15\n" TRA(18, 0) TRA(19, 1024), TRB(20, 0) TRB(21, 1024), NO),
                            T7("v_add_f32 v224, v224, v226\n v_cmp_ne_u32 vcc, %[tg], v225\n v_cmp_ne_u32 s[74:75], %[tg], v227\n v_add_f32_dpp v224, v224, v224 quad_perm:[1,0,3,2] row_mask:0xf bank_mask:0xf\n"
                               "v_cvt_pk_bf16_f32 v224, v224, v224\n v_lshlrev_b32 v224, 16, v224\n v_mul_f32 v226, 0xbfb8aa3b, v224\n v_exp_f32 v226, v226\n"
                               "v_mov_b32_dpp v225, v224 quad_perm:[2,3,0,1] row_mask:0xf bank_mask:0xf\n s_or_b64 s[74:75], vcc, s[74:75]\n" TRB(22, 2048),
                               "v_add_f32 v226, 1.0, v226\n v_rcp_f32 v226, v226\n" TRB(23, 3072),
                               "v_mul_f32 v226, v224, v226\n v_cvt_pk_bf16_f32 v226, v226, v226\n v_lshlrev_b32 v226, 16, v226\n v_mul_f32 v226, v226, v225\n v_cvt_pk_bf16_f32 v226, v226, v226\n"
                               "s_mov_b32 exec_lo, 0x11111111\n s_mov_b32 exec_hi, 0x11111111\n ds_write_b16 v16, v226\n s_mov_b64 exec, -1\n"))
                   : BODY_OUT, "=&{v[200:207]}"(wf[9]), "=&{v[208:215]}"(wf[10]), "=&{v[216:223]}"(wf[11]), "=&{s[74:75]}"(bad) : BODY_IN, "{v15}"(ga), "{v16}"(hw), [tg] "s"(tag32)
                   : "memory", "scc", "vcc", "v224", "v225", "v226", "v227"); TS(23); }
    { RELANE();
      if (__builtin_expect(moe && bad != 0, 0)) {
          u32x4 gq;
#pragma unroll 1
          for (unsigned spins = 0; spins < SPIN_CAP; ++spins) {
              const char* p = ws + WS_GU + (size_t)(r * 128 + w * 16) * 64 + lane * 16; const u64 v0 = ld8_sc1(p), v1 = ld8_sc1(p + 8); gq = (u32x4){(unsigned)v0, (unsigned)(v0 >> 32), (unsigned)v1, (unsigned)(v1 >> 32)};
              if (__all(gq[1] == tag32 && gq[3] == tag32)) break;
              __builtin_amdgcn_s_sleep(2); }
          float sm = __uint_as_float(gq[0]) + __uint_as_float(gq[2]);
          sm = rb(sm + __uint_as_float(__builtin_amdgcn_update_dpp(0, (int)__float_as_uint(sm), 0xB1, 0xf, 0xf, false)));
          const float uu = __uint_as_float(__builtin_amdgcn_update_dpp(0, (int)__float_as_uint(sm), 0x4E, 0xf, 0xf, false));
          if (!(lane & 3)) LD(unsigned short, M_H + w * 32 + (lane >> 2) * 2) = (unsigned short)pk2(rb(silu(sm)) * uu, 0.f);
      }
      const unsigned vm = R2 + (w >> 2) * 16384 + MROW() * 128 + ((MSWZ() ^ (w & 3)) * 32) + (lane & 3) * 8, mm = tag32, ha = M_H + (g4 >> 1) * 32 + (g4 & 1) * 8, yo = lane * 8;
      smo = (u64)(ws + WS_YG + (rm * 1024 + xcd * 128 + w * 16) * 8);
      BAR_VM(0); SEAM(6); TS(16);
#define DM(h, d, c) M16("v[248:251], v[" #h ":" #h "+3], v[" #d ":" #d "+3], " c)
      asm volatile(BODY_TXT(T0(WOA(0) B64(224, "v15", 0) B64(226, "v15", 16) TR2(228, "v19", 0, 1024), WOA(1024) B64(232, "v15", 64) B64(234, "v15", 80) TR2(236, "v19", 4096, 1024), NO),
                            T1(WOB(0), WOB(1024), DM(224, 228, "0") DM(232, 236, "v[248:251]")),
                            T2(WOB(2048) B64(224, "v15", 128) B64(226, "v15", 144) TR2(228, "v19", 8192, 1024), WOB(3072) B64(232, "v15", 192) B64(234, "v15", 208) TR2(236, "v19", 12288, 1024), NO),
                            T3(KV0, KVN(1024), DM(224, 228, "v[248:251]") DM(232, 236, "v[248:251]")), T4(KVN(2048), KVN(3072), NO),
                            T5("v_mov_b32 v249, %[mm]\n", "s_mov_b32 exec_lo, %[ye]\n s_mov_b32 exec_hi, 0\n global_store_dwordx2 v16, v[248:249], s[72:73] sc1\n s_mov_b64 exec, -1\n", NO),
                            T6(NO, VM(5) TRA(24, 0) TRA(25, 1024), NO), T7(TRB(26, 0) TRB(27, 1024), TRB(28, 2048) TRB(29, 3072), NO))
                   : BODY_OUT, "=&{v[224:231]}"(wf[12]), "=&{v[232:239]}"(wf[13]), "=&{v[240:247]}"(wf[14]) : BODY_IN, "{v15}"(ha), "{v16}"(yo), [ye] "s"(ye)
                   : "memory", "scc", "v248", "v249", "v250", "v251"); TS(24); }
    { BAR_VM(1); SEAM(7);
      const unsigned vm = vw ^ ((vw >> 9) & 48), mm = mb; smo = (u64)(x + (size_t)(tb * 64 + kh * 32) * 2048 + hg * 256 + cbk * 64); TS(17);
      asm volatile(BODY_TXT(T0(WOA(0), NO, NO), T1(WOA(1024), NO, NO), T2(MOE(0, 0), NO, NO), T3(MOE(1024, 32768), NO, NO), T4(NO, NO, NO), T5(NO, NO, NO), T6(NO, NO, NO), T7(VM(0) TRA(30, 0) TRA(31, 1024), NO, NO))
                   : BODY_OUT, "=&{v[248:255]}"(wf[15]) : BODY_IN : "memory", "scc"); TS(25); }
    {
    RELANE(); const unsigned vl = lane * 16;
    u32x4 o0, o1;
    const bool routed = tb == (idx >> 6) && kh == ((idx >> 5) & 1);
    u32x2 xr[4]; for (int q = 0; q < 4; ++q) xr[q] = LD(u32x2, mb + l31 * 64 + ((q ^ ((l31 >> 2) & 3)) * 16) + h2 * 8);
    const f32x2 stt = LD(f32x2, M_ST64 + (tbk * 32 + l31) * 8);
    {
        const u32x8 P = __builtin_shufflevector(PK, PK, 0, 1, 2, 3, 4, 5, 6, 7); f32x4 L = __builtin_bit_cast(f32x4, lo4(LS));
        asm volatile("s_nop 1\n\tv_smfmac_f32_16x16x64_bf16 %0, %1, %2, %3\n\ts_nop 11" : "+v"(L) : "v"(hi4(LS)), "v"(P), "v"(IDX));
        O = mfma32(hi4(V), hi4(P), O);
        const auto l16 = __builtin_amdgcn_permlane16_swap(__float_as_uint(L[0] + L[1]), __float_as_uint(L[2] + L[3]), false, false);
        float lsum = __uint_as_float(__builtin_amdgcn_permlane32_swap(l16[0], l16[0], false, false)[0]);
        const float om = fmaxf(fmaxf(fmaxf(fmaxf(fabsf(O[0]), fabsf(O[1])), fabsf(O[2])), fmaxf(fmaxf(fabsf(O[3]), fabsf(O[4])), fabsf(O[5]))), fmaxf(fmaxf(fmaxf(fabsf(O[6]), fabsf(O[7])), fabsf(O[8])),
                         fmaxf(fmaxf(fmaxf(fabsf(O[9]), fabsf(O[10])), fabsf(O[11])), fmaxf(fmaxf(fmaxf(fabsf(O[12]), fabsf(O[13])), fabsf(O[14])), fabsf(O[15])))));
        if (__builtin_expect(__any(!(lsum <= 1.8446744e19f) || !(om <= 3.0e38f)), 0)) {
            float mr = -INFINITY; lsum = 0.f; for (int i = 0; i < 16; ++i) O[i] = 0.f;
#pragma unroll 1
            for (int t = 0; t < 64; ++t) {
                u32x4 k0, k1, v0, v1;
                asm volatile("global_load_dwordx4 %0, %4, %5 sc1\n\tglobal_load_dwordx4 %1, %4, %5 offset:1024 sc1\n\tglobal_load_dwordx4 %2, %4, %6 offset:2048 sc1\n\tglobal_load_dwordx4 %3, %4, %7 offset:3072 sc1\n\ts_waitcnt vmcnt(0)"
                             : "=&v"(k0), "=&v"(k1), "=&v"(v0), "=&v"(v1) : "v"(vl), "s"(kv + ((t + 62) & 63) * 4096), "s"(kv + ((t + 63) & 63) * 4096), "s"(kv + t * 4096) : "memory");
                f32x16 z; for (int i = 0; i < 16; ++i) z[i] = 0.f;
                f32x16 s = mfma32(k0, lo4(Q), z); s = mfma32(k1, hi4(Q), s);
                float m = s[0]; for (int i = 1; i < 16; ++i) m = fmaxf(m, s[i]);
                m = fmaxf(mr, xmax32(m)); const float al = __builtin_amdgcn_exp2f(mr - m); mr = m;
                u32x8 pp; float ps = 0.f;
                for (int i = 0; i < 8; ++i) { pp[i] = pk2(__builtin_amdgcn_exp2f(s[2 * i] - m), __builtin_amdgcn_exp2f(s[2 * i + 1] - m)); ps += bf_lo(pp[i]) + bf_hi(pp[i]); }
                lsum = lsum * al + xsum32(ps); for (int i = 0; i < 16; ++i) O[i] *= al;
                O = mfma32(v0, lo4(pp), O); O = mfma32(v1, hi4(pp), O);
            }
        }
        const float rl = __builtin_amdgcn_rcpf(lsum);
        for (int i = 0; i < 4; ++i) { o0[i] = pk2(O[2 * i] * rl, O[2 * i + 1] * rl); o1[i] = pk2(O[8 + 2 * i] * rl, O[8 + 2 * i + 1] * rl); }
        asm volatile("global_store_dwordx4 %0, %1, %3 sc1\n\tglobal_store_dwordx4 %0, %2, %3 offset:2048 sc1\n\ts_nop 1"
                     :: "v"(vl), "v"(o0), "v"(o1), "s"(ws + WS_O + (size_t)(tb * 8 + hg) * 16384 + hd * 4096 + tbk * 1024) : "memory");
    }
    u64 yg[14];
    if (routed) for (int i = 0; i < 14; ++i) yg[i] = ld8_sc1(ws + WS_YG + ((h2 * 14 + i) * 1024 + hg * 128 + cbk * 32 + l31) * 8);
    TS(26);
    BAR_LDS();
    TS(27);
    const unsigned pv = w * PRIV;
    LD(u32x4, hg * PRIV + hd * 4096 + tbk * 1024 + vl) = o0; LD(u32x4, hg * PRIV + hd * 4096 + 2048 + tbk * 1024 + vl) = o1;
    unsigned xn2[8], g2[8];
    for (int i = 0; i < 8; ++i) { xn2[i] = ln2(xr[i >> 1][i & 1], stt[0], stt[1]); PIN(xn2[i]); }
    WAIT_VM(0);
    for (int i = 0; i < 14; ++i) asm volatile("" :: "v"(yg[i]));
    if (lane == 0) st8_sc1(ws + WS_FLAG4 + b * 128 + w * 8, seed);
    TS(28);
    if (routed) {
#pragma unroll 1
        for (unsigned spins = 0; spins < SPIN_CAP; ++spins) {
            bool ok = true; for (int i = 0; i < 14; ++i) ok &= (unsigned)(yg[i] >> 32) == tag32;
            if (__all(ok)) break;
            __builtin_amdgcn_s_sleep(2);
            for (int i = 0; i < 14; ++i) yg[i] = ld8_sc1(ws + WS_YG + ((h2 * 14 + i) * 1024 + hg * 128 + cbk * 32 + l31) * 8);
        }
        float acc = 0.f; for (int i = 0; i < 14; ++i) acc += __uint_as_float((unsigned)yg[i]);
        const unsigned o16 = (gkey >> 11) & 0xffffu, rbits = (o16 & 0x8000u) ? (o16 & 0x7fffu) : (~o16 & 0xffffu);
        const float rsel = rb(__builtin_amdgcn_rcpf(1.f + __builtin_amdgcn_exp2f(-1.44269504089f * bf_lo(rbits))));
        LD(float, pv + 16384 + lane * 4) = rb(rb(xsum32(acc)) * rsel);
        for (int q = 0; q < 4; ++q) { const f32x4 y = LD(f32x4, pv + 16384 + q * 32 + h2 * 16);
            for (int i = 0; i < 2; ++i) { const unsigned t = pk2(bf_lo(xn2[2 * q + i]) + y[2 * i], bf_hi(xn2[2 * q + i]) + y[2 * i + 1]); xn2[2 * q + i] = l31 == (idx & 31) ? t : xn2[2 * q + i]; } }
    }
    for (int i = 0; i < 8; ++i) { g2[i] = pk2(silu(bf_lo(xn2[i])), silu(bf_hi(xn2[i]))); PIN(g2[i]); }
    TS(31);
    if (w != hg) {
        const int pb = (((w & 3) * 8 + (tb & 7)) << 3) | ((tb >> 3) * 2 + (w >> 2));
#pragma unroll 1
        for (unsigned spins = 0; spins < SPIN_CAP; ++spins) { if (__all(ld8_sc1(ws + WS_FLAG4 + pb * 128 + (lane & 7) * 8) == seed)) break; __builtin_amdgcn_s_sleep(1); }
        TS(29);
        const char* src = ws + WS_O + (size_t)(tb * 8 + w) * 16384;
        for (int i = 0; i < 4; ++i) dma4_sc1(src + i * 4096, vl, pv + i * 4096);
    }
    BAR_VM(12);
    TS(6);
    f32x16 ao, at; for (int i = 0; i < 16; ++i) { ao[i] = 0.f; at[i] = 0.f; }
    {
        const unsigned oa = kh * 4 * PRIV + vl + kh * 1024, ob = oa ^ 1024;
        u32x4 fr[2][8]; f32x4 pt[4];
#define OAD(g, i) ((g) < 4 ? (((i) & 1) ? oa : ob) + (((g) & 1) * 2 + ((i) >> 2)) * PRIV + (((g) >> 1) * 2 + (((i) >> 1) & 1)) * 2048 : ((g) < 6 ? ob : oa) + ((i) >> 1) * PRIV + (4 + ((g) & 1) * 2 + ((i) & 1)) * 2048)
#define OWJ(g, i) ((g) < 4 ? (((g) & 1) * 2 + ((i) >> 2)) * 8 + ((g) >> 1) * 2 + (((i) >> 1) & 1) : ((i) >> 1) * 8 + 4 + ((g) & 1) * 2 + ((i) & 1))
#define ORD(p, g) do { for (int i = 0; i < 8; ++i) fr[p][i] = LD(u32x4, OAD(g, i)); } while (0)
#define WFJ(j) (((j) & 1) ? hi4(wf[(j) >> 1]) : lo4(wf[(j) >> 1]))
        __builtin_amdgcn_s_setprio(1);
        ORD(0, 0); SBAR();
#pragma unroll
        for (int g = 0; g < 8; ++g) {
            if (g == 1) asm volatile("s_waitcnt vmcnt(8)\n\ts_barrier" ::: "memory");
            if (g == 3) asm volatile("s_waitcnt vmcnt(4)\n\ts_barrier" ::: "memory");
            if (g == 4) asm volatile("s_waitcnt vmcnt(0)\n\ts_barrier" ::: "memory");
            if (g < 7) ORD((g + 1) & 1, g + 1);
            for (int i = 0; i < 8; ++i) {
                if (g < 4 ? (i & 1) : g >= 6) ao = mfma32(WFJ(OWJ(g, i)), fr[g & 1][i], ao); else at = mfma32(WFJ(OWJ(g, i)), fr[g & 1][i], at);
                if (g == 6 && i == 1) { SBAR(); for (int q = 0; q < 4; ++q) LD(f32x4, pv + 16384 + q * 1024 + vl) = (f32x4){at[4 * q], at[4 * q + 1], at[4 * q + 2], at[4 * q + 3]}; __builtin_amdgcn_s_setprio(0); SBAR(); }
                if (g == 7 && i == 3) { SBAR(); BAR_LDS(); for (int q = 0; q < 4; ++q) pt[q] = LD(f32x4, (w ^ 4) * PRIV + 16384 + q * 1024 + vl); SBAR(); } }
            if (g < 6) for (int i = 0; i < 8; ++i) { SGB(0x008, 1); SGB(0x100, 1); }
            SBAR();
        }
        TS(30);
        char* op = out + (size_t)(tb * 64 + kh * 32 + l31) * 2048 + hg * 256 + cbk * 64 + h2 * 16;
        u32x2 o[4];
        for (int q = 0; q < 4; ++q) for (int i = 0; i < 2; ++i) {
            const unsigned s2 = pk2(ao[4 * q + 2 * i] + pt[q][2 * i], ao[4 * q + 2 * i + 1] + pt[q][2 * i + 1]), gg = g2[2 * q + i];
            const unsigned p2 = pk2(bf_lo(s2) * bf_lo(gg), bf_hi(s2) * bf_hi(gg));
            o[q][i] = pk2(bf_lo(xr[q][i]) + bf_lo(p2), bf_hi(xr[q][i]) + bf_hi(p2));
        }
        for (int q = 0; q < 4; q += 2) {
            const auto s0 = __builtin_amdgcn_permlane32_swap(o[q][0], o[q + 1][0], false, false), s1 = __builtin_amdgcn_permlane32_swap(o[q][1], o[q + 1][1], false, false);
            *(u32x4*)(op + q * 16) = (u32x4){s0[0], s1[0], s0[1], s1[1]};
        }
    }
    }
    TS(7);
}

extern "C" void kernel_launch(void* const* d_in, const int* in_sizes, int n_in, void* d_out, int out_size, void* d_ws, size_t ws_size, hipStream_t stream) {
    (void)hipFuncSetAttribute((const void*)fused_block, hipFuncAttributeMaxDynamicSharedMemorySize, LDS_BYTES);
    fused_block<<<256, 512, LDS_BYTES, stream>>>((const char*)d_in[0], (const char*)d_in[1], (const char*)d_in[2], (const char*)d_in[3], (const char*)d_in[4],
                                                 (const char*)d_in[6], (const char*)d_in[7], (const char*)d_in[8], (const char*)d_in[9], (char*)d_out, (char*)d_ws);
}
```

```cpp
#define PRIOV 1
#define FSTAGE 2
#include <hip/hip_runtime.h>
#include <hip/hip_bf16.h>
#include <stdint.h>
#include <type_traits>

typedef unsigned long long u64;
typedef float f32x16 __attribute__((ext_vector_type(16)));
typedef float f32x4 __attribute__((ext_vector_type(4)));
typedef float f32x2 __attribute__((ext_vector_type(2)));
typedef unsigned u32x2 __attribute__((ext_vector_type(2)));
typedef unsigned u32x4 __attribute__((ext_vector_type(4)));
typedef unsigned u32x8 __attribute__((ext_vector_type(8)));
typedef unsigned u32x16 __attribute__((ext_vector_type(16)));
typedef __bf16 bf16x8 __attribute__((ext_vector_type(8)));
typedef __bf16 bf16x2 __attribute__((ext_vector_type(2)));
typedef short s16x4 __attribute__((ext_vector_type(4)));
#define LAS __attribute__((address_space(3)))
#define GAS __attribute__((address_space(1)))
#define DI __device__ __forceinline__
#define LD(T, a) (*(LAS T*)(unsigned)(a))

constexpr unsigned WS_EPOCH = 0, WS_FLAG1 = 4096, WS_STATS = 8192, WS_FLAG4 = 65536, WS_KV = 131072, WS_YG = 393216, WS_GU = 622592, WS_CS = 851968, WS_O = 1u << 20;
constexpr unsigned WS_DBG = 6u << 20;
constexpr unsigned SLOT = 24576, L_X16 = 49152, L_WR = 81920, L_SP = 86016, L_FIN = 87040, L_PA = 73728, L_PC = 88064, L_WKV = 98304, L_QX = 0, L_PRO = 16384, L_CSP = 98304, L_MISC = 155648, LDS_BYTES = 163840;
constexpr unsigned M_CTL = L_MISC, M_ST64 = L_MISC + 256, M_STX = L_MISC + 1280, M_CS = L_MISC + 1536, M_XSEL = L_MISC + 2560, M_H = L_MISC + 3584, M_GU = L_MISC + 4096;
constexpr unsigned KA = 122880, KB = 32768, R2 = 65536, R3 = 0, R4 = 98304, M_GR = 114688, PRIV = 20480;
constexpr int slot_of(int s) { return s % 6; }
constexpr unsigned SPIN_CAP = 1u << 20;
constexpr int CODE_SPAN = 38912;

DI float bf_lo(unsigned v) { return __uint_as_float(v << 16); }
DI float bf_hi(unsigned v) { return __uint_as_float(v & 0xffff0000u); }
DI unsigned pk2(float lo, float hi) { f32x2 v = {lo, hi}; return __builtin_bit_cast(unsigned, __builtin_convertvector(v, bf16x2)); }
DI float rb(float f) { return __uint_as_float((unsigned)__builtin_bit_cast(unsigned short, (__bf16)f) << 16); }
DI float xsum32(float v) { auto r = __builtin_amdgcn_permlane32_swap(__float_as_uint(v), __float_as_uint(v), false, false); return __uint_as_float(r[0]) + __uint_as_float(r[1]); }
DI float xmax32(float v) { auto r = __builtin_amdgcn_permlane32_swap(__float_as_uint(v), __float_as_uint(v), false, false); return fmaxf(__uint_as_float(r[0]), __uint_as_float(r[1])); }
DI f32x16 mfma32(u32x4 a, u32x4 b, f32x16 c) { return __builtin_amdgcn_mfma_f32_32x32x16_bf16(__builtin_bit_cast(bf16x8, a), __builtin_bit_cast(bf16x8, b), c, 0, 0, 0); }
DI f32x4 mfma16(u32x4 a, u32x4 b, f32x4 c) { return __builtin_amdgcn_mfma_f32_16x16x32_bf16(__builtin_bit_cast(bf16x8, a), __builtin_bit_cast(bf16x8, b), c, 0, 0, 0); }
DI u32x2 trd(unsigned a) { return __builtin_bit_cast(u32x2, __builtin_amdgcn_ds_read_tr16_b64_v4i16((LAS s16x4*)a)); }
DI u32x4 trfrag(unsigned a, unsigned step) { u32x2 lo = trd(a), hi = trd(a + step); return (u32x4){lo[0], lo[1], hi[0], hi[1]}; }
DI u32x4 lo4(u32x8 v) { return __builtin_shufflevector(v, v, 0, 1, 2, 3); }
DI u32x4 hi4(u32x8 v) { return __builtin_shufflevector(v, v, 4, 5, 6, 7); }
DI float silu(float v) { return v * __builtin_amdgcn_rcpf(1.f + __builtin_amdgcn_exp2f(-1.44269504089f * v)); }
DI unsigned ln2(unsigned v, float m, float rs) { const unsigned d = pk2(bf_lo(v) - m, bf_hi(v) - m); return pk2(bf_lo(d) * rs, bf_hi(d) * rs); }
DI u64 ld8_sc1(const void* p) { return __hip_atomic_load((GAS const u64*)p, __ATOMIC_RELAXED, __HIP_MEMORY_SCOPE_AGENT); }
DI void st8_sc1(void* p, u64 v) { __hip_atomic_store((GAS u64*)p, v, __ATOMIC_RELAXED, __HIP_MEMORY_SCOPE_AGENT); }
DI int fresh_lane() { int l; asm volatile("v_mbcnt_lo_u32_b32 %0, -1, 0\n\tv_mbcnt_hi_u32_b32 %0, -1, %0" : "=v"(l)); return l; }
#define RELANE() const int lane = fresh_lane(), l15 = lane & 15, g4 = lane >> 4, l31 = lane & 31, h2 = lane >> 5; (void)l15; (void)g4; (void)l31; (void)h2
DI u64 mix(unsigned lo, unsigned hi) { u64 z = (((u64)hi << 32) | lo) + 0x9E3779B97F4A7C15ull; z = (z ^ (z >> 30)) * 0xBF58476D1CE4E5B9ull; z = (z ^ (z >> 27)) * 0x94D049BB133111EBull; return z ^ (z >> 31); }
DI unsigned rowmax16(unsigned k) { k = max(k, (unsigned)__builtin_amdgcn_update_dpp(0, (int)k, 0x128, 0xf, 0xf, false)); k = max(k, (unsigned)__builtin_amdgcn_update_dpp(0, (int)k, 0x124, 0xf, 0xf, false));
    k = max(k, (unsigned)__builtin_amdgcn_update_dpp(0, (int)k, 0x122, 0xf, 0xf, false)); return max(k, (unsigned)__builtin_amdgcn_update_dpp(0, (int)k, 0x121, 0xf, 0xf, false)); }
DI void dma1(const void* sbase, unsigned voff, unsigned m0v) { asm volatile("s_mov_b32 m0, %2\n\ts_nop 0\n\tglobal_load_lds_dwordx4 %0, %1" :: "v"(voff), "s"(sbase), "s"(m0v) : "memory"); }
DI void dma1_sc1(const void* sbase, unsigned voff, unsigned m0v) { asm volatile("s_mov_b32 m0, %2\n\ts_nop 0\n\tglobal_load_lds_dwordx4 %0, %1 sc1" :: "v"(voff), "s"(sbase), "s"(m0v) : "memory"); }
DI void dma4(const void* sbase, unsigned voff, unsigned m0v) {
    asm volatile("s_mov_b32 m0, %2\n\ts_nop 0\n\tglobal_load_lds_dwordx4 %0, %1\n\tglobal_load_lds_dwordx4 %0, %1 offset:1024\n\tglobal_load_lds_dwordx4 %0, %1 offset:2048\n\tglobal_load_lds_dwordx4 %0, %1 offset:3072"
                 :: "v"(voff), "s"(sbase), "s"(m0v) : "memory"); }
DI void dma4_sc1(const void* sbase, unsigned voff, unsigned m0v) {
    asm volatile("s_mov_b32 m0, %2\n\ts_nop 0\n\tglobal_load_lds_dwordx4 %0, %1 sc1\n\tglobal_load_lds_dwordx4 %0, %1 offset:1024 sc1\n\tglobal_load_lds_dwordx4 %0, %1 offset:2048 sc1\n\tglobal_load_lds_dwordx4 %0, %1 offset:3072 sc1"
                 :: "v"(voff), "s"(sbase), "s"(m0v) : "memory"); }
#define BAR_ALL() asm volatile("s_waitcnt vmcnt(0) lgkmcnt(0)\n\ts_barrier" ::: "memory")
#define BAR_LDS() asm volatile("s_waitcnt lgkmcnt(0)\n\ts_barrier" ::: "memory")
#define BAR_VM(n) asm volatile("s_waitcnt vmcnt(" #n ") lgkmcnt(0)\n\ts_barrier" ::: "memory")
#define WAIT_VM(n) asm volatile("s_waitcnt vmcnt(" #n ")" ::: "memory")
#define PIN(v) asm volatile("" : "+v"(v))
#define SBAR() __builtin_amdgcn_sched_barrier(0)
#define SGB(m, n) __builtin_amdgcn_sched_group_barrier(m, n, 0)
#define TS(i) do { } while (0)

#define EX(A,i) "v_exp_f32 v[" #A "+" #i "], v[" #A "+" #i "]\n"
#define E4(A,a,b,c,d) EX(A,a) EX(A,b) EX(A,c) EX(A,d)
#define CV(p,A,i) "v_cvt_pk_bf16_f32 v[104+" #p "], v[" #A "+" #i "], v[" #A "+" #i "+1]\n"
#define RDX(dst, o, k) "ds_read_b128 " dst ", v22 offset:" o "+" #k "\n"
#define M32(x) "v_mfma_f32_32x32x16_bf16 " x "\n"
#define M16(x) "v_mfma_f32_16x16x32_bf16 " x "\n"
#define TILE(A, B, o, X1, X2, Y) \
    M32("v[" #B ":" #B "+15], v[112:115], v[96:99], v[64:79]") RDX("v[112:115]", o, 0) X1 E4(A,0,1,2,3) \
    M32("v[80:95], v[124:127], v[108:111], v[80:95]") RDX("v[124:127]", o, 3072) E4(A,4,5,6,7) \
    "v_smfmac_f32_16x16x64_bf16 v[24:27], v[28:31], v[104:111], v23\n" "s_waitcnt lgkmcnt(2)\n" CV(0,A,0) CV(1,A,2) CV(2,A,4) CV(3,A,6) \
    M32("v[" #B ":" #B "+15], v[116:119], v[100:103], v[" #B ":" #B "+15]") RDX("v[116:119]", o, 1024) X2 E4(A,8,9,10,11) \
    M32("v[80:95], v[120:123], v[104:107], v[80:95]") RDX("v[120:123]", o, 2048) E4(A,12,13,14,15) Y \
    "s_waitcnt lgkmcnt(2)\n" CV(4,A,8) CV(5,A,10) CV(6,A,12) CV(7,A,14)
#define NO ""
#define WOA(k) "s_add_u32 m0, %[ma], " #k "\n s_add_u32 s70, s70, 0x8000\n s_addc_u32 s71, s71, 0\n global_load_lds_dwordx4 v20, s[70:71]\n"
#define WOB(k) "s_add_u32 m0, %[mb], " #k "\n s_add_u32 s70, s70, 0x8000\n s_addc_u32 s71, s71, 0\n global_load_lds_dwordx4 v20, s[70:71]\n"
#define MOE(k, st) "s_add_u32 m0, %[mm], " #k "\n s_add_u32 s72, s72, " #st "\n s_addc_u32 s73, s73, 0\n global_load_lds_dwordx4 v19, s[72:73]\n"
#define KV0 "s_mov_b32 m0, %[mk]\n s_nop 0\n global_load_lds_dwordx4 v21, %[sk] sc1\n"
#define KVN(k) "global_load_lds_dwordx4 v21, %[sk] offset:" #k " sc1\n"
#define GRD "s_mov_b32 m0, %[mm]\n s_nop 0\n global_load_lds_dwordx4 v21, s[72:73] sc1\n"
#define TR2(d, a, k, st) "ds_read_b64_tr_b16 v[" #d ":" #d "+1], " a " offset:" #k "\n ds_read_b64_tr_b16 v[" #d "+2:" #d "+3], " a " offset:" #k "+" #st "\n"
#define TRA(j, k) TR2(128+4*j, "v18", k, 512)
#define TRB(j, k) TR2(128+4*j, "v17", k, 512)
#define B64(d, a, k) "ds_read_b64 v[" #d ":" #d "+1], " a " offset:" #k "\n"
#define VM(n) "s_waitcnt vmcnt(" #n ")\n"
#define BODY_TXT(T0, T1, T2, T3, T4, T5, T6, T7) "s_nop 1\n s_setprio 3\n" T0 T1 T2 T3 T4 "s_setprio 2\n" T5 T6 "s_setprio 1\n" T7 "s_waitcnt lgkmcnt(0)\n s_nop 7\n"
#define PH "s_setprio 0\n"
#define T0(a,b,c) TILE(32,48,"0",a,b,c)
#define T1(a,b,c) TILE(48,32,"4096",a,b,c)
#define T2(a,b,c) TILE(32,48,"8192",a,b,c)
#define T3(a,b,c) TILE(48,32,"12288",a,b,c)
#define T4(a,b,c) TILE(32,48,"16384",a,b,c)
#define T5(a,b,c) TILE(48,32,"20480",a,b,c)
#define T6(a,b,c) TILE(32,48,"24576",a,b,c)
#define T7(a,b,c) TILE(48,32,"28672",a,PH b,c)
#define BODY_OUT "+{v[32:47]}"(SA), "=&{v[48:63]}"(SB), "+{v[80:95]}"(O), "+{v[24:31]}"(LS), "+{v[104:119]}"(PK), "+{v[120:127]}"(V), "+{s[70:71]}"(swo), "+{s[72:73]}"(smo)
#define BODY_IN "{v[64:79]}"(NEG), "{v[96:103]}"(Q), "{v23}"(IDX), "{v22}"(ad), "{v21}"(vl), "{v20}"(vw), "{v19}"(vm), "{v18}"(ta), "{v17}"(tb_), \
      [ma] "s"(ma), [mb] "s"(mb), [mm] "s"(mm), [mk] "s"(mk), [sk] "s"(sk)
#define C8(a,b,c,d,e,f,g,h) "v" #a, "v" #b, "v" #c, "v" #d, "v" #e, "v" #f, "v" #g, "v" #h

__global__ __launch_bounds__(512, 2) void fused_block(const char* __restrict__ x, const char* __restrict__ wq, const char* __restrict__ wk, const char* __restrict__ wv,
                                                      const char* __restrict__ wo, const char* __restrict__ wr, const char* __restrict__ wg, const char* __restrict__ wu,
                                                      const char* __restrict__ wd, char* __restrict__ out, char* __restrict__ ws) {
    const int tid = threadIdx.x, lane = tid & 63, w = __builtin_amdgcn_readfirstlane(tid >> 6);
    const int b = blockIdx.x, xcd = b & 7, r = b >> 3, tb = (xcd >> 1) * 8 + (r & 7), hg = (xcd & 1) * 4 + (r >> 3);
    const int isV = r >> 4, sub = (r >> 2) & 3, T0 = (xcd * 4 + (r & 3)) * 64 + sub * 16;
    const int l15 = lane & 15, g4 = lane >> 4, l31 = lane & 31, h2 = lane >> 5;
    const int hd = w & 3, tbk = w >> 2;
    const unsigned vl = lane * 16;
    TS(0);
    {
        u64 pc; asm volatile("s_getpc_b64 %0" : "=s"(pc));
        asm volatile("s_mov_b32 m0, %2\n\ts_mov_b64 exec, 3\n\tglobal_load_lds_dword %0, %1\n\ts_mov_b64 exec, -1"
                     :: "v"(lane * 128), "s"((pc & ~127ull) + min((r * 8 + w) * 256, CODE_SPAN - 256)), "s"(w < 4 ? L_X16 + w * 8192 : L_WKV + (w - 4) * 16384) : "memory");
    }
    const int c0 = r & 7, rm = min(r, 27), mi = xcd * 28 + rm; const char* kv = ws + WS_KV;
    f32x16 qa; float csum;
    u32x8 LS, PZ; f32x16 O; unsigned IDX, vw, vm0, ta, tb_;
    const unsigned ma = R4 + w * 2048, mb = R3 + w * 4096;
#define SETUP() do { const unsigned one_ = 0x3F803F80u; LS = (u32x8){0u, 0u, 0u, 0u, l15 < 2 ? one_ : 0u, l15 < 2 ? one_ : 0u, (l15 & 14) == 2 ? one_ : 0u, (l15 & 14) == 2 ? one_ : 0u}; \
        IDX = (lane & 1) ? 0xEEEEEEEEu : 0x44444444u; vw = (lane >> 2) * 2048 + (lane & 3) * 16; vm0 = (lane >> 3) * 7168 + (((lane & 7) ^ (g4 << 1)) << 4); \
        ta = ma + (4 * h2 + (l15 >> 2)) * 64 + (g4 & 1) * 32 + (lane & 3) * 8; tb_ = ta + (mb - ma); for (int i = 0; i < 16; ++i) O[i] = 0.f; PZ = (u32x8){0u, 0u, 0u, 0u, 0u, 0u, 0u, 0u}; PIN(O); PIN(PZ); PIN(LS); PIN(IDX); PIN(vw); PIN(vm0); PIN(ta); PIN(tb_); } while (0)
    if (__builtin_expect(w < 4, 1)) {
        __builtin_amdgcn_s_setprio(3);
        u32x2 ev; u32x4 xq[8], aw[8], sel, ones; unsigned wa, wa2, spa, kvo, wb[3][2], xb_[3][4];
        {
            const char* xs = x + (size_t)T0 * 2048 + w * 512;
            for (int i = 0; i < 8; ++i) dma1(xs, (2 * i + h2) * 2048 + ((l31 ^ (2 * i + h2)) << 4), L_X16 + w * 8192 + i * 1024);
            dma1(wr + w * 512, l31 * 16, L_WR + w * 1024);
            asm volatile("s_mov_b64 exec, 1\n\tglobal_load_dwordx2 %0, %1, %2 sc1\n\ts_mov_b64 exec, -1" : "=v"(ev) : "v"(0), "s"(ws));
            SETUP();
            wa = L_WKV + w * 16384 + (8 * g4 + (l15 >> 2)) * 64 + (g4 & 1) * 32 + (lane & 3) * 8; wa2 = wa ^ 32; spa = L_SP + ((l15 + (isV ? 4 * g4 : 0)) & 15) * 16;
            const int kt = T0 >> 5, sh = sub & 1;
            kvo = isV ? ((kt - 1 + sh) & 63) * 4096 + 2048 + sh * 1024 + ((g4 & 1) * 32 + w * 16 + l15) * 16 + (g4 >> 1) * 8
                      : ((kt - 2) & 63) * 4096 + w * 1024 + ((g4 & 1) * 32 + sh * 16 + l15) * 16 + (g4 >> 1) * 8;
            const unsigned sv = (((l15 >> 2) & 1) == (g4 & 1)) ? 0x3F803F80u : 0u, xq_a = l31 * 128 + ((h2 ^ ((l31 >> 1) & 7)) << 4);
            const unsigned wq_a = 8192 + (8 * h2 + (l15 >> 2)) * 256 + ((hd ^ ((lane >> 2) & 3)) << 6) + (g4 & 1) * 32 + (lane & 3) * 8;
            sel = (u32x4){sv, sv, sv, sv}; ones = (u32x4){0x3F803F80u, 0x3F803F80u, 0x3F803F80u, 0x3F803F80u};
            for (int i = 0; i < 3; ++i) {
                wb[i][0] = wq_a + i * 2 * SLOT + (tb & 1) * 8192; wb[i][1] = wq_a + i * 2 * SLOT + (~tb & 1) * 8192; PIN(wb[i][0]); PIN(wb[i][1]);
                for (int ks = 0; ks < 4; ++ks) { xb_[i][ks] = (xq_a ^ (ks * 32) ^ ((tb & 1) * 64)) + i * 2 * SLOT; PIN(xb_[i][ks]); } }
            const unsigned oth = l15 == 1 ? 0x3F803F80u : 0u;
            for (int s = 0; s < 8; ++s) { aw[s] = (u32x4){oth, oth, oth, oth}; PIN(aw[s]); }
            PIN(kvo); PIN(wa); PIN(wa2); PIN(spa); PIN(ones); PIN(sel);
            WAIT_VM(1);
            const unsigned xa = L_X16 + w * 8192 + l15 * 512 + ((g4 ^ l15) << 4);
            for (int s = 0; s < 8; ++s) xq[s] = LD(u32x4, xa ^ (s * 64));
            if (l15 == 0) for (int s = 0; s < 8; ++s) aw[s] = LD(u32x4, L_WR + w * 1024 + s * 64 + g4 * 16);
        }
        TS(8);
        const f32x4 z4 = {0, 0, 0, 0};
        f32x4 s1 = z4, gr = z4, sw = z4;
        for (int s = 0; s < 8; ++s) { gr = mfma16(xq[s], xq[s], gr); s1 = mfma16(aw[s], xq[s], s1); sw = mfma16(aw[s], ones, sw); }
        { float d = (l15 & 2) ? ((l15 & 1) ? gr[3] : gr[2]) : ((l15 & 1) ? gr[1] : gr[0]); d = g4 == (l15 >> 2) ? d : 0.f;
          const auto e16 = __builtin_amdgcn_permlane16_swap(__float_as_uint(d), __float_as_uint(d), false, false); d = xsum32(__uint_as_float(e16[0]) + __uint_as_float(e16[1]));
          if (lane < 16) LD(f32x4, L_SP + w * 256 + vl) = (f32x4){s1[1], d, s1[0], sw[0]}; }
        TS(9);
        BAR_LDS();
        f32x4 m4, r4; unsigned key = 0;
        {
            f32x4 p[4]; for (int q = 0; q < 4; ++q) p[q] = LD(f32x4, spa + q * 256);
            const f32x4 t = (p[0] + p[1]) + (p[2] + p[3]);
            const float mean = t[0] * (1.f / 1024.f), var = fmaxf(t[1] * (1.f / 1024.f) - mean * mean, 0.f);
            const float mrow = rb(mean), rsrow = __builtin_amdgcn_rcpf(rb(__builtin_amdgcn_sqrtf(rb(rb(var) + 1e-5f))));
            const int mi_ = (int)__float_as_uint(mrow), ri_ = (int)__float_as_uint(rsrow);
#define BC(v, n) __uint_as_float((unsigned)__builtin_amdgcn_update_dpp(0, v, 0x150 + (n), 0xf, 0xf, false))
            m4 = (f32x4){BC(mi_, 0), BC(mi_, 1), BC(mi_, 2), BC(mi_, 3)}; r4 = (f32x4){BC(ri_, 0), BC(ri_, 1), BC(ri_, 2), BC(ri_, 3)};
            if (!isV) { m4 = (f32x4){mrow, mrow, mrow, mrow}; r4 = (f32x4){rsrow, rsrow, rsrow, rsrow};
                if (w == 2) { const unsigned u = __float_as_uint(rb((t[2] - mrow * t[3]) * rsrow)) >> 16;
                    key = rowmax16(((((u & 0x8000u) ? ~u : (u | 0x8000u)) & 0xffffu) << 11) | (2047 - (T0 + l15))); } }
        }
        PIN(m4); PIN(r4);
        TS(34);
        BAR_LDS();
        TS(33);
        {
            const f32x4 cs = mfma16(sel, trfrag(wb[0][0] + 4096, 1024), mfma16(sel, trfrag(wb[0][0], 1024), z4));
            asm volatile("s_nop 7\n\ts_mov_b32 exec_hi, 0\n\tglobal_store_dword %0, %1, %2 sc1\n\ts_mov_b64 exec, -1" :: "v"(lane * 4), "v"(cs[0]), "s"(ws + WS_CS + (hg * 32 + tb) * 512 + w * 128) : "memory");
        }
        auto kvpart = [&](auto V_) {
            constexpr bool V = decltype(V_)::value;
            u32x4 w0[8], w1[8]; f32x4 a0 = z4, a1 = z4, c0_ = z4, c1_ = z4;
            for (int s = 0; s < 3; ++s) { w0[s] = trfrag(wa + s * 2048, 256); w1[s] = trfrag(wa2 + s * 2048, 256); }
            SBAR();
#pragma unroll
            for (int s = 0; s < 8; ++s) {
                if (s < 5) { w0[s + 3] = trfrag(wa + (s + 3) * 2048, 256); w1[s + 3] = trfrag(wa2 + (s + 3) * 2048, 256); }
                if (V) { a0 = mfma16(xq[s], w0[s], a0); c0_ = mfma16(ones, w0[s], c0_); a1 = mfma16(xq[s], w1[s], a1); c1_ = mfma16(ones, w1[s], c1_); }
                else { a0 = mfma16(w0[s], xq[s], a0); c0_ = mfma16(w0[s], ones, c0_); a1 = mfma16(w1[s], xq[s], a1); c1_ = mfma16(w1[s], ones, c1_); }
                if (s < 5) for (int i = 0; i < 4; ++i) { SGB(0x008, 1); SGB(0x100, 1); }
                SBAR();
            }
            LD(f32x4, L_PA + w * 2048 + vl) = (a0 - m4 * c0_) * r4; LD(f32x4, L_PA + w * 2048 + 1024 + vl) = (a1 - m4 * c1_) * r4;
        };
        if (isV) kvpart(std::true_type{}); else kvpart(std::false_type{});
        asm volatile("s_waitcnt vmcnt(1)" : "+v"(ev) :: "memory");
        if (tid == 192) LD(u32x2, M_CTL) = ev;
        TS(35);
        BAR_LDS();
        TS(1);
        if (w < 2) {
            f32x4 p[4]; for (int i = 0; i < 4; ++i) p[i] = LD(f32x4, L_PA + i * 2048 + w * 1024 + vl);
            const f32x4 a = (p[0] + p[1]) + (p[2] + p[3]);
            st8_sc1(ws + WS_KV + kvo, ((u64)pk2(a[2], a[3]) << 32) | pk2(a[0], a[1]));
        } else if (w == 2 && !isV && lane < 16) st8_sc1(ws + WS_STATS + (T0 + l15) * 8, ((u64)__float_as_uint(r4[0]) << 32) | __float_as_uint(m4[0]));
        f32x16 qb; qa = O; qb = O;
        u32x4 af[2][4], b0[2][4], b1[2][4];
#define QRD(p, s) do { const unsigned sl_ = (slot_of(s) & 1) * SLOT; const int bi_ = slot_of(s) >> 1; for (int ks = 0; ks < 4; ++ks) { \
            af[p][ks] = trfrag(wb[bi_][ks >> 1] + sl_ + (ks & 1) * 4096, 1024); b1[p][ks] = LD(u32x4, xb_[bi_][ks] + sl_ + 4096); b0[p][ks] = LD(u32x4, xb_[bi_][ks] + sl_); } } while (0)
#pragma unroll
        for (int s = 0; s < 16; ++s) {

            if (s == FSTAGE || s == 10) WAIT_VM(0);
            if (!(s & 1)) { BAR_LDS(); if (s == 0) TS(37); if (s == 2) TS(39); if (s == 4) TS(41); if (s == 6) TS(43); if (s == 8) TS(45); if (s == 10) TS(47); if (s == 12) TS(49); if (s == 14) TS(51); }
            if (s == 0) { QRD(0, 0); BAR_LDS(); }
            if (s == FSTAGE && tid == 128) { const u32x2 e = LD(u32x2, M_CTL); st8_sc1(ws + WS_FLAG1 + b * 8, (mix(e[0], e[1]) & ~0xFFFFFFFull) | key); }
            if (s == 8 && w >= 2) dma1_sc1(ws + WS_FLAG1 + (w - 2) * 1024, vl, M_GU + (w - 2) * 1024);
            if (s < 15) {
                QRD((s + 1) & 1, s + 1);
                for (int ks = 0; ks < 4; ++ks) { qb = mfma32(af[s & 1][ks], b1[s & 1][ks], qb); qa = mfma32(af[s & 1][ks], b0[s & 1][ks], qa); }
                for (int i = 0; i < 8; ++i) { SGB(0x008, 1); SGB(0x100, 2); }
            } else {
                for (int ks = 0; ks < 4; ++ks) qb = mfma32(af[1][ks], b1[1][ks], qb);
                SBAR();
                for (int ks = 0; ks < 4; ++ks) { qa = mfma32(af[1][ks], b0[1][ks], qa); LD(f32x4, L_QX + w * 4096 + ks * 1024 + vl) = (f32x4){qb[4 * ks], qb[4 * ks + 1], qb[4 * ks + 2], qb[4 * ks + 3]}; }
            }
            SBAR();
        }
        TS(2);
        BAR_LDS();
        csum = LD(float, M_CS + w * 256 + lane * 4);
    } else {
        const int lw = w - 4, koff = tb >> 1;
        __builtin_amdgcn_s_sleep(10);
        { const char* wkv = (isV ? wv : wk) + lw * 16384;
          for (int i = 0; i < 4; ++i) dma4(wkv + i * 4096, vl ^ (h2 << 5), L_WKV + lw * 16384 + i * 4096); }
        unsigned vx[2], vq[4];
        for (int i = 0; i < 2; ++i) { const int row = lw * 16 + i * 8 + (lane >> 3); vx[i] = row * 2048 + (((lane & 7) ^ ((row >> 1) & 7)) << 4); }
        for (int i = 0; i < 4; ++i) vq[i] = (lw * 16 + i * 4 + g4) * 2048 + ((((lane >> 2) & 3) ^ g4) << 6) + ((lane & 3) << 4);
        const char* xb = x + (size_t)tb * 64 * 2048; const char* qb_ = wq + hg * 256;
#define RING_X(s) asm volatile("s_mov_b32 m0, %3\n\ts_nop 0\n\tglobal_load_lds_dwordx4 %0, %2\n\ts_add_u32 m0, m0, 1024\n\ts_nop 0\n\tglobal_load_lds_dwordx4 %1, %2" \
                     :: "v"(vx[0]), "v"(vx[1]), "s"(xb + (((s) + koff) & 15) * 128), "s"(slot_of(s) * SLOT + lw * 2048) : "memory", "scc")
#define RING_W(s) asm volatile("s_mov_b32 m0, %5\n\ts_nop 0\n\tglobal_load_lds_dwordx4 %0, %4\n\ts_add_u32 m0, m0, 1024\n\ts_nop 0\n\tglobal_load_lds_dwordx4 %1, %4\n\t" \
                     "s_add_u32 m0, m0, 1024\n\ts_nop 0\n\tglobal_load_lds_dwordx4 %2, %4\n\ts_add_u32 m0, m0, 1024\n\ts_nop 0\n\tglobal_load_lds_dwordx4 %3, %4" \
                     :: "v"(vq[0]), "v"(vq[1]), "v"(vq[2]), "v"(vq[3]), "s"(qb_ + (size_t)(((s) + koff) & 15) * 131072), "s"(slot_of(s) * SLOT + 8192 + lw * 4096) : "memory", "scc")
#define RING_ISSUE(s) do { RING_X(s); RING_W(s); } while (0)
        TS(32); RING_ISSUE(0); TS(8);
        BAR_LDS(); RING_ISSUE(1); RING_ISSUE(2);
        BAR_VM(12); TS(33); RING_W(3); TS(35);
        BAR_LDS(); TS(1); RING_ISSUE(4); RING_ISSUE(5); TS(36);
        BAR_VM(16); TS(37); RING_X(3);
        BAR_LDS(); RING_ISSUE(6); TS(38);
        BAR_VM(6); TS(39); RING_ISSUE(7); RING_ISSUE(8); TS(40);
        BAR_VM(12); TS(41); RING_ISSUE(9); RING_ISSUE(10); TS(42);
        BAR_VM(12); TS(43); RING_ISSUE(11); RING_ISSUE(12); TS(44);
        BAR_VM(12); TS(45);
        RING_ISSUE(13); RING_ISSUE(14); TS(46);
        const u32x2 e = LD(u32x2, M_CTL); const u64 tag36 = mix(e[0], e[1]) & ~0xFFFFFFFull;
        BAR_VM(12); TS(47); RING_ISSUE(15);
        u64 f[4]; { const u32x4 f0 = LD(u32x4, M_GU + vl), f1 = LD(u32x4, M_GU + 1024 + vl); f[0] = ((u64)f0[1] << 32) | f0[0]; f[1] = ((u64)f0[3] << 32) | f0[2]; f[2] = ((u64)f1[1] << 32) | f1[0]; f[3] = ((u64)f1[3] << 32) | f1[2]; }
#define FLAGS_OK() __all((((f[0] ^ tag36) | (f[1] ^ tag36) | (f[2] ^ tag36) | (f[3] ^ tag36)) >> 28) == 0)
        bool early = FLAGS_OK();
        if (!early) { for (int i = 0; i < 4; ++i) f[i] = ld8_sc1(ws + WS_FLAG1 + (i * 64 + lane) * 8); BAR_VM(6); early = FLAGS_OK(); }
        else BAR_VM(6);
        TS(49);
        if (!early) {
            BAR_VM(0);
#pragma unroll 1
            for (unsigned spins = 0; spins < SPIN_CAP; ++spins) {
                for (int i = 0; i < 4; ++i) f[i] = ld8_sc1(ws + WS_FLAG1 + (i * 64 + lane) * 8);
                if (FLAGS_OK()) break;
                __builtin_amdgcn_s_sleep(2);
            }
            PIN(f[0]); PIN(f[1]); PIN(f[2]); PIN(f[3]);
        }
        TS(50);
        for (int i = 0; i < 4; ++i) dma1_sc1(ws + WS_CS + hg * 16384 + i * 4096 + lw * 128, (lane >> 3) * 512 + (lane & 7) * 16, L_CSP + lw * 4096 + i * 1024);
        if (lw < 2) { dma4_sc1(kv + ((c0 * 8 + 62 + lw) & 63) * 4096, vl, L_PRO + lw * 4096); if (early) BAR_VM(8); }
        else if (lw == 2) { dma1_sc1(ws + WS_STATS + tb * 512, l31 * 16, M_ST64); if (early) BAR_VM(5); }
        else {
            unsigned key = rowmax16(max(max((unsigned)f[0] & 0xFFFFFFFu, (unsigned)f[1] & 0xFFFFFFFu), max((unsigned)f[2] & 0xFFFFFFFu, (unsigned)f[3] & 0xFFFFFFFu)));
            key = max(max((unsigned)__builtin_amdgcn_readlane(key, 0), (unsigned)__builtin_amdgcn_readlane(key, 16)), max((unsigned)__builtin_amdgcn_readlane(key, 32), (unsigned)__builtin_amdgcn_readlane(key, 48)));
            const int ix = 2047 - (key & 2047);
            dma1(x + (size_t)ix * 2048 + (mi & 3) * 512, l31 * 16, M_XSEL);
            asm volatile("s_mov_b32 m0, %2\n\ts_nop 0\n\tglobal_load_lds_dword %0, %1 sc1" :: "v"((lane & 1) * 4), "s"(ws + WS_STATS + ix * 8), "s"(M_STX) : "memory");
            if (early) BAR_VM(6);
            if (lane == 0) { LD(unsigned, M_CTL + 8) = key; if (b == 0) st8_sc1(ws + WS_EPOCH, (((u64)e[1] << 32) | e[0]) + 1); }
        }
        TS(51);
        dma4_sc1(kv + c0 * 32768 + lw * 8192, vl, KA + lw * 8192); dma4_sc1(kv + c0 * 32768 + lw * 8192 + 4096, vl, KA + lw * 8192 + 4096);
        SETUP();
        WAIT_VM(8);
        { float p[16]; for (int i = 0; i < 16; ++i) p[i] = LD(float, L_CSP + lw * 4096 + (h2 * 16 + i) * 128 + l31 * 4);
          csum = xsum32((((p[0] + p[1]) + (p[2] + p[3])) + ((p[4] + p[5]) + (p[6] + p[7]))) + (((p[8] + p[9]) + (p[10] + p[11])) + ((p[12] + p[13]) + (p[14] + p[15]))));
          LD(float, M_CS + lw * 256 + lane * 4) = csum; }
        TS(2);
        __builtin_amdgcn_s_setprio(3);
        BAR_LDS();
        for (int q = 0; q < 4; ++q) { const f32x4 t = LD(f32x4, L_QX + hd * 4096 + q * 1024 + vl); for (int i = 0; i < 4; ++i) qa[4 * q + i] = t[i]; }
    }
    TS(3);
    u32x8 Q, V; u32x16 PK; f32x16 SA, SB, NEG;
    const u32x4 ctl = LD(u32x4, M_CTL);
    {
        const f32x2 st = LD(f32x2, M_ST64 + (tbk * 32 + l31) * 8);
        const u32x4 k00 = LD(u32x4, L_PRO + vl), k01 = LD(u32x4, L_PRO + 1024 + vl), k10 = LD(u32x4, L_PRO + 4096 + vl), k11 = LD(u32x4, L_PRO + 5120 + vl), v00 = LD(u32x4, L_PRO + 6144 + vl);
        const float c1 = rb(csum), c2 = rb(csum - c1), c3 = csum - c1 - c2;
        const u32x4 ca = {h2 ? 0u : pk2(c1, c2), h2 ? 0u : pk2(c3, 0.f), 0u, 0u}, cb = {pk2(-st[0], -st[0]), pk2(-st[0], 0.f), 0u, 0u};
        qa = mfma32(ca, cb, qa);
        const float qs = st[1] * (0.17677669529663687f * 1.4426950408889634f);
        for (int i = 0; i < 8; ++i) Q[i] = pk2(qa[2 * i] * qs, qa[2 * i + 1] * qs);
        f32x16 s0 = mfma32(k00, lo4(Q), O); s0 = mfma32(k01, hi4(Q), s0);
        for (int i = 0; i < 4; ++i) { PK[i] = PZ[i]; PK[4 + i] = PZ[4 + i]; PK[8 + i] = k10[i]; PK[12 + i] = k11[i]; V[i] = v00[i]; V[4 + i] = k00[i]; }
        float m = fmaxf(fmaxf(fmaxf(fmaxf(s0[0], s0[1]), s0[2]), fmaxf(fmaxf(s0[3], s0[4]), s0[5])), fmaxf(fmaxf(fmaxf(s0[6], s0[7]), s0[8]), fmaxf(fmaxf(s0[9], s0[10]), s0[11])));
        m = xmax32(fmaxf(fmaxf(m, fmaxf(s0[12], s0[13])), fmaxf(s0[14], s0[15])));
        NEG = __builtin_amdgcn_mfma_f32_32x32x2f32(h2 ? 0.f : 1.f, -m, O, 0, 0, 0);
        for (int i = 0; i < 16; ++i) SA[i] = s0[i] - m;
        PIN(NEG); PIN(SA);
    }
    TS(4);
    const u64 seed = mix(__builtin_amdgcn_readfirstlane(ctl[0]), __builtin_amdgcn_readfirstlane(ctl[1]));
    const unsigned tag32 = (unsigned)seed ^ 0x5bd1e995u, gkey = __builtin_amdgcn_readfirstlane(ctl[2]);
    const int idx = 2047 - (gkey & 2047), cbk = hd, kh = tbk;
    const bool moe = r < 28;
    u64 swo = (u64)(wo + (size_t)kh * 512 * 2048 + hg * 256 + cbk * 64) - 0x8000, smo;
    u32x8 wf[16];
#define SEAM(c) const unsigned ad = (((c) & 1) ? KB : KA) + vl, mk = (((c) & 1) ? KA : KB) + w * 4096; const char* sk = kv + ((c0 + (c) + 1) & 7) * 32768 + w * 4096
#define MROW() (16 * (g4 >> 1) + 4 * (g4 & 1) + (l15 >> 2))
#define MSWZ() (2 * (g4 & 1) + (l15 >> 3))
    { BAR_VM(0); SEAM(0); const unsigned vm = vm0, mm = R2 + w * 4096; smo = (u64)(wg + (size_t)((mi & 3) * 256 + w * 32) * 7168 + (mi >> 2) * 128); TS(10);
      asm volatile(BODY_TXT(T0(WOA(0), NO, NO), T1(WOA(1024), NO, NO), T2(KV0, KVN(1024), NO), T3(KVN(2048), KVN(3072), NO), T4(MOE(0, 0), MOE(1024, 57344), NO), T5(MOE(2048, 57344), MOE(3072, 57344), NO),
                            T6(NO, NO, NO), T7(VM(8) TRA(0, 0) TRA(1, 1024), NO, NO))
                   : BODY_OUT, "=&{v[128:135]}"(wf[0]) : BODY_IN : "memory", "scc"); TS(18); }
    { BAR_VM(4); SEAM(1); const unsigned vm = vm0, mm = mb; smo = (u64)(wu + (size_t)((mi & 3) * 256 + w * 32) * 7168 + (mi >> 2) * 128);
      TS(11);
      asm volatile(BODY_TXT(T0(WOA(0), NO, NO), T1(WOA(1024), NO, NO), T2(KV0, KVN(1024), NO), T3(KVN(2048), KVN(3072), NO), T4(MOE(0, 0), MOE(1024, 57344), NO), T5(MOE(2048, 57344), MOE(3072, 57344), NO),
                            T6(NO, NO, NO), T7(VM(8) TRA(2, 0) TRA(3, 1024), NO, NO))
                   : BODY_OUT, "=&{v[136:143]}"(wf[1]) : BODY_IN : "memory", "scc"); TS(19); }
    { if (w == 0) { RELANE(); const f32x2 sx = LD(f32x2, M_STX); const u32x2 v = LD(u32x2, M_XSEL + lane * 8); LD(u32x2, M_XSEL + lane * 8) = (u32x2){ln2(v[0], sx[0], sx[1]), ln2(v[1], sx[0], sx[1])}; }
      BAR_VM(4); SEAM(2); const unsigned vm = 0, mm = 0; TS(12);
      asm volatile(BODY_TXT(T0(WOA(0), NO, NO), T1(WOA(1024), NO, NO), T2(KV0, NO, NO), T3(KVN(1024), NO, NO), T4(KVN(2048), NO, NO), T5(KVN(3072), NO, NO), T6(NO, NO, NO), T7(VM(4) TRA(4, 0) TRA(5, 1024), NO, NO))
                   : BODY_OUT, "=&{v[144:151]}"(wf[2]) : BODY_IN : "memory", "scc"); TS(20); }
    { RELANE(); const unsigned vm = R2 + w * 4096 + MROW() * 128 + MSWZ() * 32 + (lane & 3) * 8, mm = 0;
      const unsigned xa = M_XSEL + w * 64 + (g4 >> 1) * 32 + (g4 & 1) * 8, gw = M_GU + w * 256 + lane * 4;
      BAR_VM(0); SEAM(3); TS(13);
#define GM(d, a) M16("v[" #d ":" #d "+3], v[176:179], v[" #a ":" #a "+3], 0")
      asm volatile(BODY_TXT(T0(WOA(0) "v_xor_b32 v244, 32, v19\n v_xor_b32 v245, 64, v19\n v_xor_b32 v246, 0x60, v19\n v_subrev_u32 v247, 0x10000, v19\n" B64(176, "v15", 0) B64(178, "v15", 16) TR2(180, "v19", 0, 1024),
                               WOA(1024) TR2(184, "v244", 0, 1024) TR2(188, "v245", 0, 1024) "v_subrev_u32 v244, 0x10000, v244\n v_subrev_u32 v245, 0x10000, v245\n", NO),
                            T1(TR2(192, "v246", 0, 1024) TR2(196, "v247", 0, 1024) "v_subrev_u32 v246, 0x10000, v246\n", TR2(200, "v244", 0, 1024) TR2(204, "v245", 0, 1024), GM(212, 180) GM(216, 184)),
                            T2(TR2(208, "v246", 0, 1024) KV0, KVN(1024), GM(220, 188) GM(224, 192)), T3(KVN(2048) KVN(3072), WOB(0), GM(228, 196) GM(232, 200)), T4(WOB(1024), WOB(2048), GM(236, 204) GM(240, 208)),
                            T5(WOB(3072), NO, NO),
                            T6("s_mov_b32 vcc_lo, 0xffff0000\n s_mov_b32 vcc_hi, 0xffff0000\n v_cndmask_b32 v212, v212, v216, vcc\n v_cndmask_b32 v220, v220, v224, vcc\n v_cndmask_b32 v228, v228, v232, vcc\n"
                               "v_cndmask_b32 v236, v236, v240, vcc\n s_mov_b32 vcc_lo, 0\n s_mov_b32 vcc_hi, -1\n v_cndmask_b32 v212, v212, v220, vcc\n v_cndmask_b32 v228, v228, v236, vcc\n",
                               "ds_write_b32 v16, v212\n ds_write_b32 v16, v228 offset:2048\n" VM(8) TRA(6, 0) TRA(7, 1024), NO),
                            T7(VM(0) TRB(8, 0) TRB(9, 1024), TRB(10, 2048) TRB(11, 3072), NO))
                   : BODY_OUT, "=&{v[152:159]}"(wf[3]), "=&{v[160:167]}"(wf[4]), "=&{v[168:175]}"(wf[5]) : BODY_IN, "{v15}"(xa), "{v16}"(gw)
                   : "memory", "scc", C8(176,177,178,179,180,181,182,183), C8(184,185,186,187,188,189,190,191), C8(192,193,194,195,196,197,198,199), C8(200,201,202,203,204,205,206,207),
                     C8(208,209,210,211,212,213,214,215), C8(216,217,218,219,220,221,222,223), C8(224,225,226,227,228,229,230,231), C8(232,233,234,235,236,237,238,239), C8(240,241,242,243,244,245,246,247), "vcc"); TS(21); }
    const unsigned ye = moe ? 0xFFFFu : 0u;
    { RELANE(); const unsigned vm = (lane >> 3) * 2048 + (((lane & 7) ^ (g4 << 1)) << 4), mm = R2 + w * 2048; smo = (u64)(wd + (size_t)(rm * 128 + w * 16) * 2048 + xcd * 256);
      const unsigned ga = M_GU + (w >> 2) * 2048 + ((w & 3) * 16 + l15) * 4, go = l15 * 64; const char* gs = ws + WS_GU + ((mi >> 2) * 64 + (w & 3) * 16) * 64 + (w >> 2) * 32 + (mi & 3) * 8;
      BAR_VM(0); SEAM(4); TS(14);
#define R2S(d, a, b) "ds_read2st64_b32 v[" #d ":" #d "+1], v15 offset0:" #a " offset1:" #b "\n"
      asm volatile(BODY_TXT(T0(WOA(0) R2S(224, 0, 1) R2S(226, 2, 3), WOA(1024) R2S(228, 4, 5) R2S(230, 6, 7), NO),
                            T1(WOB(0), WOB(1024) "v_add_f32 v224, v224, v225\n v_add_f32 v226, v226, v227\n v_add_f32 v228, v228, v229\n v_add_f32 v230, v230, v231\n v_add_f32 v224, v224, v226\n v_add_f32 v228, v228, v230\n"
                               "v_mov_b32 v225, %[tg]\n v_add_f32 v224, v224, v228\n", "s_mov_b32 exec_lo, %[ye]\n s_mov_b32 exec_hi, 0\n global_store_dwordx2 v16, v[224:225], %[gs] sc1\n s_mov_b64 exec, -1\n"),
                            T2(WOB(2048), WOB(3072), NO), T3(KV0, KVN(1024), NO), T4(KVN(2048), KVN(3072), NO), T5(MOE(0, 0), MOE(16384, 128), NO),
                            T6(MOE(1024, 16256), MOE(17408, 128) VM(8) TRA(12, 0) TRA(13, 1024), NO), T7(TRB(14, 0) TRB(15, 1024), TRB(16, 2048) TRB(17, 3072), NO))
                   : BODY_OUT, "=&{v[176:183]}"(wf[6]), "=&{v[184:191]}"(wf[7]), "=&{v[192:199]}"(wf[8]) : BODY_IN, "{v15}"(ga), "{v16}"(go), [ye] "s"(ye), [tg] "s"(tag32), [gs] "s"(gs)
                   : "memory", "scc", C8(224,225,226,227,228,229,230,231)); TS(22); }
    u64 bad;
    { RELANE(); const unsigned vm = 0, mm = M_GR + w * 1024, ga = M_GR + w * 1024 + lane * 16, hw = M_H + w * 32 + (lane >> 2) * 2; smo = (u64)(ws + WS_GU + (size_t)(rm * 128 + w * 16) * 64);
      BAR_VM(4); SEAM(5); TS(15);
      asm volatile(BODY_TXT(T0(WOA(0), WOA(1024), NO), T1(WOB(0), WOB(1024), NO), T2(WOB(2048), WOB(3072), GRD), T3(KV0, KVN(1024), NO), T4(KVN(2048), KVN(3072), NO), T5(NO, NO, NO),
                            T6(VM(4) "ds_read_b128 v[224:227], v15\n" TRA(18, 0) TRA(19, 1024), TRB(20, 0) TRB(21, 1024), NO),
                            T7("v_add_f32 v224, v224, v226\n v_cmp_ne_u32 vcc, %[tg], v225\n v_cmp_ne_u32 s[74:75], %[tg], v227\n v_add_f32_dpp v224, v224, v224 quad_perm:[1,0,3,2] row_mask:0xf bank_mask:0xf\n"
                               "v_cvt_pk_bf16_f32 v224, v224, v224\n v_lshlrev_b32 v224, 16, v224\n v_mul_f32 v226, 0xbfb8aa3b, v224\n v_exp_f32 v226, v226\n"
                               "v_mov_b32_dpp v225, v224 quad_perm:[2,3,0,1] row_mask:0xf bank_mask:0xf\n s_or_b64 s[74:75], vcc, s[74:75]\n" TRB(22, 2048),
                               "v_add_f32 v226, 1.0, v226\n v_rcp_f32 v226, v226\n" TRB(23, 3072),
                               "v_mul_f32 v226, v224, v226\n v_cvt_pk_bf16_f32 v226, v226, v226\n v_lshlrev_b32 v226, 16, v226\n v_mul_f32 v226, v226, v225\n v_cvt_pk_bf16_f32 v226, v226, v226\n"
                               "s_mov_b32 exec_lo, 0x11111111\n s_mov_b32 exec_hi, 0x11111111\n ds_write_b16 v16, v226\n s_mov_b64 exec, -1\n"))
                   : BODY_OUT, "=&{v[200:207]}"(wf[9]), "=&{v[208:215]}"(wf[10]), "=&{v[216:223]}"(wf[11]), "=&{s[74:75]}"(bad) : BODY_IN, "{v15}"(ga), "{v16}"(hw), [tg] "s"(tag32)
                   : "memory", "scc", "vcc", "v224", "v225", "v226", "v227"); TS(23); }
    { RELANE();
      if (__builtin_expect(moe && bad != 0, 0)) {
          u32x4 gq;
#pragma unroll 1
          for (unsigned spins = 0; spins < SPIN_CAP; ++spins) {
              const char* p = ws + WS_GU + (size_t)(r * 128 + w * 16) * 64 + lane * 16; const u64 v0 = ld8_sc1(p), v1 = ld8_sc1(p + 8); gq = (u32x4){(unsigned)v0, (unsigned)(v0 >> 32), (unsigned)v1, (unsigned)(v1 >> 32)};
              if (__all(gq[1] == tag32 && gq[3] == tag32)) break;
              __builtin_amdgcn_s_sleep(2); }
          float sm = __uint_as_float(gq[0]) + __uint_as_float(gq[2]);
          sm = rb(sm + __uint_as_float(__builtin_amdgcn_update_dpp(0, (int)__float_as_uint(sm), 0xB1, 0xf, 0xf, false)));
          const float uu = __uint_as_float(__builtin_amdgcn_update_dpp(0, (int)__float_as_uint(sm), 0x4E, 0xf, 0xf, false));
          if (!(lane & 3)) LD(unsigned short, M_H + w * 32 + (lane >> 2) * 2) = (unsigned short)pk2(rb(silu(sm)) * uu, 0.f);
      }
      const unsigned vm = R2 + (w >> 2) * 16384 + MROW() * 128 + ((MSWZ() ^ (w & 3)) * 32) + (lane & 3) * 8, mm = tag32, ha = M_H + (g4 >> 1) * 32 + (g4 & 1) * 8, yo = lane * 8;
      smo = (u64)(ws + WS_YG + (rm * 1024 + xcd * 128 + w * 16) * 8);
      BAR_VM(0); SEAM(6); TS(16);
#define DM(h, d, c) M16("v[248:251], v[" #h ":" #h "+3], v[" #d ":" #d "+3], " c)
      asm volatile(BODY_TXT(T0(WOA(0) B64(224, "v15", 0) B64(226, "v15", 16) TR2(228, "v19", 0, 1024), WOA(1024) B64(232, "v15", 64) B64(234, "v15", 80) TR2(236, "v19", 4096, 1024), NO),
                            T1(WOB(0), WOB(1024), DM(224, 228, "0") DM(232, 236, "v[248:251]")),
                            T2(WOB(2048) B64(224, "v15", 128) B64(226, "v15", 144) TR2(228, "v19", 8192, 1024), WOB(3072) B64(232, "v15", 192) B64(234, "v15", 208) TR2(236, "v19", 12288, 1024), NO),
                            T3(KV0, KVN(1024), DM(224, 228, "v[248:251]") DM(232, 236, "v[248:251]")), T4(KVN(2048), KVN(3072), NO),
                            T5("v_mov_b32 v249, %[mm]\n", "s_mov_b32 exec_lo, %[ye]\n s_mov_b32 exec_hi, 0\n global_store_dwordx2 v16, v[248:249], s[72:73] sc1\n s_mov_b64 exec, -1\n", NO),
                            T6(NO, VM(5) TRA(24, 0) TRA(25, 1024), NO), T7(TRB(26, 0) TRB(27, 1024), TRB(28, 2048) TRB(29, 3072), NO))
                   : BODY_OUT, "=&{v[224:231]}"(wf[12]), "=&{v[232:239]}"(wf[13]), "=&{v[240:247]}"(wf[14]) : BODY_IN, "{v15}"(ha), "{v16}"(yo), [ye] "s"(ye)
                   : "memory", "scc", "v248", "v249", "v250", "v251"); TS(24); }
    { BAR_VM(1); SEAM(7);
      const unsigned vm = vw ^ ((vw >> 9) & 48), mm = mb; smo = (u64)(x + (size_t)(tb * 64 + kh * 32) * 2048 + hg * 256 + cbk * 64); TS(17);
      asm volatile(BODY_TXT(T0(WOA(0), NO, NO), T1(WOA(1024), NO, NO), T2(MOE(0, 0), NO, NO), T3(MOE(1024, 32768), NO, NO), T4(NO, NO, NO), T5(NO, NO, NO), T6(NO, NO, NO), T7(VM(0) TRA(30, 0) TRA(31, 1024), NO, NO))
                   : BODY_OUT, "=&{v[248:255]}"(wf[15]) : BODY_IN : "memory", "scc"); TS(25); }
    {
    RELANE(); const unsigned vl = lane * 16;
    u32x4 o0, o1;
    const bool routed = tb == (idx >> 6) && kh == ((idx >> 5) & 1);
    u32x2 xr[4]; for (int q = 0; q < 4; ++q) xr[q] = LD(u32x2, mb + l31 * 64 + ((q ^ ((l31 >> 2) & 3)) * 16) + h2 * 8);
    const f32x2 stt = LD(f32x2, M_ST64 + (tbk * 32 + l31) * 8);
    {
        const u32x8 P = __builtin_shufflevector(PK, PK, 0, 1, 2, 3, 4, 5, 6, 7); f32x4 L = __builtin_bit_cast(f32x4, lo4(LS));
        asm volatile("s_nop 1\n\tv_smfmac_f32_16x16x64_bf16 %0, %1, %2, %3\n\ts_nop 11" : "+v"(L) : "v"(hi4(LS)), "v"(P), "v"(IDX));
        O = mfma32(hi4(V), hi4(P), O);
        const auto l16 = __builtin_amdgcn_permlane16_swap(__float_as_uint(L[0] + L[1]), __float_as_uint(L[2] + L[3]), false, false);
        float lsum = __uint_as_float(__builtin_amdgcn_permlane32_swap(l16[0], l16[0], false, false)[0]);
        const float om = fmaxf(fmaxf(fmaxf(fmaxf(fabsf(O[0]), fabsf(O[1])), fabsf(O[2])), fmaxf(fmaxf(fabsf(O[3]), fabsf(O[4])), fabsf(O[5]))), fmaxf(fmaxf(fmaxf(fabsf(O[6]), fabsf(O[7])), fabsf(O[8])),
                         fmaxf(fmaxf(fmaxf(fabsf(O[9]), fabsf(O[10])), fabsf(O[11])), fmaxf(fmaxf(fmaxf(fabsf(O[12]), fabsf(O[13])), fabsf(O[14])), fabsf(O[15])))));
        if (__builtin_expect(__any(!(lsum <= 1.8446744e19f) || !(om <= 3.0e38f)), 0)) {
            float mr = -INFINITY; lsum = 0.f; for (int i = 0; i < 16; ++i) O[i] = 0.f;
#pragma unroll 1
            for (int t = 0; t < 64; ++t) {
                u32x4 k0, k1, v0, v1;
                asm volatile("global_load_dwordx4 %0, %4, %5 sc1\n\tglobal_load_dwordx4 %1, %4, %5 offset:1024 sc1\n\tglobal_load_dwordx4 %2, %4, %6 offset:2048 sc1\n\tglobal_load_dwordx4 %3, %4, %7 offset:3072 sc1\n\ts_waitcnt vmcnt(0)"
                             : "=&v"(k0), "=&v"(k1), "=&v"(v0), "=&v"(v1) : "v"(vl), "s"(kv + ((t + 62) & 63) * 4096), "s"(kv + ((t + 63) & 63) * 4096), "s"(kv + t * 4096) : "memory");
                f32x16 z; for (int i = 0; i < 16; ++i) z[i] = 0.f;
                f32x16 s = mfma32(k0, lo4(Q), z); s = mfma32(k1, hi4(Q), s);
                float m = s[0]; for (int i = 1; i < 16; ++i) m = fmaxf(m, s[i]);
                m = fmaxf(mr, xmax32(m)); const float al = __builtin_amdgcn_exp2f(mr - m); mr = m;
                u32x8 pp; float ps = 0.f;
                for (int i = 0; i < 8; ++i) { pp[i] = pk2(__builtin_amdgcn_exp2f(s[2 * i] - m), __builtin_amdgcn_exp2f(s[2 * i + 1] - m)); ps += bf_lo(pp[i]) + bf_hi(pp[i]); }
                lsum = lsum * al + xsum32(ps); for (int i = 0; i < 16; ++i) O[i] *= al;
                O = mfma32(v0, lo4(pp), O); O = mfma32(v1, hi4(pp), O);
            }
        }
        const float rl = __builtin_amdgcn_rcpf(lsum);
        for (int i = 0; i < 4; ++i) { o0[i] = pk2(O[2 * i] * rl, O[2 * i + 1] * rl); o1[i] = pk2(O[8 + 2 * i] * rl, O[8 + 2 * i + 1] * rl); }
        asm volatile("global_store_dwordx4 %0, %1, %3 sc1\n\tglobal_store_dwordx4 %0, %2, %3 offset:2048 sc1\n\ts_nop 1"
                     :: "v"(vl), "v"(o0), "v"(o1), "s"(ws + WS_O + (size_t)(tb * 8 + hg) * 16384 + hd * 4096 + tbk * 1024) : "memory");
    }
    u64 yg[14];
    if (routed) for (int i = 0; i < 14; ++i) yg[i] = ld8_sc1(ws + WS_YG + ((h2 * 14 + i) * 1024 + hg * 128 + cbk * 32 + l31) * 8);
    TS(26);
    BAR_LDS();
    TS(27);
    const unsigned pv = w * PRIV;
    LD(u32x4, hg * PRIV + hd * 4096 + tbk * 1024 + vl) = o0; LD(u32x4, hg * PRIV + hd * 4096 + 2048 + tbk * 1024 + vl) = o1;
    unsigned xn2[8], g2[8];
    for (int i = 0; i < 8; ++i) { xn2[i] = ln2(xr[i >> 1][i & 1], stt[0], stt[1]); PIN(xn2[i]); }
    WAIT_VM(0);
    for (int i = 0; i < 14; ++i) asm volatile("" :: "v"(yg[i]));
    if (lane == 0) st8_sc1(ws + WS_FLAG4 + b * 128 + w * 8, seed);
    TS(28);
    if (routed) {
#pragma unroll 1
        for (unsigned spins = 0; spins < SPIN_CAP; ++spins) {
            bool ok = true; for (int i = 0; i < 14; ++i) ok &= (unsigned)(yg[i] >> 32) == tag32;
            if (__all(ok)) break;
            __builtin_amdgcn_s_sleep(2);
            for (int i = 0; i < 14; ++i) yg[i] = ld8_sc1(ws + WS_YG + ((h2 * 14 + i) * 1024 + hg * 128 + cbk * 32 + l31) * 8);
        }
        float acc = 0.f; for (int i = 0; i < 14; ++i) acc += __uint_as_float((unsigned)yg[i]);
        const unsigned o16 = (gkey >> 11) & 0xffffu, rbits = (o16 & 0x8000u) ? (o16 & 0x7fffu) : (~o16 & 0xffffu);
        const float rsel = rb(__builtin_amdgcn_rcpf(1.f + __builtin_amdgcn_exp2f(-1.44269504089f * bf_lo(rbits))));
        LD(float, pv + 16384 + lane * 4) = rb(rb(xsum32(acc)) * rsel);
        for (int q = 0; q < 4; ++q) { const f32x4 y = LD(f32x4, pv + 16384 + q * 32 + h2 * 16);
            for (int i = 0; i < 2; ++i) { const unsigned t = pk2(bf_lo(xn2[2 * q + i]) + y[2 * i], bf_hi(xn2[2 * q + i]) + y[2 * i + 1]); xn2[2 * q + i] = l31 == (idx & 31) ? t : xn2[2 * q + i]; } }
    }
    for (int i = 0; i < 8; ++i) { g2[i] = pk2(silu(bf_lo(xn2[i])), silu(bf_hi(xn2[i]))); PIN(g2[i]); }
    TS(31);
    if (w != hg) {
        const int pb = (((w & 3) * 8 + (tb & 7)) << 3) | ((tb >> 3) * 2 + (w >> 2));
#pragma unroll 1
        for (unsigned spins = 0; spins < SPIN_CAP; ++spins) { if (__all(ld8_sc1(ws + WS_FLAG4 + pb * 128 + (lane & 7) * 8) == seed)) break; __builtin_amdgcn_s_sleep(1); }
        TS(29);
        const char* src = ws + WS_O + (size_t)(tb * 8 + w) * 16384;
        for (int i = 0; i < 4; ++i) dma4_sc1(src + i * 4096, vl, pv + i * 4096);
    }
    BAR_VM(12);
    TS(6);
    f32x16 ao, at; for (int i = 0; i < 16; ++i) { ao[i] = 0.f; at[i] = 0.f; }
    {
        const unsigned oa = kh * 4 * PRIV + vl + kh * 1024, ob = oa ^ 1024;
        u32x4 fr[2][8]; f32x4 pt[4];
#define OAD(g, i) ((g) < 4 ? (((i) & 1) ? oa : ob) + (((g) & 1) * 2 + ((i) >> 2)) * PRIV + (((g) >> 1) * 2 + (((i) >> 1) & 1)) * 2048 : ((g) < 6 ? ob : oa) + ((i) >> 1) * PRIV + (4 + ((g) & 1) * 2 + ((i) & 1)) * 2048)
#define OWJ(g, i) ((g) < 4 ? (((g) & 1) * 2 + ((i) >> 2)) * 8 + ((g) >> 1) * 2 + (((i) >> 1) & 1) : ((i) >> 1) * 8 + 4 + ((g) & 1) * 2 + ((i) & 1))
#define ORD(p, g) do { for (int i = 0; i < 8; ++i) fr[p][i] = LD(u32x4, OAD(g, i)); } while (0)
#define WFJ(j) (((j) & 1) ? hi4(wf[(j) >> 1]) : lo4(wf[(j) >> 1]))
        __builtin_amdgcn_s_setprio(1);
        ORD(0, 0); SBAR();
#pragma unroll
        for (int g = 0; g < 8; ++g) {
            if (g == 1) asm volatile("s_waitcnt vmcnt(8)\n\ts_barrier" ::: "memory");
            if (g == 3) asm volatile("s_waitcnt vmcnt(4)\n\ts_barrier" ::: "memory");
            if (g == 4) asm volatile("s_waitcnt vmcnt(0)\n\ts_barrier" ::: "memory");
            if (g == 6) { __builtin_amdgcn_s_setprio(0); ORD(1, 7); SBAR(); for (int q = 0; q < 4; ++q) LD(f32x4, pv + 16384 + q * 1024 + vl) = (f32x4){at[4 * q], at[4 * q + 1], at[4 * q + 2], at[4 * q + 3]}; SBAR(); }
            if (g < 6) ORD((g + 1) & 1, g + 1);
            for (int i = 0; i < 8; ++i) {
                if (g < 4 ? (i & 1) : g >= 6) ao = mfma32(WFJ(OWJ(g, i)), fr[g & 1][i], ao); else at = mfma32(WFJ(OWJ(g, i)), fr[g & 1][i], at);
                if (g == 7 && i == 5) { SBAR(); __builtin_amdgcn_s_setprio(1); BAR_LDS(); for (int q = 0; q < 4; ++q) pt[q] = LD(f32x4, (w ^ 4) * PRIV + 16384 + q * 1024 + vl); SBAR(); } }
            if (g < 6) for (int i = 0; i < 8; ++i) { SGB(0x008, 1); SGB(0x100, 1); }
            SBAR();
        }
        __builtin_amdgcn_s_setprio(0);
        TS(30);
        char* op = out + (size_t)(tb * 64 + kh * 32 + l31) * 2048 + hg * 256 + cbk * 64 + h2 * 16;
        u32x2 o[4];
        for (int q = 0; q < 4; ++q) for (int i = 0; i < 2; ++i) {
            const unsigned s2 = pk2(ao[4 * q + 2 * i] + pt[q][2 * i], ao[4 * q + 2 * i + 1] + pt[q][2 * i + 1]), gg = g2[2 * q + i];
            const unsigned p2 = pk2(bf_lo(s2) * bf_lo(gg), bf_hi(s2) * bf_hi(gg));
            o[q][i] = pk2(bf_lo(xr[q][i]) + bf_lo(p2), bf_hi(xr[q][i]) + bf_hi(p2));
        }
        for (int q = 0; q < 4; q += 2) {
            const auto s0 = __builtin_amdgcn_permlane32_swap(o[q][0], o[q + 1][0], false, false), s1 = __builtin_amdgcn_permlane32_swap(o[q][1], o[q + 1][1], false, false);
            *(u32x4*)(op + q * 16) = (u32x4){s0[0], s1[0], s0[1], s1[1]};
        }
    }
    }
    TS(7);
}

extern "C" void kernel_launch(void* const* d_in, const int* in_sizes, int n_in, void* d_out, int out_size, void* d_ws, size_t ws_size, hipStream_t stream) {
    (void)hipFuncSetAttribute((const void*)fused_block, hipFuncAttributeMaxDynamicSharedMemorySize, LDS_BYTES);
    fused_block<<<256, 512, LDS_BYTES, stream>>>((const char*)d_in[0], (const char*)d_in[1], (const char*)d_in[2], (const char*)d_in[3], (const char*)d_in[4],
                                                 (const char*)d_in[6], (const char*)d_in[7], (const char*)d_in[8], (const char*)d_in[9], (char*)d_out, (char*)d_ws);
}
```

```cpp
#define PRIOV 1
#define FSTAGE 2
#include <hip/hip_runtime.h>
#include <hip/hip_bf16.h>
#include <stdint.h>
#include <type_traits>

typedef unsigned long long u64;
typedef float f32x16 __attribute__((ext_vector_type(16)));
typedef float f32x4 __attribute__((ext_vector_type(4)));
typedef float f32x2 __attribute__((ext_vector_type(2)));
typedef unsigned u32x2 __attribute__((ext_vector_type(2)));
typedef unsigned u32x4 __attribute__((ext_vector_type(4)));
typedef unsigned u32x8 __attribute__((ext_vector_type(8)));
typedef unsigned u32x16 __attribute__((ext_vector_type(16)));
typedef __bf16 bf16x8 __attribute__((ext_vector_type(8)));
typedef __bf16 bf16x2 __attribute__((ext_vector_type(2)));
typedef short s16x4 __attribute__((ext_vector_type(4)));
#define LAS __attribute__((address_space(3)))
#define GAS __attribute__((address_space(1)))
#define DI __device__ __forceinline__
#define LD(T, a) (*(LAS T*)(unsigned)(a))

constexpr unsigned WS_EPOCH = 0, WS_FLAG1 = 4096, WS_STATS = 8192, WS_FLAG4 = 65536, WS_KV = 131072, WS_YG = 393216, WS_GU = 622592, WS_CS = 851968, WS_O = 1u << 20;
constexpr unsigned WS_DBG = 6u << 20;
constexpr unsigned SLOT = 24576, L_X16 = 49152, L_WR = 81920, L_SP = 86016, L_FIN = 87040, L_PA = 73728, L_PC = 88064, L_WKV = 98304, L_QX = 0, L_PRO = 16384, L_CSP = 98304, L_MISC = 155648, LDS_BYTES = 163840;
constexpr unsigned M_CTL = L_MISC, M_ST64 = L_MISC + 256, M_STX = L_MISC + 1280, M_CS = L_MISC + 6144, M_XSEL = L_MISC + 2560, M_H = L_MISC + 3584, M_GU = L_MISC + 4096;
constexpr unsigned KA = 122880, KB = 32768, R2 = 65536, R3 = 0, R4 = 98304, M_GR = 114688, PRIV = 20480;
constexpr int slot_of(int s) { return s % 6; }
constexpr unsigned SPIN_CAP = 1u << 20;
constexpr int CODE_SPAN = 38912;

DI float bf_lo(unsigned v) { return __uint_as_float(v << 16); }
DI float bf_hi(unsigned v) { return __uint_as_float(v & 0xffff0000u); }
DI unsigned pk2(float lo, float hi) { f32x2 v = {lo, hi}; return __builtin_bit_cast(unsigned, __builtin_convertvector(v, bf16x2)); }
DI float rb(float f) { return __uint_as_float((unsigned)__builtin_bit_cast(unsigned short, (__bf16)f) << 16); }
DI float xsum32(float v) { auto r = __builtin_amdgcn_permlane32_swap(__float_as_uint(v), __float_as_uint(v), false, false); return __uint_as_float(r[0]) + __uint_as_float(r[1]); }
DI float xmax32(float v) { auto r = __builtin_amdgcn_permlane32_swap(__float_as_uint(v), __float_as_uint(v), false, false); return fmaxf(__uint_as_float(r[0]), __uint_as_float(r[1])); }
DI f32x16 mfma32(u32x4 a, u32x4 b, f32x16 c) { return __builtin_amdgcn_mfma_f32_32x32x16_bf16(__builtin_bit_cast(bf16x8, a), __builtin_bit_cast(bf16x8, b), c, 0, 0, 0); }
DI f32x4 mfma16(u32x4 a, u32x4 b, f32x4 c) { return __builtin_amdgcn_mfma_f32_16x16x32_bf16(__builtin_bit_cast(bf16x8, a), __builtin_bit_cast(bf16x8, b), c, 0, 0, 0); }
DI u32x2 trd(unsigned a) { return __builtin_bit_cast(u32x2, __builtin_amdgcn_ds_read_tr16_b64_v4i16((LAS s16x4*)a)); }
DI u32x4 trfrag(unsigned a, unsigned step) { u32x2 lo = trd(a), hi = trd(a + step); return (u32x4){lo[0], lo[1], hi[0], hi[1]}; }
DI u32x4 lo4(u32x8 v) { return __builtin_shufflevector(v, v, 0, 1, 2, 3); }
DI u32x4 hi4(u32x8 v) { return __builtin_shufflevector(v, v, 4, 5, 6, 7); }
DI float max3(float a, float b, float c) { float r; asm("v_max3_f32 %0, %1, %2, %3" : "=v"(r) : "v"(a), "v"(b), "v"(c)); return r; }
DI float silu(float v) { return v * __builtin_amdgcn_rcpf(1.f + __builtin_amdgcn_exp2f(-1.44269504089f * v)); }
DI unsigned ln2(unsigned v, float m, float rs) { const unsigned d = pk2(bf_lo(v) - m, bf_hi(v) - m); return pk2(bf_lo(d) * rs, bf_hi(d) * rs); }
DI u64 ld8_sc1(const void* p) { return __hip_atomic_load((GAS const u64*)p, __ATOMIC_RELAXED, __HIP_MEMORY_SCOPE_AGENT); }
DI void st8_sc1(void* p, u64 v) { __hip_atomic_store((GAS u64*)p, v, __ATOMIC_RELAXED, __HIP_MEMORY_SCOPE_AGENT); }
DI int fresh_lane() { int l; asm volatile("v_mbcnt_lo_u32_b32 %0, -1, 0\n\tv_mbcnt_hi_u32_b32 %0, -1, %0" : "=v"(l)); return l; }
#define RELANE() const int lane = fresh_lane(), l15 = lane & 15, g4 = lane >> 4, l31 = lane & 31, h2 = lane >> 5; (void)l15; (void)g4; (void)l31; (void)h2
DI u64 mix(unsigned lo, unsigned hi) { u64 z = (((u64)hi << 32) | lo) + 0x9E3779B97F4A7C15ull; z = (z ^ (z >> 30)) * 0xBF58476D1CE4E5B9ull; z = (z ^ (z >> 27)) * 0x94D049BB133111EBull; return z ^ (z >> 31); }
DI unsigned rowmax16(unsigned k) { k = max(k, (unsigned)__builtin_amdgcn_update_dpp(0, (int)k, 0x128, 0xf, 0xf, false)); k = max(k, (unsigned)__builtin_amdgcn_update_dpp(0, (int)k, 0x124, 0xf, 0xf, false));
    k = max(k, (unsigned)__builtin_amdgcn_update_dpp(0, (int)k, 0x122, 0xf, 0xf, false)); return max(k, (unsigned)__builtin_amdgcn_update_dpp(0, (int)k, 0x121, 0xf, 0xf, false)); }
DI void dma1(const void* sbase, unsigned voff, unsigned m0v) { asm volatile("s_mov_b32 m0, %2\n\ts_nop 0\n\tglobal_load_lds_dwordx4 %0, %1" :: "v"(voff), "s"(sbase), "s"(m0v) : "memory"); }
DI void dma1_sc1(const void* sbase, unsigned voff, unsigned m0v) { asm volatile("s_mov_b32 m0, %2\n\ts_nop 0\n\tglobal_load_lds_dwordx4 %0, %1 sc1" :: "v"(voff), "s"(sbase), "s"(m0v) : "memory"); }
DI void dma4(const void* sbase, unsigned voff, unsigned m0v) {
    asm volatile("s_mov_b32 m0, %2\n\ts_nop 0\n\tglobal_load_lds_dwordx4 %0, %1\n\tglobal_load_lds_dwordx4 %0, %1 offset:1024\n\tglobal_load_lds_dwordx4 %0, %1 offset:2048\n\tglobal_load_lds_dwordx4 %0, %1 offset:3072"
                 :: "v"(voff), "s"(sbase), "s"(m0v) : "memory"); }
DI void dma4_sc1(const void* sbase, unsigned voff, unsigned m0v) {
    asm volatile("s_mov_b32 m0, %2\n\ts_nop 0\n\tglobal_load_lds_dwordx4 %0, %1 sc1\n\tglobal_load_lds_dwordx4 %0, %1 offset:1024 sc1\n\tglobal_load_lds_dwordx4 %0, %1 offset:2048 sc1\n\tglobal_load_lds_dwordx4 %0, %1 offset:3072 sc1"
                 :: "v"(voff), "s"(sbase), "s"(m0v) : "memory"); }
#define BAR_ALL() asm volatile("s_waitcnt vmcnt(0) lgkmcnt(0)\n\ts_barrier" ::: "memory")
#define BAR_LDS() asm volatile("s_waitcnt lgkmcnt(0)\n\ts_barrier" ::: "memory")
#define BAR_VM(n) asm volatile("s_waitcnt vmcnt(" #n ") lgkmcnt(0)\n\ts_barrier" ::: "memory")
#define WAIT_VM(n) asm volatile("s_waitcnt vmcnt(" #n ")" ::: "memory")
#define PIN(v) asm volatile("" : "+v"(v))
#define SBAR() __builtin_amdgcn_sched_barrier(0)
#define SGB(m, n) __builtin_amdgcn_sched_group_barrier(m, n, 0)
#define TS(i) do { } while (0)

#define EX(A,i) "v_exp_f32 v[" #A "+" #i "], v[" #A "+" #i "]\n"
#define E4(A,a,b,c,d) EX(A,a) EX(A,b) EX(A,c) EX(A,d)
#define CV(p,A,i) "v_cvt_pk_bf16_f32 v[104+" #p "], v[" #A "+" #i "], v[" #A "+" #i "+1]\n"
#define RDX(dst, o, k) "ds_read_b128 " dst ", v22 offset:" o "+" #k "\n"
#define M32(x) "v_mfma_f32_32x32x16_bf16 " x "\n"
#define M16(x) "v_mfma_f32_16x16x32_bf16 " x "\n"
#define TILE(A, B, o, X1, X2, Y) \
    M32("v[" #B ":" #B "+15], v[112:115], v[96:99], v[64:79]") RDX("v[112:115]", o, 0) X1 E4(A,0,1,2,3) \
    M32("v[80:95], v[124:127], v[108:111], v[80:95]") RDX("v[124:127]", o, 3072) E4(A,4,5,6,7) \
    "v_smfmac_f32_16x16x64_bf16 v[24:27], v[28:31], v[104:111], v23\n" "s_waitcnt lgkmcnt(2)\n" CV(0,A,0) CV(1,A,2) CV(2,A,4) CV(3,A,6) \
    M32("v[" #B ":" #B "+15], v[116:119], v[100:103], v[" #B ":" #B "+15]") RDX("v[116:119]", o, 1024) X2 E4(A,8,9,10,11) \
    M32("v[80:95], v[120:123], v[104:107], v[80:95]") RDX("v[120:123]", o, 2048) E4(A,12,13,14,15) Y \
    "s_waitcnt lgkmcnt(2)\n" CV(4,A,8) CV(5,A,10) CV(6,A,12) CV(7,A,14)
#define NO ""
#define WOA(k) "s_add_u32 m0, %[ma], " #k "\n s_add_u32 s70, s70, 0x8000\n s_addc_u32 s71, s71, 0\n global_load_lds_dwordx4 v20, s[70:71]\n"
#define WOB(k) "s_add_u32 m0, %[mb], " #k "\n s_add_u32 s70, s70, 0x8000\n s_addc_u32 s71, s71, 0\n global_load_lds_dwordx4 v20, s[70:71]\n"
#define MOE(k, st) "s_add_u32 m0, %[mm], " #k "\n s_add_u32 s72, s72, " #st "\n s_addc_u32 s73, s73, 0\n global_load_lds_dwordx4 v19, s[72:73] nt\n"
#define MOX(k, st) "s_add_u32 m0, %[mm], " #k "\n s_add_u32 s72, s72, " #st "\n s_addc_u32 s73, s73, 0\n global_load_lds_dwordx4 v19, s[72:73]\n"
#define KV0 "s_mov_b32 m0, %[mk]\n s_nop 0\n global_load_lds_dwordx4 v21, %[sk] sc1\n"
#define KVN(k) "global_load_lds_dwordx4 v21, %[sk] offset:" #k " sc1\n"
#define GRD "s_mov_b32 m0, %[mm]\n s_nop 0\n global_load_lds_dwordx4 v21, s[72:73] sc1\n"
#define TR2(d, a, k, st) "ds_read_b64_tr_b16 v[" #d ":" #d "+1], " a " offset:" #k "\n ds_read_b64_tr_b16 v[" #d "+2:" #d "+3], " a " offset:" #k "+" #st "\n"
#define TRA(j, k) TR2(128+4*j, "v18", k, 512)
#define TRB(j, k) TR2(128+4*j, "v17", k, 512)
#define B64(d, a, k) "ds_read_b64 v[" #d ":" #d "+1], " a " offset:" #k "\n"
#define VM(n) "s_waitcnt vmcnt(" #n ")\n"
#define BODY_TXT(T0, T1, T2, T3, T4, T5, T6, T7) "s_nop 1\n s_setprio 3\n" T0 T1 T2 T3 T4 "s_setprio 2\n" T5 T6 "s_setprio 1\n" T7 "s_waitcnt lgkmcnt(0)\n s_nop 7\n"
#define PH "s_setprio 0\n"
#define T0(a,b,c) TILE(32,48,"0",a,b,c)
#define T1(a,b,c) TILE(48,32,"4096",a,b,c)
#define T2(a,b,c) TILE(32,48,"8192",a,b,c)
#define T3(a,b,c) TILE(48,32,"12288",a,b,c)
#define T4(a,b,c) TILE(32,48,"16384",a,b,c)
#define T5(a,b,c) TILE(48,32,"20480",a,b,c)
#define T6(a,b,c) TILE(32,48,"24576",a,b,c)
#define T7(a,b,c) TILE(48,32,"28672",a,PH b,c)
#define BODY_OUT "+{v[32:47]}"(SA), "=&{v[48:63]}"(SB), "+{v[80:95]}"(O), "+{v[24:31]}"(LS), "+{v[104:119]}"(PK), "+{v[120:127]}"(V), "+{s[70:71]}"(swo), "+{s[72:73]}"(smo)
#define BODY_IN "{v[64:79]}"(NEG), "{v[96:103]}"(Q), "{v23}"(IDX), "{v22}"(ad), "{v21}"(vl), "{v20}"(vw), "{v19}"(vm), "{v18}"(ta), "{v17}"(tb_), \
      [ma] "s"(ma), [mb] "s"(mb), [mm] "s"(mm), [mk] "s"(mk), [sk] "s"(sk)
#define C8(a,b,c,d,e,f,g,h) "v" #a, "v" #b, "v" #c, "v" #d, "v" #e, "v" #f, "v" #g, "v" #h

__global__ __launch_bounds__(512, 2) void fused_block(const char* __restrict__ x, const char* __restrict__ wq, const char* __restrict__ wk, const char* __restrict__ wv,
                                                      const char* __restrict__ wo, const char* __restrict__ wr, const char* __restrict__ wg, const char* __restrict__ wu,
                                                      const char* __restrict__ wd, char* __restrict__ out, char* __restrict__ ws) {
    const int tid = threadIdx.x, lane = tid & 63, w = __builtin_amdgcn_readfirstlane(tid >> 6);
    const int b = blockIdx.x, xcd = b & 7, r = b >> 3, tb = (xcd >> 1) * 8 + (r & 7), hg = (xcd & 1) * 4 + (r >> 3);
    const int isV = r >> 4, sub = (r >> 2) & 3, T0 = (xcd * 4 + (r & 3)) * 64 + sub * 16;
    const int l15 = lane & 15, g4 = lane >> 4, l31 = lane & 31, h2 = lane >> 5;
    const int hd = w & 3, tbk = w >> 2;
    const unsigned vl = lane * 16;
    TS(0);
    {
        u64 pc; asm volatile("s_getpc_b64 %0" : "=s"(pc));
        asm volatile("s_mov_b32 m0, %2\n\ts_mov_b64 exec, 3\n\tglobal_load_lds_dword %0, %1\n\ts_mov_b64 exec, -1"
                     :: "v"(lane * 128), "s"((pc & ~127ull) + min((r * 8 + w) * 256, CODE_SPAN - 256)), "s"(w < 4 ? L_X16 + w * 8192 : L_WKV + (w - 4) * 16384) : "memory");
    }
    const int c0 = r & 7, rm = min(r, 27), mi = xcd * 28 + rm; const char* kv = ws + WS_KV;
    f32x16 qa;
    float aone; u32x8 LS, PZ; f32x16 O; unsigned IDX, vw, vm0, ta, tb_;
    const unsigned ma = R4 + w * 2048, mb = R3 + w * 4096;
    u64 swo, smo, sk0;
#define SETUP() do { swo = (u64)(wo + (size_t)tbk * 512 * 2048 + hg * 256 + hd * 64) - 0x8000; smo = (u64)(wg + (size_t)((mi & 3) * 256 + w * 32) * 7168 + (mi >> 2) * 128); \
        sk0 = (u64)(kv + ((c0 + 1) & 7) * 32768 + w * 4096); asm volatile("" : "+s"(swo), "+s"(smo), "+s"(sk0) : "s"(out), "s"(wu), "s"(wd)); aone = h2 ? 0.f : 1.f; PIN(aone); \
        const unsigned one_ = 0x3F803F80u; LS = (u32x8){0u, 0u, 0u, 0u, l15 < 2 ? one_ : 0u, l15 < 2 ? one_ : 0u, (l15 & 14) == 2 ? one_ : 0u, (l15 & 14) == 2 ? one_ : 0u}; \
        IDX = (lane & 1) ? 0xEEEEEEEEu : 0x44444444u; vw = (lane >> 2) * 2048 + (lane & 3) * 16; vm0 = (lane >> 3) * 7168 + (((lane & 7) ^ (g4 << 1)) << 4); \
        ta = ma + (4 * h2 + (l15 >> 2)) * 64 + (g4 & 1) * 32 + (lane & 3) * 8; tb_ = ta + (mb - ma); for (int i = 0; i < 16; ++i) O[i] = 0.f; PZ = (u32x8){0u, 0u, 0u, 0u, 0u, 0u, 0u, 0u}; PIN(O); PIN(PZ); PIN(LS); PIN(IDX); PIN(vw); PIN(vm0); PIN(ta); PIN(tb_); } while (0)
    if (__builtin_expect(w < 4, 1)) {
        __builtin_amdgcn_s_setprio(3);
        u32x2 ev; u32x4 xq[8], aw[8], sel, ones; unsigned wa, wa2, spa, kvo, wb[3][2], xb_[3][4];
        {
            const char* xs = x + (size_t)T0 * 2048 + w * 512;
            for (int i = 0; i < 8; ++i) dma1(xs, (2 * i + h2) * 2048 + ((l31 ^ (2 * i + h2)) << 4), L_X16 + w * 8192 + i * 1024);
            dma1(wr + w * 512, l31 * 16, L_WR + w * 1024);
            asm volatile("s_mov_b64 exec, 1\n\tglobal_load_dwordx2 %0, %1, %2 sc1\n\ts_mov_b64 exec, -1" : "=v"(ev) : "v"(0), "s"(ws));
            SETUP();
            wa = L_WKV + w * 16384 + (8 * g4 + (l15 >> 2)) * 64 + (g4 & 1) * 32 + (lane & 3) * 8; wa2 = wa ^ 32; spa = L_SP + ((l15 + (isV ? 4 * g4 : 0)) & 15) * 16;
            const int kt = T0 >> 5, sh = sub & 1;
            kvo = isV ? ((kt - 1 + sh) & 63) * 4096 + 2048 + sh * 1024 + ((g4 & 1) * 32 + w * 16 + l15) * 16 + (g4 >> 1) * 8
                      : ((kt - 2) & 63) * 4096 + w * 1024 + ((g4 & 1) * 32 + sh * 16 + l15) * 16 + (g4 >> 1) * 8;
            const unsigned sv = (((l15 >> 2) & 1) == (g4 & 1)) ? 0x3F803F80u : 0u, xq_a = l31 * 128 + ((h2 ^ ((l31 >> 1) & 7)) << 4);
            const unsigned wq_a = 8192 + (8 * h2 + (l15 >> 2)) * 256 + ((hd ^ ((lane >> 2) & 3)) << 6) + (g4 & 1) * 32 + (lane & 3) * 8;
            sel = (u32x4){sv, sv, sv, sv}; ones = (u32x4){0x3F803F80u, 0x3F803F80u, 0x3F803F80u, 0x3F803F80u};
            for (int i = 0; i < 3; ++i) {
                wb[i][0] = wq_a + i * 2 * SLOT + (tb & 1) * 8192; wb[i][1] = wq_a + i * 2 * SLOT + (~tb & 1) * 8192; PIN(wb[i][0]); PIN(wb[i][1]);
                for (int ks = 0; ks < 4; ++ks) { xb_[i][ks] = (xq_a ^ (ks * 32) ^ ((tb & 1) * 64)) + i * 2 * SLOT; PIN(xb_[i][ks]); } }
            const unsigned oth = l15 == 1 ? 0x3F803F80u : 0u;
            for (int s = 0; s < 8; ++s) { aw[s] = (u32x4){oth, oth, oth, oth}; PIN(aw[s]); }
            PIN(kvo); PIN(wa); PIN(wa2); PIN(spa); PIN(ones); PIN(sel);
            WAIT_VM(1);
            const unsigned xa = L_X16 + w * 8192 + l15 * 512 + ((g4 ^ l15) << 4);
            for (int s = 0; s < 8; ++s) xq[s] = LD(u32x4, xa ^ (s * 64));
            if (l15 == 0) for (int s = 0; s < 8; ++s) aw[s] = LD(u32x4, L_WR + w * 1024 + s * 64 + g4 * 16);
        }
        TS(8);
        const f32x4 z4 = {0, 0, 0, 0};
        f32x4 s1 = z4, gr = z4, sw = z4;
        for (int s = 0; s < 8; ++s) { gr = mfma16(xq[s], xq[s], gr); s1 = mfma16(aw[s], xq[s], s1); sw = mfma16(aw[s], ones, sw); }
        { float d = (l15 & 2) ? ((l15 & 1) ? gr[3] : gr[2]) : ((l15 & 1) ? gr[1] : gr[0]); d = g4 == (l15 >> 2) ? d : 0.f;
          const auto e16 = __builtin_amdgcn_permlane16_swap(__float_as_uint(d), __float_as_uint(d), false, false); d = xsum32(__uint_as_float(e16[0]) + __uint_as_float(e16[1]));
          if (lane < 16) LD(f32x4, L_SP + w * 256 + vl) = (f32x4){s1[1], d, s1[0], sw[0]}; }
        TS(9);
        BAR_LDS();
        f32x4 m4, r4; unsigned key = 0;
        {
            f32x4 p[4]; for (int q = 0; q < 4; ++q) p[q] = LD(f32x4, spa + q * 256);
            const f32x4 t = (p[0] + p[1]) + (p[2] + p[3]);
            const float mean = t[0] * (1.f / 1024.f), var = fmaxf(t[1] * (1.f / 1024.f) - mean * mean, 0.f);
            const float mrow = rb(mean), rsrow = __builtin_amdgcn_rcpf(rb(__builtin_amdgcn_sqrtf(rb(rb(var) + 1e-5f))));
            const int mi_ = (int)__float_as_uint(mrow), ri_ = (int)__float_as_uint(rsrow);
#define BC(v, n) __uint_as_float((unsigned)__builtin_amdgcn_update_dpp(0, v, 0x150 + (n), 0xf, 0xf, false))
            m4 = (f32x4){BC(mi_, 0), BC(mi_, 1), BC(mi_, 2), BC(mi_, 3)}; r4 = (f32x4){BC(ri_, 0), BC(ri_, 1), BC(ri_, 2), BC(ri_, 3)};
            if (!isV) { m4 = (f32x4){mrow, mrow, mrow, mrow}; r4 = (f32x4){rsrow, rsrow, rsrow, rsrow};
                if (w == 2) { const unsigned u = __float_as_uint(rb((t[2] - mrow * t[3]) * rsrow)) >> 16;
                    key = rowmax16(((((u & 0x8000u) ? ~u : (u | 0x8000u)) & 0xffffu) << 11) | (2047 - (T0 + l15))); } }
        }
        PIN(m4); PIN(r4);
        TS(34);
        BAR_LDS();
        TS(33);
        {
            const f32x4 cs = mfma16(sel, trfrag(wb[0][0] + 4096, 1024), mfma16(sel, trfrag(wb[0][0], 1024), z4));
            asm volatile("s_nop 7\n\ts_mov_b32 exec_hi, 0\n\tglobal_store_dword %0, %1, %2 sc1\n\ts_mov_b64 exec, -1" :: "v"(lane * 4), "v"(cs[0]), "s"(ws + WS_CS + (hg * 32 + tb) * 512 + w * 128) : "memory");
        }
        auto kvpart = [&](auto V_) {
            constexpr bool V = decltype(V_)::value;
            u32x4 w0[8], w1[8]; f32x4 a0 = z4, a1 = z4, c0_ = z4, c1_ = z4;
            for (int s = 0; s < 3; ++s) { w0[s] = trfrag(wa + s * 2048, 256); w1[s] = trfrag(wa2 + s * 2048, 256); }
            SBAR();
#pragma unroll
            for (int s = 0; s < 8; ++s) {
                if (s < 5) { w0[s + 3] = trfrag(wa + (s + 3) * 2048, 256); w1[s + 3] = trfrag(wa2 + (s + 3) * 2048, 256); }
                if (V) { a0 = mfma16(xq[s], w0[s], a0); c0_ = mfma16(ones, w0[s], c0_); a1 = mfma16(xq[s], w1[s], a1); c1_ = mfma16(ones, w1[s], c1_); }
                else { a0 = mfma16(w0[s], xq[s], a0); c0_ = mfma16(w0[s], ones, c0_); a1 = mfma16(w1[s], xq[s], a1); c1_ = mfma16(w1[s], ones, c1_); }
                if (s < 5) for (int i = 0; i < 4; ++i) { SGB(0x008, 1); SGB(0x100, 1); }
                SBAR();
            }
            LD(f32x4, L_PA + w * 2048 + vl) = (a0 - m4 * c0_) * r4; LD(f32x4, L_PA + w * 2048 + 1024 + vl) = (a1 - m4 * c1_) * r4;
        };
        if (isV) kvpart(std::true_type{}); else kvpart(std::false_type{});
        asm volatile("s_waitcnt vmcnt(1)" : "+v"(ev) :: "memory");
        if (tid == 192) { const u64 sd = mix(ev[0], ev[1]); LD(u32x4, M_CTL + 16) = (u32x4){ev[0], ev[1], 0u, 0u}; LD(u64, M_CTL) = sd; }
        TS(35);
        BAR_LDS();
        TS(1);
        if (w < 2) {
            f32x4 p[4]; for (int i = 0; i < 4; ++i) p[i] = LD(f32x4, L_PA + i * 2048 + w * 1024 + vl);
            const f32x4 a = (p[0] + p[1]) + (p[2] + p[3]);
            st8_sc1(ws + WS_KV + kvo, ((u64)pk2(a[2], a[3]) << 32) | pk2(a[0], a[1]));
        } else if (w == 2 && !isV && lane < 16) st8_sc1(ws + WS_STATS + (T0 + l15) * 8, ((u64)__float_as_uint(r4[0]) << 32) | __float_as_uint(m4[0]));
        f32x16 qb; qa = O; qb = O;
        u32x4 af[2][4], b0[2][4], b1[2][4];
#define QRD(p, s) do { const unsigned sl_ = (slot_of(s) & 1) * SLOT; const int bi_ = slot_of(s) >> 1; for (int ks = 0; ks < 4; ++ks) { \
            af[p][ks] = trfrag(wb[bi_][ks >> 1] + sl_ + (ks & 1) * 4096, 1024); b1[p][ks] = LD(u32x4, xb_[bi_][ks] + sl_ + 4096); b0[p][ks] = LD(u32x4, xb_[bi_][ks] + sl_); } } while (0)
#pragma unroll
        for (int s = 0; s < 16; ++s) {

            if (s == FSTAGE || s == 10) WAIT_VM(0);
            if (!(s & 1)) { BAR_LDS(); if (s == 0) TS(37); if (s == 2) TS(39); if (s == 4) TS(41); if (s == 6) TS(43); if (s == 8) TS(45); if (s == 10) TS(47); if (s == 12) TS(49); if (s == 14) TS(51); }
            if (s == 0) { QRD(0, 0); BAR_LDS(); }
            if (s == FSTAGE && tid == 128) st8_sc1(ws + WS_FLAG1 + b * 8, (LD(u64, M_CTL) & ~0xFFFFFFFull) | key);
            if (s == 8 && w >= 2) dma1_sc1(ws + WS_FLAG1 + (w - 2) * 1024, vl, M_GU + (w - 2) * 1024);
            if (s < 15) {
                QRD((s + 1) & 1, s + 1);
                for (int ks = 0; ks < 4; ++ks) { qb = mfma32(af[s & 1][ks], b1[s & 1][ks], qb); qa = mfma32(af[s & 1][ks], b0[s & 1][ks], qa); }
                for (int i = 0; i < 8; ++i) { SGB(0x008, 1); SGB(0x100, 2); }
            } else {
                for (int ks = 0; ks < 4; ++ks) qb = mfma32(af[1][ks], b1[1][ks], qb);
                SBAR();
                qa = mfma32(af[1][0], b0[1][0], qa); qa = mfma32(af[1][1], b0[1][1], qa); SBAR();
                for (int ks = 0; ks < 4; ++ks) { LD(f32x4, L_QX + w * 4096 + ks * 1024 + vl) = (f32x4){qb[4 * ks], qb[4 * ks + 1], qb[4 * ks + 2], qb[4 * ks + 3]}; SBAR();
                    if (ks & 1) { qa = mfma32(af[1][2 + (ks >> 1)], b0[1][2 + (ks >> 1)], qa); SBAR(); } }
            }
            SBAR();
        }
        TS(2);
        BAR_LDS();
    } else {
        const int lw = w - 4, koff = tb >> 1;
        __builtin_amdgcn_s_sleep(10);
        { const char* wkv = (isV ? wv : wk) + lw * 16384;
          for (int i = 0; i < 4; ++i) dma4(wkv + i * 4096, vl ^ (h2 << 5), L_WKV + lw * 16384 + i * 4096); }
        unsigned vx[2], vq[4];
        for (int i = 0; i < 2; ++i) { const int row = lw * 16 + i * 8 + (lane >> 3); vx[i] = row * 2048 + (((lane & 7) ^ ((row >> 1) & 7)) << 4); }
        for (int i = 0; i < 4; ++i) vq[i] = (lw * 16 + i * 4 + g4) * 2048 + ((((lane >> 2) & 3) ^ g4) << 6) + ((lane & 3) << 4);
        const char* xb = x + (size_t)tb * 64 * 2048; const char* qb_ = wq + hg * 256;
#define RING_X(s) asm volatile("s_mov_b32 m0, %3\n\ts_nop 0\n\tglobal_load_lds_dwordx4 %0, %2\n\ts_add_u32 m0, m0, 1024\n\ts_nop 0\n\tglobal_load_lds_dwordx4 %1, %2" \
                     :: "v"(vx[0]), "v"(vx[1]), "s"(xb + (((s) + koff) & 15) * 128), "s"(slot_of(s) * SLOT + lw * 2048) : "memory", "scc")
#define RING_W(s) asm volatile("s_mov_b32 m0, %5\n\ts_nop 0\n\tglobal_load_lds_dwordx4 %0, %4\n\ts_add_u32 m0, m0, 1024\n\ts_nop 0\n\tglobal_load_lds_dwordx4 %1, %4\n\t" \
                     "s_add_u32 m0, m0, 1024\n\ts_nop 0\n\tglobal_load_lds_dwordx4 %2, %4\n\ts_add_u32 m0, m0, 1024\n\ts_nop 0\n\tglobal_load_lds_dwordx4 %3, %4" \
                     :: "v"(vq[0]), "v"(vq[1]), "v"(vq[2]), "v"(vq[3]), "s"(qb_ + (size_t)(((s) + koff) & 15) * 131072), "s"(slot_of(s) * SLOT + 8192 + lw * 4096) : "memory", "scc")
#define RING_ISSUE(s) do { RING_X(s); RING_W(s); } while (0)
        TS(32); RING_ISSUE(0); TS(8);
        BAR_LDS(); RING_ISSUE(1); RING_ISSUE(2);
        BAR_VM(12); TS(33); RING_W(3); TS(35);
        BAR_LDS(); TS(1); RING_ISSUE(4); RING_ISSUE(5); TS(36);
        BAR_VM(16); TS(37); RING_X(3);
        BAR_LDS(); RING_ISSUE(6); TS(38);
        BAR_VM(6); TS(39); RING_ISSUE(7); RING_ISSUE(8); TS(40);
        BAR_VM(12); TS(41); RING_ISSUE(9); RING_ISSUE(10); TS(42);
        BAR_VM(12); TS(43); RING_ISSUE(11); RING_ISSUE(12); TS(44);
        BAR_VM(12); TS(45);
        RING_ISSUE(13); RING_ISSUE(14); TS(46);
        const u64 tag36 = LD(u64, M_CTL) & ~0xFFFFFFFull;
        BAR_VM(12); TS(47); RING_ISSUE(15);
        u64 f[4]; { const u32x4 f0 = LD(u32x4, M_GU + vl), f1 = LD(u32x4, M_GU + 1024 + vl); f[0] = ((u64)f0[1] << 32) | f0[0]; f[1] = ((u64)f0[3] << 32) | f0[2]; f[2] = ((u64)f1[1] << 32) | f1[0]; f[3] = ((u64)f1[3] << 32) | f1[2]; }
#define FLAGS_OK() __all((((f[0] ^ tag36) | (f[1] ^ tag36) | (f[2] ^ tag36) | (f[3] ^ tag36)) >> 28) == 0)
        bool early = FLAGS_OK();
        if (!early) { for (int i = 0; i < 4; ++i) f[i] = ld8_sc1(ws + WS_FLAG1 + (i * 64 + lane) * 8); BAR_VM(6); early = FLAGS_OK(); }
        else BAR_VM(6);
        TS(49);
        if (!early) {
            BAR_VM(0);
#pragma unroll 1
            for (unsigned spins = 0; spins < SPIN_CAP; ++spins) {
                for (int i = 0; i < 4; ++i) f[i] = ld8_sc1(ws + WS_FLAG1 + (i * 64 + lane) * 8);
                if (FLAGS_OK()) break;
                __builtin_amdgcn_s_sleep(2);
            }
            PIN(f[0]); PIN(f[1]); PIN(f[2]); PIN(f[3]);
        }
        TS(50);
        for (int i = 0; i < 4; ++i) dma1_sc1(ws + WS_CS + hg * 16384 + i * 4096 + lw * 128, (lane >> 3) * 512 + (lane & 7) * 16, L_CSP + lw * 4096 + i * 1024);
        if (lw < 2) { dma4_sc1(kv + ((c0 * 8 + 62 + lw) & 63) * 4096, vl, L_PRO + lw * 4096); if (early) BAR_VM(8); }
        else if (lw == 2) { dma1_sc1(ws + WS_STATS + tb * 512, l31 * 16, M_ST64); if (early) BAR_VM(5); }
        else {
            unsigned key = rowmax16(max(max((unsigned)f[0] & 0xFFFFFFFu, (unsigned)f[1] & 0xFFFFFFFu), max((unsigned)f[2] & 0xFFFFFFFu, (unsigned)f[3] & 0xFFFFFFFu)));
            key = max(max((unsigned)__builtin_amdgcn_readlane(key, 0), (unsigned)__builtin_amdgcn_readlane(key, 16)), max((unsigned)__builtin_amdgcn_readlane(key, 32), (unsigned)__builtin_amdgcn_readlane(key, 48)));
            const int ix = 2047 - (key & 2047);
            dma1(x + (size_t)ix * 2048 + (mi & 3) * 512, l31 * 16, M_XSEL);
            asm volatile("s_mov_b32 m0, %2\n\ts_nop 0\n\tglobal_load_lds_dword %0, %1 sc1" :: "v"((lane & 1) * 4), "s"(ws + WS_STATS + ix * 8), "s"(M_STX) : "memory");
            if (early) BAR_VM(6);
            if (lane == 0) { LD(unsigned, M_CTL + 8) = key; if (b == 0) st8_sc1(ws + WS_EPOCH, LD(u64, M_CTL + 16) + 1); }
        }
        TS(51);
        dma4_sc1(kv + c0 * 32768 + lw * 8192, vl, KA + lw * 8192); dma4_sc1(kv + c0 * 32768 + lw * 8192 + 4096, vl, KA + lw * 8192 + 4096);
        SETUP();
        WAIT_VM(8);
        { float p[16]; for (int i = 0; i < 16; ++i) p[i] = LD(float, L_CSP + lw * 4096 + (h2 * 16 + i) * 128 + l31 * 4);
          const float csum = xsum32((((p[0] + p[1]) + (p[2] + p[3])) + ((p[4] + p[5]) + (p[6] + p[7]))) + (((p[8] + p[9]) + (p[10] + p[11])) + ((p[12] + p[13]) + (p[14] + p[15]))));
          const float c1 = rb(csum), c2 = rb(csum - c1); const u32x2 csp = (u32x2){pk2(c1, c2), pk2(csum - c1 - c2, 0.f)};
          LD(u32x2, M_CS + lw * 512 + lane * 8) = h2 ? (u32x2){0u, 0u} : csp; }
        TS(2);
        __builtin_amdgcn_s_setprio(3);
        BAR_LDS();
        for (int q = 0; q < 4; ++q) { const f32x4 t = LD(f32x4, L_QX + hd * 4096 + q * 1024 + vl); for (int i = 0; i < 4; ++i) qa[4 * q + i] = t[i]; }
    }
    TS(3);
    u32x8 Q, V; u32x16 PK; f32x16 SA, SB, NEG;
    const u32x4 ctl = LD(u32x4, M_CTL);
    {
        const f32x2 st = LD(f32x2, M_ST64 + (tbk * 32 + l31) * 8); const u32x2 csp = LD(u32x2, M_CS + hd * 512 + lane * 8);
        const u32x4 k00 = LD(u32x4, L_PRO + vl), k01 = LD(u32x4, L_PRO + 1024 + vl), k10 = LD(u32x4, L_PRO + 4096 + vl), k11 = LD(u32x4, L_PRO + 5120 + vl), v00 = LD(u32x4, L_PRO + 6144 + vl);
        const unsigned nm = (__float_as_uint(st[0]) ^ 0x80000000u) >> 16;
        const u32x4 ca = {csp[0], csp[1], PZ[0], PZ[1]}, cb = {nm * 0x10001u, nm, PZ[0], PZ[1]};
        qa = mfma32(ca, cb, qa);
        const float qs = st[1] * (0.17677669529663687f * 1.4426950408889634f);
        for (int i = 0; i < 8; ++i) Q[i] = pk2(qa[2 * i] * qs, qa[2 * i + 1] * qs);
        f32x16 s0 = mfma32(k00, lo4(Q), O); s0 = mfma32(k01, hi4(Q), s0);
        for (int i = 0; i < 4; ++i) { PK[i] = PZ[i]; PK[4 + i] = PZ[4 + i]; PK[8 + i] = k10[i]; PK[12 + i] = k11[i]; V[i] = v00[i]; V[4 + i] = k00[i]; }
        asm volatile("s_nop 7\n\ts_nop 3" : "+v"(s0));
        float m = max3(max3(s0[0], s0[1], s0[2]), max3(s0[3], s0[4], s0[5]), s0[15]);
        m = max3(max3(max3(s0[6], s0[7], s0[8]), max3(s0[9], s0[10], s0[11]), max3(s0[12], s0[13], s0[14])), m, m);
        const auto mm_ = __builtin_amdgcn_permlane32_swap(__float_as_uint(m), __float_as_uint(m), false, false);
        m = max3(__uint_as_float(mm_[0]), __uint_as_float(mm_[1]), __uint_as_float(mm_[1]));
        NEG = __builtin_amdgcn_mfma_f32_32x32x2f32(aone, -m, O, 0, 0, 0);
        PIN(NEG);
        for (int i = 0; i < 16; ++i) SA[i] = s0[i] - m;
        PIN(SA);
    }
    TS(4);
    const u64 seed = ((u64)(unsigned)__builtin_amdgcn_readfirstlane(ctl[1]) << 32) | (unsigned)__builtin_amdgcn_readfirstlane(ctl[0]);
    const unsigned tag32 = (unsigned)seed ^ 0x5bd1e995u, gkey = __builtin_amdgcn_readfirstlane(ctl[2]);
    const int idx = 2047 - (gkey & 2047), cbk = hd, kh = tbk;
    const bool moe = r < 28;
    u32x8 wf[16];
#define SEAM(c) const unsigned ad = (((c) & 1) ? KB : KA) + vl, mk = (((c) & 1) ? KA : KB) + w * 4096; const char* sk = kv + ((c0 + (c) + 1) & 7) * 32768 + w * 4096
#define MROW() (16 * (g4 >> 1) + 4 * (g4 & 1) + (l15 >> 2))
#define MSWZ() (2 * (g4 & 1) + (l15 >> 3))
    { const unsigned ad = KA + vl, mk = KB + w * 4096, vm = vm0, mm = R2 + w * 4096; const u64 sk = sk0;
      asm volatile("s_waitcnt vmcnt(0) lgkmcnt(0)\n\ts_barrier" :: "{v22}"(ad), "s"(seed), "s"(gkey), "s"(mk), "s"(sk) : "memory"); TS(10);
      asm volatile(BODY_TXT(T0(WOA(0), NO, NO), T1(WOA(1024), NO, NO), T2(KV0, KVN(1024), NO), T3(KVN(2048), KVN(3072), NO), T4(MOE(0, 0), MOE(1024, 57344), NO), T5(MOE(2048, 57344), MOE(3072, 57344), NO),
                            T6(NO, NO, NO), T7(VM(8) TRA(0, 0) TRA(1, 1024), NO, NO))
                   : BODY_OUT, "=&{v[128:135]}"(wf[0]) : BODY_IN : "memory", "scc"); TS(18); }
    { BAR_VM(4); SEAM(1); const unsigned vm = vm0, mm = mb; smo = (u64)(wu + (size_t)((mi & 3) * 256 + w * 32) * 7168 + (mi >> 2) * 128);
      TS(11);
      asm volatile(BODY_TXT(T0(WOA(0), NO, NO), T1(WOA(1024), NO, NO), T2(KV0, KVN(1024), NO), T3(KVN(2048), KVN(3072), NO), T4(MOE(0, 0), MOE(1024, 57344), NO), T5(MOE(2048, 57344), MOE(3072, 57344), NO),
                            T6(NO, NO, NO), T7(VM(8) TRA(2, 0) TRA(3, 1024), NO, NO))
                   : BODY_OUT, "=&{v[136:143]}"(wf[1]) : BODY_IN : "memory", "scc"); TS(19); }
    { if (w == 0) { RELANE(); const f32x2 sx = LD(f32x2, M_STX); const u32x2 v = LD(u32x2, M_XSEL + lane * 8); LD(u32x2, M_XSEL + lane * 8) = (u32x2){ln2(v[0], sx[0], sx[1]), ln2(v[1], sx[0], sx[1])}; }
      BAR_VM(4); SEAM(2); const unsigned vm = 0, mm = 0; TS(12);
      asm volatile(BODY_TXT(T0(WOA(0), NO, NO), T1(WOA(1024), NO, NO), T2(KV0, NO, NO), T3(KVN(1024), NO, NO), T4(KVN(2048), NO, NO), T5(KVN(3072), NO, NO), T6(NO, NO, NO), T7(VM(4) TRA(4, 0) TRA(5, 1024), NO, NO))
                   : BODY_OUT, "=&{v[144:151]}"(wf[2]) : BODY_IN : "memory", "scc"); TS(20); }
    { RELANE(); const unsigned vm = R2 + w * 4096 + MROW() * 128 + MSWZ() * 32 + (lane & 3) * 8, mm = 0;
      const unsigned xa = M_XSEL + w * 64 + (g4 >> 1) * 32 + (g4 & 1) * 8, gw = M_GU + w * 256 + lane * 4;
      BAR_VM(0); SEAM(3); TS(13);
#define GM(d, a) M16("v[" #d ":" #d "+3], v[176:179], v[" #a ":" #a "+3], 0")
      asm volatile(BODY_TXT(T0(WOA(0) "v_xor_b32 v244, 32, v19\n v_xor_b32 v245, 64, v19\n v_xor_b32 v246, 0x60, v19\n v_subrev_u32 v247, 0x10000, v19\n" B64(176, "v15", 0) B64(178, "v15", 16) TR2(180, "v19", 0, 1024),
                               WOA(1024) TR2(184, "v244", 0, 1024) TR2(188, "v245", 0, 1024) "v_subrev_u32 v244, 0x10000, v244\n v_subrev_u32 v245, 0x10000, v245\n", NO),
                            T1(TR2(192, "v246", 0, 1024) TR2(196, "v247", 0, 1024) "v_subrev_u32 v246, 0x10000, v246\n", TR2(200, "v244", 0, 1024) TR2(204, "v245", 0, 1024), GM(212, 180) GM(216, 184)),
                            T2(TR2(208, "v246", 0, 1024) KV0, KVN(1024), GM(220, 188) GM(224, 192)), T3(KVN(2048) KVN(3072), WOB(0), GM(228, 196) GM(232, 200)), T4(WOB(1024), WOB(2048), GM(236, 204) GM(240, 208)),
                            T5(WOB(3072), NO, NO),
                            T6("s_mov_b32 vcc_lo, 0xffff0000\n s_mov_b32 vcc_hi, 0xffff0000\n v_cndmask_b32 v212, v212, v216, vcc\n v_cndmask_b32 v220, v220, v224, vcc\n v_cndmask_b32 v228, v228, v232, vcc\n"
                               "v_cndmask_b32 v236, v236, v240, vcc\n s_mov_b32 vcc_lo, 0\n s_mov_b32 vcc_hi, -1\n v_cndmask_b32 v212, v212, v220, vcc\n v_cndmask_b32 v228, v228, v236, vcc\n",
                               "ds_write_b32 v16, v212\n ds_write_b32 v16, v228 offset:2048\n" VM(8) TRA(6, 0) TRA(7, 1024), NO),
                            T7(VM(0) TRB(8, 0) TRB(9, 1024), TRB(10, 2048) TRB(11, 3072), NO))
                   : BODY_OUT, "=&{v[152:159]}"(wf[3]), "=&{v[160:167]}"(wf[4]), "=&{v[168:175]}"(wf[5]) : BODY_IN, "{v15}"(xa), "{v16}"(gw)
                   : "memory", "scc", C8(176,177,178,179,180,181,182,183), C8(184,185,186,187,188,189,190,191), C8(192,193,194,195,196,197,198,199), C8(200,201,202,203,204,205,206,207),
                     C8(208,209,210,211,212,213,214,215), C8(216,217,218,219,220,221,222,223), C8(224,225,226,227,228,229,230,231), C8(232,233,234,235,236,237,238,239), C8(240,241,242,243,244,245,246,247), "vcc"); TS(21); }
    const unsigned ye = moe ? 0xFFFFu : 0u;
    { RELANE(); const unsigned vm = (lane >> 3) * 2048 + (((lane & 7) ^ (g4 << 1)) << 4), mm = R2 + w * 2048; smo = (u64)(wd + (size_t)(rm * 128 + w * 16) * 2048 + xcd * 256);
      const unsigned ga = M_GU + (w >> 2) * 2048 + ((w & 3) * 16 + l15) * 4, go = l15 * 64; const char* gs = ws + WS_GU + ((mi >> 2) * 64 + (w & 3) * 16) * 64 + (w >> 2) * 32 + (mi & 3) * 8;
      BAR_VM(0); SEAM(4); TS(14);
#define R2S(d, a, b) "ds_read2st64_b32 v[" #d ":" #d "+1], v15 offset0:" #a " offset1:" #b "\n"
      asm volatile(BODY_TXT(T0(WOA(0) R2S(224, 0, 1) R2S(226, 2, 3), WOA(1024) R2S(228, 4, 5) R2S(230, 6, 7), NO),
                            T1(WOB(0), WOB(1024) "v_add_f32 v224, v224, v225\n v_add_f32 v226, v226, v227\n v_add_f32 v228, v228, v229\n v_add_f32 v230, v230, v231\n v_add_f32 v224, v224, v226\n v_add_f32 v228, v228, v230\n"
                               "v_mov_b32 v225, %[tg]\n v_add_f32 v224, v224, v228\n", "s_mov_b32 exec_lo, %[ye]\n s_mov_b32 exec_hi, 0\n global_store_dwordx2 v16, v[224:225], %[gs] sc1\n s_mov_b64 exec, -1\n"),
                            T2(WOB(2048), WOB(3072), NO), T3(KV0, KVN(1024), NO), T4(KVN(2048), KVN(3072), NO), T5(MOE(0, 0), MOE(16384, 128), NO),
                            T6(MOE(1024, 16256), MOE(17408, 128) VM(8) TRA(12, 0) TRA(13, 1024), NO), T7(TRB(14, 0) TRB(15, 1024), TRB(16, 2048) TRB(17, 3072), NO))
                   : BODY_OUT, "=&{v[176:183]}"(wf[6]), "=&{v[184:191]}"(wf[7]), "=&{v[192:199]}"(wf[8]) : BODY_IN, "{v15}"(ga), "{v16}"(go), [ye] "s"(ye), [tg] "s"(tag32), [gs] "s"(gs)
                   : "memory", "scc", C8(224,225,226,227,228,229,230,231)); TS(22); }
    u64 bad;
    { RELANE(); const unsigned vm = 0, mm = M_GR + w * 1024, ga = M_GR + w * 1024 + lane * 16, hw = M_H + w * 32 + (lane >> 2) * 2; smo = (u64)(ws + WS_GU + (size_t)(rm * 128 + w * 16) * 64);
      BAR_VM(4); SEAM(5); TS(15);
      asm volatile(BODY_TXT(T0(WOA(0), WOA(1024), NO), T1(WOB(0), WOB(1024), NO), T2(WOB(2048), WOB(3072), GRD), T3(KV0, KVN(1024), NO), T4(KVN(2048), KVN(3072), NO), T5(NO, NO, NO),
                            T6(VM(4) "ds_read_b128 v[224:227], v15\n" TRA(18, 0) TRA(19, 1024), TRB(20, 0) TRB(21, 1024), NO),
                            T7("v_add_f32 v224, v224, v226\n v_cmp_ne_u32 vcc, %[tg], v225\n v_cmp_ne_u32 s[74:75], %[tg], v227\n v_add_f32_dpp v224, v224, v224 quad_perm:[1,0,3,2] row_mask:0xf bank_mask:0xf\n"
                               "v_cvt_pk_bf16_f32 v224, v224, v224\n v_lshlrev_b32 v224, 16, v224\n v_mul_f32 v226, 0xbfb8aa3b, v224\n v_exp_f32 v226, v226\n"
                               "v_mov_b32_dpp v225, v224 quad_perm:[2,3,0,1] row_mask:0xf bank_mask:0xf\n s_or_b64 s[74:75], vcc, s[74:75]\n" TRB(22, 2048),
                               "v_add_f32 v226, 1.0, v226\n v_rcp_f32 v226, v226\n" TRB(23, 3072),
                               "v_mul_f32 v226, v224, v226\n v_cvt_pk_bf16_f32 v226, v226, v226\n v_lshlrev_b32 v226, 16, v226\n v_mul_f32 v226, v226, v225\n v_cvt_pk_bf16_f32 v226, v226, v226\n"
                               "s_mov_b32 exec_lo, 0x11111111\n s_mov_b32 exec_hi, 0x11111111\n ds_write_b16 v16, v226\n s_mov_b64 exec, -1\n"))
                   : BODY_OUT, "=&{v[200:207]}"(wf[9]), "=&{v[208:215]}"(wf[10]), "=&{v[216:223]}"(wf[11]), "=&{s[74:75]}"(bad) : BODY_IN, "{v15}"(ga), "{v16}"(hw), [tg] "s"(tag32)
                   : "memory", "scc", "vcc", "v224", "v225", "v226", "v227"); TS(23); }
    { RELANE();
      if (__builtin_expect(moe && bad != 0, 0)) {
          u32x4 gq;
#pragma unroll 1
          for (unsigned spins = 0; spins < SPIN_CAP; ++spins) {
              const char* p = ws + WS_GU + (size_t)(r * 128 + w * 16) * 64 + lane * 16; const u64 v0 = ld8_sc1(p), v1 = ld8_sc1(p + 8); gq = (u32x4){(unsigned)v0, (unsigned)(v0 >> 32), (unsigned)v1, (unsigned)(v1 >> 32)};
              if (__all(gq[1] == tag32 && gq[3] == tag32)) break;
              __builtin_amdgcn_s_sleep(2); }
          float sm = __uint_as_float(gq[0]) + __uint_as_float(gq[2]);
          sm = rb(sm + __uint_as_float(__builtin_amdgcn_update_dpp(0, (int)__float_as_uint(sm), 0xB1, 0xf, 0xf, false)));
          const float uu = __uint_as_float(__builtin_amdgcn_update_dpp(0, (int)__float_as_uint(sm), 0x4E, 0xf, 0xf, false));
          if (!(lane & 3)) LD(unsigned short, M_H + w * 32 + (lane >> 2) * 2) = (unsigned short)pk2(rb(silu(sm)) * uu, 0.f);
      }
      const unsigned vm = R2 + (w >> 2) * 16384 + MROW() * 128 + ((MSWZ() ^ (w & 3)) * 32) + (lane & 3) * 8, mm = tag32, ha = M_H + (g4 >> 1) * 32 + (g4 & 1) * 8, yo = lane * 8;
      smo = (u64)(ws + WS_YG + (rm * 1024 + xcd * 128 + w * 16) * 8);
      BAR_VM(0); SEAM(6); TS(16);
#define DM(h, d, c) M16("v[248:251], v[" #h ":" #h "+3], v[" #d ":" #d "+3], " c)
      asm volatile(BODY_TXT(T0(WOA(0) B64(224, "v15", 0) B64(226, "v15", 16) TR2(228, "v19", 0, 1024), WOA(1024) B64(232, "v15", 64) B64(234, "v15", 80) TR2(236, "v19", 4096, 1024), NO),
                            T1(WOB(0), WOB(1024), DM(224, 228, "0") DM(232, 236, "v[248:251]")),
                            T2(WOB(2048) B64(224, "v15", 128) B64(226, "v15", 144) TR2(228, "v19", 8192, 1024), WOB(3072) B64(232, "v15", 192) B64(234, "v15", 208) TR2(236, "v19", 12288, 1024), NO),
                            T3(KV0, KVN(1024), DM(224, 228, "v[248:251]") DM(232, 236, "v[248:251]")), T4(KVN(2048), KVN(3072), NO),
                            T5("v_mov_b32 v249, %[mm]\n", "s_mov_b32 exec_lo, %[ye]\n s_mov_b32 exec_hi, 0\n global_store_dwordx2 v16, v[248:249], s[72:73] sc1\n s_mov_b64 exec, -1\n", NO),
                            T6(NO, VM(5) TRA(24, 0) TRA(25, 1024), NO), T7(TRB(26, 0) TRB(27, 1024), TRB(28, 2048) TRB(29, 3072), NO))
                   : BODY_OUT, "=&{v[224:231]}"(wf[12]), "=&{v[232:239]}"(wf[13]), "=&{v[240:247]}"(wf[14]) : BODY_IN, "{v15}"(ha), "{v16}"(yo), [ye] "s"(ye)
                   : "memory", "scc", "v248", "v249", "v250", "v251"); TS(24); }
    { BAR_VM(1); SEAM(7);
      const unsigned vm = vw ^ ((vw >> 9) & 48), mm = mb; smo = (u64)(x + (size_t)(tb * 64 + kh * 32) * 2048 + hg * 256 + cbk * 64); TS(17);
      asm volatile(BODY_TXT(T0(WOA(0), NO, NO), T1(WOA(1024), NO, NO), T2(MOX(0, 0), NO, NO), T3(MOX(1024, 32768), NO, NO), T4(NO, NO, NO), T5(NO, NO, NO), T6(NO, NO, NO), T7(VM(0) TRA(30, 0) TRA(31, 1024), NO, NO))
                   : BODY_OUT, "=&{v[248:255]}"(wf[15]) : BODY_IN : "memory", "scc"); TS(25); }
    {
    RELANE(); const unsigned vl = lane * 16;
    u32x4 o0, o1;
    const bool routed = tb == (idx >> 6) && kh == ((idx >> 5) & 1);
    u32x2 xr[4]; for (int q = 0; q < 4; ++q) xr[q] = LD(u32x2, mb + l31 * 64 + ((q ^ ((l31 >> 2) & 3)) * 16) + h2 * 8);
    const f32x2 stt = LD(f32x2, M_ST64 + (tbk * 32 + l31) * 8);
    {
        const u32x8 P = __builtin_shufflevector(PK, PK, 0, 1, 2, 3, 4, 5, 6, 7); f32x4 L = __builtin_bit_cast(f32x4, lo4(LS));
        asm volatile("s_nop 1\n\tv_smfmac_f32_16x16x64_bf16 %0, %1, %2, %3\n\ts_nop 11" : "+v"(L) : "v"(hi4(LS)), "v"(P), "v"(IDX));
        O = mfma32(hi4(V), hi4(P), O);
        const auto l16 = __builtin_amdgcn_permlane16_swap(__float_as_uint(L[0] + L[1]), __float_as_uint(L[2] + L[3]), false, false);
        float lsum = __uint_as_float(__builtin_amdgcn_permlane32_swap(l16[0], l16[0], false, false)[0]);
        const float om = fmaxf(fmaxf(fmaxf(fmaxf(fabsf(O[0]), fabsf(O[1])), fabsf(O[2])), fmaxf(fmaxf(fabsf(O[3]), fabsf(O[4])), fabsf(O[5]))), fmaxf(fmaxf(fmaxf(fabsf(O[6]), fabsf(O[7])), fabsf(O[8])),
                         fmaxf(fmaxf(fmaxf(fabsf(O[9]), fabsf(O[10])), fabsf(O[11])), fmaxf(fmaxf(fmaxf(fabsf(O[12]), fabsf(O[13])), fabsf(O[14])), fabsf(O[15])))));
        if (__builtin_expect(__any(!(lsum <= 1.8446744e19f) || !(om <= 3.0e38f)), 0)) {
            float mr = -INFINITY; lsum = 0.f; for (int i = 0; i < 16; ++i) O[i] = 0.f;
#pragma unroll 1
            for (int t = 0; t < 64; ++t) {
                u32x4 k0, k1, v0, v1;
                asm volatile("global_load_dwordx4 %0, %4, %5 sc1\n\tglobal_load_dwordx4 %1, %4, %5 offset:1024 sc1\n\tglobal_load_dwordx4 %2, %4, %6 offset:2048 sc1\n\tglobal_load_dwordx4 %3, %4, %7 offset:3072 sc1\n\ts_waitcnt vmcnt(0)"
                             : "=&v"(k0), "=&v"(k1), "=&v"(v0), "=&v"(v1) : "v"(vl), "s"(kv + ((t + 62) & 63) * 4096), "s"(kv + ((t + 63) & 63) * 4096), "s"(kv + t * 4096) : "memory");
                f32x16 z; for (int i = 0; i < 16; ++i) z[i] = 0.f;
                f32x16 s = mfma32(k0, lo4(Q), z); s = mfma32(k1, hi4(Q), s);
                float m = s[0]; for (int i = 1; i < 16; ++i) m = fmaxf(m, s[i]);
                m = fmaxf(mr, xmax32(m)); const float al = __builtin_amdgcn_exp2f(mr - m); mr = m;
                u32x8 pp; float ps = 0.f;
                for (int i = 0; i < 8; ++i) { pp[i] = pk2(__builtin_amdgcn_exp2f(s[2 * i] - m), __builtin_amdgcn_exp2f(s[2 * i + 1] - m)); ps += bf_lo(pp[i]) + bf_hi(pp[i]); }
                lsum = lsum * al + xsum32(ps); for (int i = 0; i < 16; ++i) O[i] *= al;
                O = mfma32(v0, lo4(pp), O); O = mfma32(v1, hi4(pp), O);
            }
        }
        const float rl = __builtin_amdgcn_rcpf(lsum);
        for (int i = 0; i < 4; ++i) { o0[i] = pk2(O[2 * i] * rl, O[2 * i + 1] * rl); o1[i] = pk2(O[8 + 2 * i] * rl, O[8 + 2 * i + 1] * rl); }
        asm volatile("global_store_dwordx4 %0, %1, %3 sc1\n\tglobal_store_dwordx4 %0, %2, %3 offset:2048 sc1\n\ts_nop 1"
                     :: "v"(vl), "v"(o0), "v"(o1), "s"(ws + WS_O + (size_t)(tb * 8 + hg) * 16384 + hd * 4096 + tbk * 1024) : "memory");
    }
    u64 yg[14];
    if (routed) for (int i = 0; i < 14; ++i) yg[i] = ld8_sc1(ws + WS_YG + ((h2 * 14 + i) * 1024 + hg * 128 + cbk * 32 + l31) * 8);
    TS(26);
    BAR_LDS();
    TS(27);
    const unsigned pv = w * PRIV;
    LD(u32x4, hg * PRIV + hd * 4096 + tbk * 1024 + vl) = o0; LD(u32x4, hg * PRIV + hd * 4096 + 2048 + tbk * 1024 + vl) = o1;
    unsigned xn2[8], g2[8];
    for (int i = 0; i < 8; ++i) { xn2[i] = ln2(xr[i >> 1][i & 1], stt[0], stt[1]); PIN(xn2[i]); }
    WAIT_VM(0);
    for (int i = 0; i < 14; ++i) asm volatile("" :: "v"(yg[i]));
    if (lane == 0) st8_sc1(ws + WS_FLAG4 + b * 128 + w * 8, seed);
    TS(28);
    if (routed) {
#pragma unroll 1
        for (unsigned spins = 0; spins < SPIN_CAP; ++spins) {
            bool ok = true; for (int i = 0; i < 14; ++i) ok &= (unsigned)(yg[i] >> 32) == tag32;
            if (__all(ok)) break;
            __builtin_amdgcn_s_sleep(2);
            for (int i = 0; i < 14; ++i) yg[i] = ld8_sc1(ws + WS_YG + ((h2 * 14 + i) * 1024 + hg * 128 + cbk * 32 + l31) * 8);
        }
        float acc = 0.f; for (int i = 0; i < 14; ++i) acc += __uint_as_float((unsigned)yg[i]);
        const unsigned o16 = (gkey >> 11) & 0xffffu, rbits = (o16 & 0x8000u) ? (o16 & 0x7fffu) : (~o16 & 0xffffu);
        const float rsel = rb(__builtin_amdgcn_rcpf(1.f + __builtin_amdgcn_exp2f(-1.44269504089f * bf_lo(rbits))));
        LD(float, pv + 16384 + lane * 4) = rb(rb(xsum32(acc)) * rsel);
        for (int q = 0; q < 4; ++q) { const f32x4 y = LD(f32x4, pv + 16384 + q * 32 + h2 * 16);
            for (int i = 0; i < 2; ++i) { const unsigned t = pk2(bf_lo(xn2[2 * q + i]) + y[2 * i], bf_hi(xn2[2 * q + i]) + y[2 * i + 1]); xn2[2 * q + i] = l31 == (idx & 31) ? t : xn2[2 * q + i]; } }
    }
    for (int i = 0; i < 8; ++i) { g2[i] = pk2(silu(bf_lo(xn2[i])), silu(bf_hi(xn2[i]))); PIN(g2[i]); }
    TS(31);
    if (w != hg) {
        const int pb = (((w & 3) * 8 + (tb & 7)) << 3) | ((tb >> 3) * 2 + (w >> 2));
#pragma unroll 1
        for (unsigned spins = 0; spins < SPIN_CAP; ++spins) { if (__all(ld8_sc1(ws + WS_FLAG4 + pb * 128 + (lane & 7) * 8) == seed)) break; __builtin_amdgcn_s_sleep(1); }
        TS(29);
        const char* src = ws + WS_O + (size_t)(tb * 8 + w) * 16384;
        for (int i = 0; i < 4; ++i) dma4_sc1(src + i * 4096, vl, pv + i * 4096);
    }
    BAR_VM(12);
    TS(6);
    f32x16 ao, at; for (int i = 0; i < 16; ++i) { ao[i] = 0.f; at[i] = 0.f; }
    {
        const unsigned oa = kh * 4 * PRIV + vl + kh * 1024, ob = oa ^ 1024;
        u32x4 fr[2][8]; f32x4 pt[4];
#define OAD(g, i) ((g) < 4 ? (((i) & 1) ? oa : ob) + (((g) & 1) * 2 + ((i) >> 2)) * PRIV + (((g) >> 1) * 2 + (((i) >> 1) & 1)) * 2048 : ((g) < 6 ? ob : oa) + ((i) >> 1) * PRIV + (4 + ((g) & 1) * 2 + ((i) & 1)) * 2048)
#define OWJ(g, i) ((g) < 4 ? (((g) & 1) * 2 + ((i) >> 2)) * 8 + ((g) >> 1) * 2 + (((i) >> 1) & 1) : ((i) >> 1) * 8 + 4 + ((g) & 1) * 2 + ((i) & 1))
#define ORD(p, g) do { for (int i = 0; i < 8; ++i) fr[p][i] = LD(u32x4, OAD(g, i)); } while (0)
#define WFJ(j) (((j) & 1) ? hi4(wf[(j) >> 1]) : lo4(wf[(j) >> 1]))
        __builtin_amdgcn_s_setprio(1);
        ORD(0, 0); SBAR();
#pragma unroll
        for (int g = 0; g < 8; ++g) {
            if (g == 1) asm volatile("s_waitcnt vmcnt(8)\n\ts_barrier" ::: "memory");
            if (g == 3) asm volatile("s_waitcnt vmcnt(4)\n\ts_barrier" ::: "memory");
            if (g == 4) asm volatile("s_waitcnt vmcnt(0)\n\ts_barrier" ::: "memory");
            if (g == 6) { __builtin_amdgcn_s_setprio(0); ORD(1, 7); SBAR(); for (int q = 0; q < 4; ++q) LD(f32x4, pv + 16384 + q * 1024 + vl) = (f32x4){at[4 * q], at[4 * q + 1], at[4 * q + 2], at[4 * q + 3]}; SBAR(); }
            if (g < 6) ORD((g + 1) & 1, g + 1);
            for (int i = 0; i < 8; ++i) {
                if (g < 4 ? (i & 1) : g >= 6) ao = mfma32(WFJ(OWJ(g, i)), fr[g & 1][i], ao); else at = mfma32(WFJ(OWJ(g, i)), fr[g & 1][i], at);
                if (g == 7 && i == 5) { SBAR(); __builtin_amdgcn_s_setprio(1); BAR_LDS(); for (int q = 0; q < 4; ++q) pt[q] = LD(f32x4, (w ^ 4) * PRIV + 16384 + q * 1024 + vl); SBAR(); } }
            if (g < 6) for (int i = 0; i < 8; ++i) { SGB(0x008, 1); SGB(0x100, 1); }
            SBAR();
        }
        __builtin_amdgcn_s_setprio(0);
        TS(30);
        char* op = out + (size_t)(tb * 64 + kh * 32 + l31) * 2048 + hg * 256 + cbk * 64 + h2 * 16;
        u32x2 o[4];
        for (int q = 0; q < 4; ++q) for (int i = 0; i < 2; ++i) {
            const unsigned s2 = pk2(ao[4 * q + 2 * i] + pt[q][2 * i], ao[4 * q + 2 * i + 1] + pt[q][2 * i + 1]), gg = g2[2 * q + i];
            const unsigned p2 = pk2(bf_lo(s2) * bf_lo(gg), bf_hi(s2) * bf_hi(gg));
            o[q][i] = pk2(bf_lo(xr[q][i]) + bf_lo(p2), bf_hi(xr[q][i]) + bf_hi(p2));
        }
        for (int q = 0; q < 4; q += 2) {
            const auto s0 = __builtin_amdgcn_permlane32_swap(o[q][0], o[q + 1][0], false, false), s1 = __builtin_amdgcn_permlane32_swap(o[q][1], o[q + 1][1], false, false);
            *(u32x4*)(op + q * 16) = (u32x4){s0[0], s1[0], s0[1], s1[1]};
        }
    }
    }
    TS(7);
}

extern "C" void kernel_launch(void* const* d_in, const int* in_sizes, int n_in, void* d_out, int out_size, void* d_ws, size_t ws_size, hipStream_t stream) {
    (void)hipFuncSetAttribute((const void*)fused_block, hipFuncAttributeMaxDynamicSharedMemorySize, LDS_BYTES);
    fused_block<<<256, 512, LDS_BYTES, stream>>>((const char*)d_in[0], (const char*)d_in[1], (const char*)d_in[2], (const char*)d_in[3], (const char*)d_in[4],
                                                 (const char*)d_in[6], (const char*)d_in[7], (const char*)d_in[8], (const char*)d_in[9], (char*)d_out, (char*)d_ws);
}
```

```cpp
#include <hip/hip_runtime.h>
#include <hip/hip_bf16.h>
#include <stdint.h>
#include <type_traits>

typedef unsigned long long u64;
typedef float f32x16 __attribute__((ext_vector_type(16)));
typedef float f32x4 __attribute__((ext_vector_type(4)));
typedef float f32x2 __attribute__((ext_vector_type(2)));
typedef unsigned u32x2 __attribute__((ext_vector_type(2)));
typedef unsigned u32x4 __attribute__((ext_vector_type(4)));
typedef unsigned u32x8 __attribute__((ext_vector_type(8)));
typedef unsigned u32x16 __attribute__((ext_vector_type(16)));
typedef __bf16 bf16x8 __attribute__((ext_vector_type(8)));
typedef __bf16 bf16x2 __attribute__((ext_vector_type(2)));
typedef short s16x4 __attribute__((ext_vector_type(4)));
#define LAS __attribute__((address_space(3)))
#define GAS __attribute__((address_space(1)))
#define DI __device__ __forceinline__
#define LD(T, a) (*(LAS T*)(unsigned)(a))

constexpr unsigned WS_EPOCH = 0, WS_FLAG1 = 4096, WS_STATS = 8192, WS_FLAG4 = 65536, WS_KV = 131072, WS_YG = 393216, WS_GU = 622592, WS_CS = 851968, WS_O = 1u << 20;
constexpr unsigned SLOT = 24576, L_X16 = 49152, L_WR = 81920, L_SP = 86016, L_PA = 73728, L_WKV = 98304, L_QX = 0, L_PRO = 16384, L_CSP = 98304, L_MISC = 155648, LDS_BYTES = 163840;
constexpr unsigned M_CTL = L_MISC, M_ST64 = L_MISC + 256, M_STX = L_MISC + 1280, M_CS = L_MISC + 6144, M_XSEL = L_MISC + 2560, M_H = L_MISC + 3584, M_GU = L_MISC + 4096;
constexpr unsigned KA = 122880, KB = 32768, R2 = 65536, R3 = 0, R4 = 98304, M_GR = 114688, PRIV = 20480;
constexpr int slot_of(int s) { return s % 6; }
constexpr unsigned SPIN_CAP = 1u << 18;
constexpr int CODE_SPAN = 38912;

DI float bf_lo(unsigned v) { return __uint_as_float(v << 16); }
DI float bf_hi(unsigned v) { return __uint_as_float(v & 0xffff0000u); }
DI unsigned pk2(float lo, float hi) { f32x2 v = {lo, hi}; return __builtin_bit_cast(unsigned, __builtin_convertvector(v, bf16x2)); }
DI float rb(float f) { return __uint_as_float((unsigned)__builtin_bit_cast(unsigned short, (__bf16)f) << 16); }
DI float xsum32(float v) { auto r = __builtin_amdgcn_permlane32_swap(__float_as_uint(v), __float_as_uint(v), false, false); return __uint_as_float(r[0]) + __uint_as_float(r[1]); }
DI float xmax32(float v) { auto r = __builtin_amdgcn_permlane32_swap(__float_as_uint(v), __float_as_uint(v), false, false); return fmaxf(__uint_as_float(r[0]), __uint_as_float(r[1])); }
DI f32x16 mfma32(u32x4 a, u32x4 b, f32x16 c) { return __builtin_amdgcn_mfma_f32_32x32x16_bf16(__builtin_bit_cast(bf16x8, a), __builtin_bit_cast(bf16x8, b), c, 0, 0, 0); }
DI f32x4 mfma16(u32x4 a, u32x4 b, f32x4 c) { return __builtin_amdgcn_mfma_f32_16x16x32_bf16(__builtin_bit_cast(bf16x8, a), __builtin_bit_cast(bf16x8, b), c, 0, 0, 0); }
DI u32x2 trd(unsigned a) { return __builtin_bit_cast(u32x2, __builtin_amdgcn_ds_read_tr16_b64_v4i16((LAS s16x4*)a)); }
DI u32x4 trfrag(unsigned a, unsigned step) { u32x2 lo = trd(a), hi = trd(a + step); return (u32x4){lo[0], lo[1], hi[0], hi[1]}; }
DI u32x4 lo4(u32x8 v) { return __builtin_shufflevector(v, v, 0, 1, 2, 3); }
DI u32x4 hi4(u32x8 v) { return __builtin_shufflevector(v, v, 4, 5, 6, 7); }
DI float max3(float a, float b, float c) { float r; asm("v_max3_f32 %0, %1, %2, %3" : "=v"(r) : "v"(a), "v"(b), "v"(c)); return r; }
DI float silu(float v) { return v * __builtin_amdgcn_rcpf(1.f + __builtin_amdgcn_exp2f(-1.44269504089f * v)); }
DI unsigned ln2(unsigned v, float m, float rs) { const unsigned d = pk2(bf_lo(v) - m, bf_hi(v) - m); return pk2(bf_lo(d) * rs, bf_hi(d) * rs); }
DI u64 ld8_sc1(const void* p) { return __hip_atomic_load((GAS const u64*)p, __ATOMIC_RELAXED, __HIP_MEMORY_SCOPE_AGENT); }
DI void st8_sc1(void* p, u64 v) { __hip_atomic_store((GAS u64*)p, v, __ATOMIC_RELAXED, __HIP_MEMORY_SCOPE_AGENT); }
DI int fresh_lane() { int l; asm volatile("v_mbcnt_lo_u32_b32 %0, -1, 0\n\tv_mbcnt_hi_u32_b32 %0, -1, %0" : "=v"(l)); return l; }
#define RELANE() const int lane = fresh_lane(), l15 = lane & 15, g4 = lane >> 4, l31 = lane & 31, h2 = lane >> 5; (void)l15; (void)g4; (void)l31; (void)h2
DI u64 mix(unsigned lo, unsigned hi) { u64 z = (((u64)hi << 32) | lo) + 0x9E3779B97F4A7C15ull; z = (z ^ (z >> 30)) * 0xBF58476D1CE4E5B9ull; z = (z ^ (z >> 27)) * 0x94D049BB133111EBull; return z ^ (z >> 31); }
DI unsigned rowmax16(unsigned k) { k = max(k, (unsigned)__builtin_amdgcn_update_dpp(0, (int)k, 0x128, 0xf, 0xf, false)); k = max(k, (unsigned)__builtin_amdgcn_update_dpp(0, (int)k, 0x124, 0xf, 0xf, false));
    k = max(k, (unsigned)__builtin_amdgcn_update_dpp(0, (int)k, 0x122, 0xf, 0xf, false)); return max(k, (unsigned)__builtin_amdgcn_update_dpp(0, (int)k, 0x121, 0xf, 0xf, false)); }
DI void dma1(const void* sbase, unsigned voff, unsigned m0v) { asm volatile("s_mov_b32 m0, %2\n\ts_nop 0\n\tglobal_load_lds_dwordx4 %0, %1" :: "v"(voff), "s"(sbase), "s"(m0v) : "memory"); }
DI void dma1_sc1(const void* sbase, unsigned voff, unsigned m0v) { asm volatile("s_mov_b32 m0, %2\n\ts_nop 0\n\tglobal_load_lds_dwordx4 %0, %1 sc1" :: "v"(voff), "s"(sbase), "s"(m0v) : "memory"); }
DI void dma4(const void* sbase, unsigned voff, unsigned m0v) {
    asm volatile("s_mov_b32 m0, %2\n\ts_nop 0\n\tglobal_load_lds_dwordx4 %0, %1\n\tglobal_load_lds_dwordx4 %0, %1 offset:1024\n\tglobal_load_lds_dwordx4 %0, %1 offset:2048\n\tglobal_load_lds_dwordx4 %0, %1 offset:3072"
                 :: "v"(voff), "s"(sbase), "s"(m0v) : "memory"); }
DI void dma4_sc1(const void* sbase, unsigned voff, unsigned m0v) {
    asm volatile("s_mov_b32 m0, %2\n\ts_nop 0\n\tglobal_load_lds_dwordx4 %0, %1 sc1\n\tglobal_load_lds_dwordx4 %0, %1 offset:1024 sc1\n\tglobal_load_lds_dwordx4 %0, %1 offset:2048 sc1\n\tglobal_load_lds_dwordx4 %0, %1 offset:3072 sc1"
                 :: "v"(voff), "s"(sbase), "s"(m0v) : "memory"); }
#define BAR_ALL() asm volatile("s_waitcnt vmcnt(0) lgkmcnt(0)\n\ts_barrier" ::: "memory")
#define BAR_LDS() asm volatile("s_waitcnt lgkmcnt(0)\n\ts_barrier" ::: "memory")
#define BAR_VM(n) asm volatile("s_waitcnt vmcnt(" #n ") lgkmcnt(0)\n\ts_barrier" ::: "memory")
#define WAIT_VM(n) asm volatile("s_waitcnt vmcnt(" #n ")" ::: "memory")
#define PIN(v) asm volatile("" : "+v"(v))
#define SBAR() __builtin_amdgcn_sched_barrier(0)
#define SGB(m, n) __builtin_amdgcn_sched_group_barrier(m, n, 0)

#define EX(A,i) "v_exp_f32 v[" #A "+" #i "], v[" #A "+" #i "]\n"
#define E4(A,a,b,c,d) EX(A,a) EX(A,b) EX(A,c) EX(A,d)
#define CV(p,A,i) "v_cvt_pk_bf16_f32 v[104+" #p "], v[" #A "+" #i "], v[" #A "+" #i "+1]\n"
#define RDX(dst, o, k) "ds_read_b128 " dst ", v22 offset:" o "+" #k "\n"
#define M32(x) "v_mfma_f32_32x32x16_bf16 " x "\n"
#define M16(x) "v_mfma_f32_16x16x32_bf16 " x "\n"
#define TILE(A, B, o, X1, X2, Y) \
    M32("v[" #B ":" #B "+15], v[112:115], v[96:99], v[64:79]") RDX("v[112:115]", o, 0) X1 E4(A,0,1,2,3) \
    M32("v[80:95], v[124:127], v[108:111], v[80:95]") RDX("v[124:127]", o, 3072) E4(A,4,5,6,7) \
    "v_smfmac_f32_16x16x64_bf16 v[24:27], v[28:31], v[104:111], v23\n" "s_waitcnt lgkmcnt(2)\n" CV(0,A,0) CV(1,A,2) CV(2,A,4) CV(3,A,6) \
    M32("v[" #B ":" #B "+15], v[116:119], v[100:103], v[" #B ":" #B "+15]") RDX("v[116:119]", o, 1024) X2 E4(A,8,9,10,11) \
    M32("v[80:95], v[120:123], v[104:107], v[80:95]") RDX("v[120:123]", o, 2048) E4(A,12,13,14,15) Y \
    "s_waitcnt lgkmcnt(2)\n" CV(4,A,8) CV(5,A,10) CV(6,A,12) CV(7,A,14)
#define NO ""
#define WOA(k) "s_add_u32 m0, %[ma], " #k "\n s_add_u32 s70, s70, 0x8000\n s_addc_u32 s71, s71, 0\n global_load_lds_dwordx4 v20, s[70:71]\n"
#define WOB(k) "s_add_u32 m0, %[mb], " #k "\n s_add_u32 s70, s70, 0x8000\n s_addc_u32 s71, s71, 0\n global_load_lds_dwordx4 v20, s[70:71]\n"
#define MOE(k, st) "s_add_u32 m0, %[mm], " #k "\n s_add_u32 s72, s72, " #st "\n s_addc_u32 s73, s73, 0\n global_load_lds_dwordx4 v19, s[72:73] nt\n"
#define MOX(k, st) "s_add_u32 m0, %[mm], " #k "\n s_add_u32 s72, s72, " #st "\n s_addc_u32 s73, s73, 0\n global_load_lds_dwordx4 v19, s[72:73]\n"
#define KV0 "s_mov_b32 m0, %[mk]\n s_nop 0\n global_load_lds_dwordx4 v21, %[sk] sc1\n"
#define KVN(k) "global_load_lds_dwordx4 v21, %[sk] offset:" #k " sc1\n"
#define GRD "s_mov_b32 m0, %[mm]\n s_nop 0\n global_load_lds_dwordx4 v21, s[72:73] sc1\n"
#define TR2(d, a, k, st) "ds_read_b64_tr_b16 v[" #d ":" #d "+1], " a " offset:" #k "\n ds_read_b64_tr_b16 v[" #d "+2:" #d "+3], " a " offset:" #k "+" #st "\n"
#define TRA(j, k) TR2(128+4*j, "v18", k, 512)
#define TRB(j, k) TR2(128+4*j, "v17", k, 512)
#define B64(d, a, k) "ds_read_b64 v[" #d ":" #d "+1], " a " offset:" #k "\n"
#define VM(n) "s_waitcnt vmcnt(" #n ")\n"
#define BODY_TXT(T0, T1, T2, T3, T4, T5, T6, T7) "s_nop 1\n s_setprio 3\n" T0 T1 T2 T3 T4 "s_setprio 2\n" T5 T6 "s_setprio 1\n" T7 "s_waitcnt lgkmcnt(0)\n s_nop 7\n"
#define PH "s_setprio 0\n"
#define T0(a,b,c) TILE(32,48,"0",a,b,c)
#define T1(a,b,c) TILE(48,32,"4096",a,b,c)
#define T2(a,b,c) TILE(32,48,"8192",a,b,c)
#define T3(a,b,c) TILE(48,32,"12288",a,b,c)
#define T4(a,b,c) TILE(32,48,"16384",a,b,c)
#define T5(a,b,c) TILE(48,32,"20480",a,b,c)
#define T6(a,b,c) TILE(32,48,"24576",a,b,c)
#define T7(a,b,c) TILE(48,32,"28672",a,PH b,c)
#define BODY_OUT "+{v[32:47]}"(SA), "=&{v[48:63]}"(SB), "+{v[80:95]}"(O), "+{v[24:31]}"(LS), "+{v[104:119]}"(PK), "+{v[120:127]}"(V), "+{s[70:71]}"(swo), "+{s[72:73]}"(smo)
#define BODY_IN "{v[64:79]}"(NEG), "{v[96:103]}"(Q), "{v23}"(IDX), "{v22}"(ad), "{v21}"(vl), "{v20}"(vw), "{v19}"(vm), "{v18}"(ta), "{v17}"(tb_), \
      [ma] "s"(ma), [mb] "s"(mb), [mm] "s"(mm), [mk] "s"(mk), [sk] "s"(sk)
#define C8(a,b,c,d,e,f,g,h) "v" #a, "v" #b, "v" #c, "v" #d, "v" #e, "v" #f, "v" #g, "v" #h

__global__ __launch_bounds__(512, 2) void fused_block(const char* __restrict__ x, const char* __restrict__ wq, const char* __restrict__ wk, const char* __restrict__ wv,
                                                      const char* __restrict__ wo, const char* __restrict__ wr, const char* __restrict__ wg, const char* __restrict__ wu,
                                                      const char* __restrict__ wd, char* __restrict__ out, char* __restrict__ ws) {
    const int tid = threadIdx.x, lane = tid & 63, w = __builtin_amdgcn_readfirstlane(tid >> 6);
    const int b = blockIdx.x, xcd = b & 7, r = b >> 3, tb = (xcd >> 1) * 8 + (r & 7), hg = (xcd & 1) * 4 + (r >> 3);
    const int isV = r >> 4, sub = (r >> 2) & 3, T0 = (xcd * 4 + (r & 3)) * 64 + sub * 16;
    const int l15 = lane & 15, g4 = lane >> 4, l31 = lane & 31, h2 = lane >> 5;
    const int hd = w & 3, tbk = w >> 2;
    const unsigned vl = lane * 16;
    {
        u64 pc; asm volatile("s_getpc_b64 %0" : "=s"(pc));
        asm volatile("s_mov_b32 m0, %2\n\ts_mov_b64 exec, 3\n\tglobal_load_lds_dword %0, %1\n\ts_mov_b64 exec, -1"
                     :: "v"(lane * 128), "s"((pc & ~127ull) + min((r * 8 + w) * 256, CODE_SPAN - 256)), "s"(w < 4 ? L_X16 + w * 8192 : L_WKV + (w - 4) * 16384) : "memory");
    }
    const int c0 = r & 7, rm = min(r, 27), mi = xcd * 28 + rm; const char* kv = ws + WS_KV;
    f32x16 qa;
    float aone; u32x8 LS, PZ; f32x16 O; unsigned IDX, vw, vm0, ta, tb_;
    const unsigned ma = R4 + w * 2048, mb = R3 + w * 4096;
    u64 swo, smo, sk0;
#define SETUP() do { swo = (u64)(wo + (size_t)tbk * 512 * 2048 + hg * 256 + hd * 64) - 0x8000; smo = (u64)(wg + (size_t)((mi & 3) * 256 + w * 32) * 7168 + (mi >> 2) * 128); \
        sk0 = (u64)(kv + ((c0 + 1) & 7) * 32768 + w * 4096); asm volatile("" : "+s"(swo), "+s"(smo), "+s"(sk0) : "s"(out), "s"(wu), "s"(wd)); aone = h2 ? 0.f : 1.f; PIN(aone); \
        const unsigned one_ = 0x3F803F80u; LS = (u32x8){0u, 0u, 0u, 0u, l15 < 2 ? one_ : 0u, l15 < 2 ? one_ : 0u, (l15 & 14) == 2 ? one_ : 0u, (l15 & 14) == 2 ? one_ : 0u}; \
        IDX = (lane & 1) ? 0xEEEEEEEEu : 0x44444444u; vw = (lane >> 2) * 2048 + (lane & 3) * 16; vm0 = (lane >> 3) * 7168 + (((lane & 7) ^ (g4 << 1)) << 4); \
        ta = ma + (4 * h2 + (l15 >> 2)) * 64 + (g4 & 1) * 32 + (lane & 3) * 8; tb_ = ta + (mb - ma); for (int i = 0; i < 16; ++i) O[i] = 0.f; PZ = (u32x8){0u, 0u, 0u, 0u, 0u, 0u, 0u, 0u}; PIN(O); PIN(PZ); PIN(LS); PIN(IDX); PIN(vw); PIN(vm0); PIN(ta); PIN(tb_); } while (0)
    if (__builtin_expect(w < 4, 1)) {
        __builtin_amdgcn_s_setprio(3);
        u32x2 ev; u32x4 xq[8], aw[8], sel, ones; unsigned wa, wa2, spa, kvo, wb[3][2], xb_[3][4];
        {
            const char* xs = x + (size_t)T0 * 2048 + w * 512;
            for (int i = 0; i < 8; ++i) dma1(xs, (2 * i + h2) * 2048 + ((l31 ^ (2 * i + h2)) << 4), L_X16 + w * 8192 + i * 1024);
            dma1(wr + w * 512, l31 * 16, L_WR + w * 1024);
            asm volatile("s_mov_b64 exec, 1\n\tglobal_load_dwordx2 %0, %1, %2 sc1\n\ts_mov_b64 exec, -1" : "=v"(ev) : "v"(0), "s"(ws));
            SETUP();
            wa = L_WKV + w * 16384 + (8 * g4 + (l15 >> 2)) * 64 + (g4 & 1) * 32 + (lane & 3) * 8; wa2 = wa ^ 32; spa = L_SP + ((l15 + (isV ? 4 * g4 : 0)) & 15) * 16;
            const int kt = T0 >> 5, sh = sub & 1;
            kvo = isV ? ((kt - 1 + sh) & 63) * 4096 + 2048 + sh * 1024 + ((g4 & 1) * 32 + w * 16 + l15) * 16 + (g4 >> 1) * 8
                      : ((kt - 2) & 63) * 4096 + w * 1024 + ((g4 & 1) * 32 + sh * 16 + l15) * 16 + (g4 >> 1) * 8;
            const unsigned sv = (((l15 >> 2) & 1) == (g4 & 1)) ? 0x3F803F80u : 0u, xq_a = l31 * 128 + ((h2 ^ ((l31 >> 1) & 7)) << 4);
            const unsigned wq_a = 8192 + (8 * h2 + (l15 >> 2)) * 256 + ((hd ^ ((lane >> 2) & 3)) << 6) + (g4 & 1) * 32 + (lane & 3) * 8;
            sel = (u32x4){sv, sv, sv, sv}; ones = (u32x4){0x3F803F80u, 0x3F803F80u, 0x3F803F80u, 0x3F803F80u};
            for (int i = 0; i < 3; ++i) {
                wb[i][0] = wq_a + i * 2 * SLOT + (tb & 1) * 8192; wb[i][1] = wq_a + i * 2 * SLOT + (~tb & 1) * 8192; PIN(wb[i][0]); PIN(wb[i][1]);
                for (int ks = 0; ks < 4; ++ks) { xb_[i][ks] = (xq_a ^ (ks * 32) ^ ((tb & 1) * 64)) + i * 2 * SLOT; PIN(xb_[i][ks]); } }
            const unsigned oth = l15 == 1 ? 0x3F803F80u : 0u;
            for (int s = 0; s < 8; ++s) { aw[s] = (u32x4){oth, oth, oth, oth}; PIN(aw[s]); }
            PIN(kvo); PIN(wa); PIN(wa2); PIN(spa); PIN(ones); PIN(sel);
            WAIT_VM(1);
            const unsigned xa = L_X16 + w * 8192 + l15 * 512 + ((g4 ^ l15) << 4);
            for (int s = 0; s < 8; ++s) xq[s] = LD(u32x4, xa ^ (s * 64));
            if (l15 == 0) for (int s = 0; s < 8; ++s) aw[s] = LD(u32x4, L_WR + w * 1024 + s * 64 + g4 * 16);
        }
        const f32x4 z4 = {0, 0, 0, 0};
        f32x4 s1 = z4, gr = z4, sw = z4;
        for (int s = 0; s < 8; ++s) { gr = mfma16(xq[s], xq[s], gr); s1 = mfma16(aw[s], xq[s], s1); sw = mfma16(aw[s], ones, sw); }
        { float d = (l15 & 2) ? ((l15 & 1) ? gr[3] : gr[2]) : ((l15 & 1) ? gr[1] : gr[0]); d = g4 == (l15 >> 2) ? d : 0.f;
          const auto e16 = __builtin_amdgcn_permlane16_swap(__float_as_uint(d), __float_as_uint(d), false, false); d = xsum32(__uint_as_float(e16[0]) + __uint_as_float(e16[1]));
          if (lane < 16) LD(f32x4, L_SP + w * 256 + vl) = (f32x4){s1[1], d, s1[0], sw[0]}; }
        BAR_LDS();
        f32x4 m4, r4; unsigned key = 0;
        {
            f32x4 p[4]; for (int q = 0; q < 4; ++q) p[q] = LD(f32x4, spa + q * 256);
            const f32x4 t = (p[0] + p[1]) + (p[2] + p[3]);
            const float mean = t[0] * (1.f / 1024.f), var = fmaxf(t[1] * (1.f / 1024.f) - mean * mean, 0.f);
            const float mrow = rb(mean), rsrow = __builtin_amdgcn_rcpf(rb(__builtin_amdgcn_sqrtf(rb(rb(var) + 1e-5f))));
            const int mi_ = (int)__float_as_uint(mrow), ri_ = (int)__float_as_uint(rsrow);
#define BC(v, n) __uint_as_float((unsigned)__builtin_amdgcn_update_dpp(0, v, 0x150 + (n), 0xf, 0xf, false))
            m4 = (f32x4){BC(mi_, 0), BC(mi_, 1), BC(mi_, 2), BC(mi_, 3)}; r4 = (f32x4){BC(ri_, 0), BC(ri_, 1), BC(ri_, 2), BC(ri_, 3)};
            if (!isV) { m4 = (f32x4){mrow, mrow, mrow, mrow}; r4 = (f32x4){rsrow, rsrow, rsrow, rsrow};
                if (w == 2) { const unsigned u = __float_as_uint(rb((t[2] - mrow * t[3]) * rsrow)) >> 16;
                    key = rowmax16(((((u & 0x8000u) ? ~u : (u | 0x8000u)) & 0xffffu) << 11) | (2047 - (T0 + l15))); } }
        }
        PIN(m4); PIN(r4);
        BAR_LDS();
        auto kvpart = [&](auto V_) {
            constexpr bool V = decltype(V_)::value;
            u32x4 w0[8], w1[8]; f32x4 a0 = z4, a1 = z4, c0_ = z4, c1_ = z4;
            for (int s = 0; s < 3; ++s) { w0[s] = trfrag(wa + s * 2048, 256); w1[s] = trfrag(wa2 + s * 2048, 256); }
            SBAR();
#pragma unroll
            for (int s = 0; s < 8; ++s) {
                if (s < 5) { w0[s + 3] = trfrag(wa + (s + 3) * 2048, 256); w1[s + 3] = trfrag(wa2 + (s + 3) * 2048, 256); }
                if (V) { a0 = mfma16(xq[s], w0[s], a0); c0_ = mfma16(ones, w0[s], c0_); a1 = mfma16(xq[s], w1[s], a1); c1_ = mfma16(ones, w1[s], c1_); }
                else { a0 = mfma16(w0[s], xq[s], a0); c0_ = mfma16(w0[s], ones, c0_); a1 = mfma16(w1[s], xq[s], a1); c1_ = mfma16(w1[s], ones, c1_); }
                if (s < 5) for (int i = 0; i < 4; ++i) { SGB(0x008, 1); SGB(0x100, 1); }
                SBAR();
            }
            LD(f32x4, L_PA + w * 2048 + vl) = (a0 - m4 * c0_) * r4; LD(f32x4, L_PA + w * 2048 + 1024 + vl) = (a1 - m4 * c1_) * r4;
        };
        if (isV) kvpart(std::true_type{}); else kvpart(std::false_type{});
        asm volatile("s_waitcnt vmcnt(0)" : "+v"(ev) :: "memory");
        if (tid == 192) { const u64 sd = mix(ev[0], ev[1]); LD(u32x4, M_CTL + 16) = (u32x4){ev[0], ev[1], 0u, 0u}; LD(u64, M_CTL) = sd; }
        BAR_LDS();
        {
            const f32x4 cs = mfma16(sel, trfrag(wb[0][0] + 4096, 1024), mfma16(sel, trfrag(wb[0][0], 1024), z4));
            asm volatile("s_nop 7\n\ts_mov_b32 exec_hi, 0\n\tglobal_store_dword %0, %1, %2 sc1\n\ts_mov_b64 exec, -1" :: "v"(lane * 4), "v"(cs[0]), "s"(ws + WS_CS + (hg * 32 + tb) * 512 + w * 128) : "memory");
        }
        if (w < 2) {
            f32x4 p[4]; for (int i = 0; i < 4; ++i) p[i] = LD(f32x4, L_PA + i * 2048 + w * 1024 + vl);
            const f32x4 a = (p[0] + p[1]) + (p[2] + p[3]);
            st8_sc1(ws + WS_KV + kvo, ((u64)pk2(a[2], a[3]) << 32) | pk2(a[0], a[1]));
        } else if (w == 2 && !isV && lane < 16) st8_sc1(ws + WS_STATS + (T0 + l15) * 8, ((u64)__float_as_uint(r4[0]) << 32) | __float_as_uint(m4[0]));
        f32x16 qb; qa = O; qb = O;
        u32x4 af[2][4], b0[2][4], b1[2][4];
#define QRD(p, s) do { const unsigned sl_ = (slot_of(s) & 1) * SLOT; const int bi_ = slot_of(s) >> 1; for (int ks = 0; ks < 4; ++ks) { \
            af[p][ks] = trfrag(wb[bi_][ks >> 1] + sl_ + (ks & 1) * 4096, 1024); b1[p][ks] = LD(u32x4, xb_[bi_][ks] + sl_ + 4096); b0[p][ks] = LD(u32x4, xb_[bi_][ks] + sl_); } } while (0)
#pragma unroll
        for (int s = 0; s < 16; ++s) {

            if (s == 2 || s == 10) WAIT_VM(0);
            if (!(s & 1)) { BAR_LDS(); }
            if (s == 0) { QRD(0, 0); BAR_LDS(); }
            if (s == 2 && tid == 128) st8_sc1(ws + WS_FLAG1 + b * 8, (LD(u64, M_CTL) & ~0xFFFFFFFull) | key);
            if (s == 8 && w >= 2) dma1_sc1(ws + WS_FLAG1 + (w - 2) * 1024, vl, M_GU + (w - 2) * 1024);
            if (s < 15) {
                QRD((s + 1) & 1, s + 1);
                for (int ks = 0; ks < 4; ++ks) { qb = mfma32(af[s & 1][ks], b1[s & 1][ks], qb); qa = mfma32(af[s & 1][ks], b0[s & 1][ks], qa); }
                for (int i = 0; i < 8; ++i) { SGB(0x008, 1); SGB(0x100, 2); }
            } else {
                for (int ks = 0; ks < 4; ++ks) qb = mfma32(af[1][ks], b1[1][ks], qb);
                SBAR();
                qa = mfma32(af[1][0], b0[1][0], qa); qa = mfma32(af[1][1], b0[1][1], qa); SBAR();
                for (int ks = 0; ks < 4; ++ks) { LD(f32x4, L_QX + w * 4096 + ks * 1024 + vl) = (f32x4){qb[4 * ks], qb[4 * ks + 1], qb[4 * ks + 2], qb[4 * ks + 3]}; SBAR();
                    if (ks & 1) { qa = mfma32(af[1][2 + (ks >> 1)], b0[1][2 + (ks >> 1)], qa); SBAR(); } }
            }
            SBAR();
        }
        BAR_LDS();
    } else {
        const int lw = w - 4, koff = tb >> 1;
        __builtin_amdgcn_s_sleep(10);
        { const char* wkv = (isV ? wv : wk) + lw * 16384;
          for (int i = 0; i < 4; ++i) dma4(wkv + i * 4096, vl ^ (h2 << 5), L_WKV + lw * 16384 + i * 4096); }
        unsigned vx[2], vq[4];
        for (int i = 0; i < 2; ++i) { const int row = lw * 16 + i * 8 + (lane >> 3); vx[i] = row * 2048 + (((lane & 7) ^ ((row >> 1) & 7)) << 4); }
        for (int i = 0; i < 4; ++i) vq[i] = (lw * 16 + i * 4 + g4) * 2048 + ((((lane >> 2) & 3) ^ g4) << 6) + ((lane & 3) << 4);
        const char* xb = x + (size_t)tb * 64 * 2048; const char* qb_ = wq + hg * 256;
#define RING_X(s) asm volatile("s_mov_b32 m0, %3\n\ts_nop 0\n\tglobal_load_lds_dwordx4 %0, %2\n\ts_add_u32 m0, m0, 1024\n\ts_nop 0\n\tglobal_load_lds_dwordx4 %1, %2" \
                     :: "v"(vx[0]), "v"(vx[1]), "s"(xb + (((s) + koff) & 15) * 128), "s"(slot_of(s) * SLOT + lw * 2048) : "memory", "scc")
#define RING_W(s) asm volatile("s_mov_b32 m0, %5\n\ts_nop 0\n\tglobal_load_lds_dwordx4 %0, %4\n\ts_add_u32 m0, m0, 1024\n\ts_nop 0\n\tglobal_load_lds_dwordx4 %1, %4\n\t" \
                     "s_add_u32 m0, m0, 1024\n\ts_nop 0\n\tglobal_load_lds_dwordx4 %2, %4\n\ts_add_u32 m0, m0, 1024\n\ts_nop 0\n\tglobal_load_lds_dwordx4 %3, %4" \
                     :: "v"(vq[0]), "v"(vq[1]), "v"(vq[2]), "v"(vq[3]), "s"(qb_ + (size_t)(((s) + koff) & 15) * 131072), "s"(slot_of(s) * SLOT + 8192 + lw * 4096) : "memory", "scc")
#define RING_ISSUE(s) do { RING_X(s); RING_W(s); } while (0)
        RING_ISSUE(0);
        BAR_LDS(); RING_ISSUE(1);
        BAR_VM(12); RING_ISSUE(2); RING_W(3);
        BAR_VM(16); RING_ISSUE(4); RING_ISSUE(5);
        BAR_VM(16); RING_X(3);
        BAR_LDS(); RING_ISSUE(6);
        BAR_VM(6); RING_ISSUE(7); RING_ISSUE(8);
        BAR_VM(12); RING_ISSUE(9); RING_ISSUE(10);
        BAR_VM(12); RING_ISSUE(11); RING_ISSUE(12);
        BAR_VM(12);
        RING_ISSUE(13); RING_ISSUE(14);
        const u64 tag36 = LD(u64, M_CTL) & ~0xFFFFFFFull;
        BAR_VM(12); RING_ISSUE(15);
        u64 f[4]; { const u32x4 f0 = LD(u32x4, M_GU + vl), f1 = LD(u32x4, M_GU + 1024 + vl); f[0] = ((u64)f0[1] << 32) | f0[0]; f[1] = ((u64)f0[3] << 32) | f0[2]; f[2] = ((u64)f1[1] << 32) | f1[0]; f[3] = ((u64)f1[3] << 32) | f1[2]; }
#define FLAGS_OK() __all((((f[0] ^ tag36) | (f[1] ^ tag36) | (f[2] ^ tag36) | (f[3] ^ tag36)) >> 28) == 0)
        bool early = FLAGS_OK();
        if (!early) { for (int i = 0; i < 4; ++i) f[i] = ld8_sc1(ws + WS_FLAG1 + (i * 64 + lane) * 8); BAR_VM(6); early = FLAGS_OK(); }
        else BAR_VM(6);
        if (!early) {
            BAR_VM(0);
#pragma unroll 1
            for (unsigned spins = 0; spins < SPIN_CAP; ++spins) {
                for (int i = 0; i < 4; ++i) f[i] = ld8_sc1(ws + WS_FLAG1 + (i * 64 + lane) * 8);
                if (FLAGS_OK()) break;
                __builtin_amdgcn_s_sleep(2);
            }
            PIN(f[0]); PIN(f[1]); PIN(f[2]); PIN(f[3]);
        }
        for (int i = 0; i < 4; ++i) dma1_sc1(ws + WS_CS + hg * 16384 + i * 4096 + lw * 128, (lane >> 3) * 512 + (lane & 7) * 16, L_CSP + lw * 4096 + i * 1024);
        if (lw < 2) { dma4_sc1(kv + ((c0 * 8 + 62 + lw) & 63) * 4096, vl, L_PRO + lw * 4096); if (early) BAR_VM(8); }
        else if (lw == 2) { dma1_sc1(ws + WS_STATS + tb * 512, l31 * 16, M_ST64); if (early) BAR_VM(5); }
        else {
            unsigned key = rowmax16(max(max((unsigned)f[0] & 0xFFFFFFFu, (unsigned)f[1] & 0xFFFFFFFu), max((unsigned)f[2] & 0xFFFFFFFu, (unsigned)f[3] & 0xFFFFFFFu)));
            key = max(max((unsigned)__builtin_amdgcn_readlane(key, 0), (unsigned)__builtin_amdgcn_readlane(key, 16)), max((unsigned)__builtin_amdgcn_readlane(key, 32), (unsigned)__builtin_amdgcn_readlane(key, 48)));
            const int ix = 2047 - (key & 2047);
            dma1(x + (size_t)ix * 2048 + (mi & 3) * 512, l31 * 16, M_XSEL);
            asm volatile("s_mov_b32 m0, %2\n\ts_nop 0\n\tglobal_load_lds_dword %0, %1 sc1" :: "v"((lane & 1) * 4), "s"(ws + WS_STATS + ix * 8), "s"(M_STX) : "memory");
            if (early) BAR_VM(6);
            if (lane == 0) { LD(unsigned, M_CTL + 8) = key; if (b == 0) st8_sc1(ws + WS_EPOCH, LD(u64, M_CTL + 16) + 1); }
        }
        dma4_sc1(kv + c0 * 32768 + lw * 8192, vl, KA + lw * 8192); dma4_sc1(kv + c0 * 32768 + lw * 8192 + 4096, vl, KA + lw * 8192 + 4096);
        SETUP();
        WAIT_VM(8);
        { float p[16]; for (int i = 0; i < 16; ++i) p[i] = LD(float, L_CSP + lw * 4096 + (h2 * 16 + i) * 128 + l31 * 4);
          const float csum = xsum32((((p[0] + p[1]) + (p[2] + p[3])) + ((p[4] + p[5]) + (p[6] + p[7]))) + (((p[8] + p[9]) + (p[10] + p[11])) + ((p[12] + p[13]) + (p[14] + p[15]))));
          const float c1 = rb(csum), c2 = rb(csum - c1); const u32x2 csp = (u32x2){pk2(c1, c2), pk2(csum - c1 - c2, 0.f)};
          LD(u32x2, M_CS + lw * 512 + lane * 8) = h2 ? (u32x2){0u, 0u} : csp; }
        __builtin_amdgcn_s_setprio(3);
        BAR_LDS();
        for (int q = 0; q < 4; ++q) { const f32x4 t = LD(f32x4, L_QX + hd * 4096 + q * 1024 + vl); for (int i = 0; i < 4; ++i) qa[4 * q + i] = t[i]; }
    }
    u32x8 Q, V; u32x16 PK; f32x16 SA, SB, NEG;
    const u32x4 ctl = LD(u32x4, M_CTL);
    {
        const f32x2 st = LD(f32x2, M_ST64 + (tbk * 32 + l31) * 8); const u32x2 csp = LD(u32x2, M_CS + hd * 512 + lane * 8);
        const u32x4 k00 = LD(u32x4, L_PRO + vl), k01 = LD(u32x4, L_PRO + 1024 + vl), k10 = LD(u32x4, L_PRO + 4096 + vl), k11 = LD(u32x4, L_PRO + 5120 + vl), v00 = LD(u32x4, L_PRO + 6144 + vl);
        const unsigned nm = (__float_as_uint(st[0]) ^ 0x80000000u) >> 16;
        const u32x4 ca = {csp[0], csp[1], PZ[0], PZ[1]}, cb = {nm * 0x10001u, nm, PZ[0], PZ[1]};
        qa = mfma32(ca, cb, qa);
        const float qs = st[1] * (0.17677669529663687f * 1.4426950408889634f);
        for (int i = 0; i < 8; ++i) Q[i] = pk2(qa[2 * i] * qs, qa[2 * i + 1] * qs);
        f32x16 s0 = mfma32(k00, lo4(Q), O); s0 = mfma32(k01, hi4(Q), s0);
        for (int i = 0; i < 4; ++i) { PK[i] = PZ[i]; PK[4 + i] = PZ[4 + i]; PK[8 + i] = k10[i]; PK[12 + i] = k11[i]; V[i] = v00[i]; V[4 + i] = k00[i]; }
        asm volatile("s_nop 7\n\ts_nop 3" : "+v"(s0));
        float m = max3(max3(s0[0], s0[1], s0[2]), max3(s0[3], s0[4], s0[5]), s0[15]);
        m = max3(max3(max3(s0[6], s0[7], s0[8]), max3(s0[9], s0[10], s0[11]), max3(s0[12], s0[13], s0[14])), m, m);
        const auto mm_ = __builtin_amdgcn_permlane32_swap(__float_as_uint(m), __float_as_uint(m), false, false);
        m = max3(__uint_as_float(mm_[0]), __uint_as_float(mm_[1]), __uint_as_float(mm_[1]));
        NEG = __builtin_amdgcn_mfma_f32_32x32x2f32(aone, -m, O, 0, 0, 0);
        PIN(NEG);
        for (int i = 0; i < 16; ++i) SA[i] = s0[i] - m;
        PIN(SA);
    }
    const u64 seed = ((u64)(unsigned)__builtin_amdgcn_readfirstlane(ctl[1]) << 32) | (unsigned)__builtin_amdgcn_readfirstlane(ctl[0]);
    const unsigned tag32 = (unsigned)seed ^ 0x5bd1e995u, gkey = __builtin_amdgcn_readfirstlane(ctl[2]);
    const int idx = 2047 - (gkey & 2047), cbk = hd, kh = tbk;
    const bool moe = r < 28;
    u32x8 wf[16];
#define SEAM(c) const unsigned ad = (((c) & 1) ? KB : KA) + vl, mk = (((c) & 1) ? KA : KB) + w * 4096; const char* sk = kv + ((c0 + (c) + 1) & 7) * 32768 + w * 4096
#define MROW() (16 * (g4 >> 1) + 4 * (g4 & 1) + (l15 >> 2))
#define MSWZ() (2 * (g4 & 1) + (l15 >> 3))
    { const unsigned ad = KA + vl, mk = KB + w * 4096, vm = vm0, mm = R2 + w * 4096; const u64 sk = sk0;
      asm volatile("s_waitcnt vmcnt(0) lgkmcnt(0)\n\ts_barrier" :: "{v22}"(ad), "s"(seed), "s"(gkey), "s"(mk), "s"(sk) : "memory");
      asm volatile(BODY_TXT(T0(WOA(0), NO, NO), T1(WOA(1024), NO, NO), T2(KV0, KVN(1024), NO), T3(KVN(2048), KVN(3072), NO), T4(MOE(0, 0), MOE(1024, 57344), NO), T5(MOE(2048, 57344), MOE(3072, 57344), NO),
                            T6(NO, NO, NO), T7(VM(8) TRA(0, 0) TRA(1, 1024), NO, NO))
                   : BODY_OUT, "=&{v[128:135]}"(wf[0]) : BODY_IN : "memory", "scc"); }
    { BAR_VM(4); SEAM(1); const unsigned vm = vm0, mm = mb; smo = (u64)(wu + (size_t)((mi & 3) * 256 + w * 32) * 7168 + (mi >> 2) * 128);
      asm volatile(BODY_TXT(T0(WOA(0), NO, NO), T1(WOA(1024), NO, NO), T2(KV0, KVN(1024), NO), T3(KVN(2048), KVN(3072), NO), T4(MOE(0, 0), MOE(1024, 57344), NO), T5(MOE(2048, 57344), MOE(3072, 57344), NO),
                            T6(NO, NO, NO), T7(VM(8) TRA(2, 0) TRA(3, 1024), NO, NO))
                   : BODY_OUT, "=&{v[136:143]}"(wf[1]) : BODY_IN : "memory", "scc"); }
    { if (w == 0) { RELANE(); const f32x2 sx = LD(f32x2, M_STX); const u32x2 v = LD(u32x2, M_XSEL + lane * 8); LD(u32x2, M_XSEL + lane * 8) = (u32x2){ln2(v[0], sx[0], sx[1]), ln2(v[1], sx[0], sx[1])}; }
      BAR_VM(4); SEAM(2); const unsigned vm = 0, mm = 0;
      asm volatile(BODY_TXT(T0(WOA(0), NO, NO), T1(WOA(1024), NO, NO), T2(KV0, NO, NO), T3(KVN(1024), NO, NO), T4(KVN(2048), NO, NO), T5(KVN(3072), NO, NO), T6(NO, NO, NO), T7(VM(4) TRA(4, 0) TRA(5, 1024), NO, NO))
                   : BODY_OUT, "=&{v[144:151]}"(wf[2]) : BODY_IN : "memory", "scc"); }
    { RELANE(); const unsigned vm = R2 + w * 4096 + MROW() * 128 + MSWZ() * 32 + (lane & 3) * 8, mm = 0;
      const unsigned xa = M_XSEL + w * 64 + (g4 >> 1) * 32 + (g4 & 1) * 8, gw = M_GU + w * 256 + lane * 4;
      BAR_VM(0); SEAM(3);
#define GM(d, a) M16("v[" #d ":" #d "+3], v[176:179], v[" #a ":" #a "+3], 0")
      asm volatile(BODY_TXT(T0(WOA(0) "v_xor_b32 v244, 32, v19\n v_xor_b32 v245, 64, v19\n v_xor_b32 v246, 0x60, v19\n v_subrev_u32 v247, 0x10000, v19\n" B64(176, "v15", 0) B64(178, "v15", 16) TR2(180, "v19", 0, 1024),
                               WOA(1024) TR2(184, "v244", 0, 1024) TR2(188, "v245", 0, 1024) "v_subrev_u32 v244, 0x10000, v244\n v_subrev_u32 v245, 0x10000, v245\n", NO),
                            T1(TR2(192, "v246", 0, 1024) TR2(196, "v247", 0, 1024) "v_subrev_u32 v246, 0x10000, v246\n", TR2(200, "v244", 0, 1024) TR2(204, "v245", 0, 1024), GM(212, 180) GM(216, 184)),
                            T2(TR2(208, "v246", 0, 1024) KV0, KVN(1024), GM(220, 188) GM(224, 192)), T3(KVN(2048) KVN(3072), WOB(0), GM(228, 196) GM(232, 200)), T4(WOB(1024), WOB(2048), GM(236, 204) GM(240, 208)),
                            T5(WOB(3072), NO, NO),
                            T6("s_mov_b32 vcc_lo, 0xffff0000\n s_mov_b32 vcc_hi, 0xffff0000\n v_cndmask_b32 v212, v212, v216, vcc\n v_cndmask_b32 v220, v220, v224, vcc\n v_cndmask_b32 v228, v228, v232, vcc\n"
                               "v_cndmask_b32 v236, v236, v240, vcc\n s_mov_b32 vcc_lo, 0\n s_mov_b32 vcc_hi, -1\n v_cndmask_b32 v212, v212, v220, vcc\n v_cndmask_b32 v228, v228, v236, vcc\n",
                               "ds_write_b32 v16, v212\n ds_write_b32 v16, v228 offset:2048\n" VM(8) TRA(6, 0) TRA(7, 1024), NO),
                            T7(VM(0) TRB(8, 0) TRB(9, 1024), TRB(10, 2048) TRB(11, 3072), NO))
                   : BODY_OUT, "=&{v[152:159]}"(wf[3]), "=&{v[160:167]}"(wf[4]), "=&{v[168:175]}"(wf[5]) : BODY_IN, "{v15}"(xa), "{v16}"(gw)
                   : "memory", "scc", C8(176,177,178,179,180,181,182,183), C8(184,185,186,187,188,189,190,191), C8(192,193,194,195,196,197,198,199), C8(200,201,202,203,204,205,206,207),
                     C8(208,209,210,211,212,213,214,215), C8(216,217,218,219,220,221,222,223), C8(224,225,226,227,228,229,230,231), C8(232,233,234,235,236,237,238,239), C8(240,241,242,243,244,245,246,247), "vcc"); }
    const unsigned ye = moe ? 0xFFFFu : 0u;
    { RELANE(); const unsigned vm = (lane >> 3) * 2048 + (((lane & 7) ^ (g4 << 1)) << 4), mm = R2 + w * 2048; smo = (u64)(wd + (size_t)(rm * 128 + w * 16) * 2048 + xcd * 256);
      const unsigned ga = M_GU + (w >> 2) * 2048 + ((w & 3) * 16 + l15) * 4, go = l15 * 64; const char* gs = ws + WS_GU + ((mi >> 2) * 64 + (w & 3) * 16) * 64 + (w >> 2) * 32 + (mi & 3) * 8;
      BAR_VM(0); SEAM(4);
#define R2S(d, a, b) "ds_read2st64_b32 v[" #d ":" #d "+1], v15 offset0:" #a " offset1:" #b "\n"
      asm volatile(BODY_TXT(T0(WOA(0) R2S(224, 0, 1) R2S(226, 2, 3), WOA(1024) R2S(228, 4, 5) R2S(230, 6, 7), NO),
                            T1(WOB(0), WOB(1024) "v_add_f32 v224, v224, v225\n v_add_f32 v226, v226, v227\n v_add_f32 v228, v228, v229\n v_add_f32 v230, v230, v231\n v_add_f32 v224, v224, v226\n v_add_f32 v228, v228, v230\n"
                               "v_mov_b32 v225, %[tg]\n v_add_f32 v224, v224, v228\n", "s_mov_b32 exec_lo, %[ye]\n s_mov_b32 exec_hi, 0\n global_store_dwordx2 v16, v[224:225], %[gs] sc1\n s_mov_b64 exec, -1\n"),
                            T2(WOB(2048), WOB(3072), NO), T3(KV0, KVN(1024), NO), T4(KVN(2048), KVN(3072), NO), T5(MOE(0, 0), MOE(16384, 128), NO),
                            T6(MOE(1024, 16256), MOE(17408, 128) VM(8) TRA(12, 0) TRA(13, 1024), NO), T7(TRB(14, 0) TRB(15, 1024), TRB(16, 2048) TRB(17, 3072), NO))
                   : BODY_OUT, "=&{v[176:183]}"(wf[6]), "=&{v[184:191]}"(wf[7]), "=&{v[192:199]}"(wf[8]) : BODY_IN, "{v15}"(ga), "{v16}"(go), [ye] "s"(ye), [tg] "s"(tag32), [gs] "s"(gs)
                   : "memory", "scc", C8(224,225,226,227,228,229,230,231)); }
    u64 bad;
    { RELANE(); const unsigned vm = 0, mm = M_GR + w * 1024, ga = M_GR + w * 1024 + lane * 16, hw = M_H + w * 32 + (lane >> 2) * 2; smo = (u64)(ws + WS_GU + (size_t)(rm * 128 + w * 16) * 64);
      BAR_VM(4); SEAM(5);
      asm volatile(BODY_TXT(T0(WOA(0), WOA(1024), NO), T1(WOB(0), WOB(1024), NO), T2(WOB(2048), WOB(3072), GRD), T3(KV0, KVN(1024), NO), T4(KVN(2048), KVN(3072), NO), T5(NO, NO, NO),
                            T6(VM(4) "ds_read_b128 v[224:227], v15\n" TRA(18, 0) TRA(19, 1024), TRB(20, 0) TRB(21, 1024), NO),
                            T7("v_add_f32 v224, v224, v226\n v_cmp_ne_u32 vcc, %[tg], v225\n v_cmp_ne_u32 s[74:75], %[tg], v227\n v_add_f32_dpp v224, v224, v224 quad_perm:[1,0,3,2] row_mask:0xf bank_mask:0xf\n"
                               "v_cvt_pk_bf16_f32 v224, v224, v224\n v_lshlrev_b32 v224, 16, v224\n v_mul_f32 v226, 0xbfb8aa3b, v224\n v_exp_f32 v226, v226\n"
                               "v_mov_b32_dpp v225, v224 quad_perm:[2,3,0,1] row_mask:0xf bank_mask:0xf\n s_or_b64 s[74:75], vcc, s[74:75]\n" TRB(22, 2048),
                               "v_add_f32 v226, 1.0, v226\n v_rcp_f32 v226, v226\n" TRB(23, 3072),
                               "v_mul_f32 v226, v224, v226\n v_cvt_pk_bf16_f32 v226, v226, v226\n v_lshlrev_b32 v226, 16, v226\n v_mul_f32 v226, v226, v225\n v_cvt_pk_bf16_f32 v226, v226, v226\n"
                               "s_mov_b32 exec_lo, 0x11111111\n s_mov_b32 exec_hi, 0x11111111\n ds_write_b16 v16, v226\n s_mov_b64 exec, -1\n"))
                   : BODY_OUT, "=&{v[200:207]}"(wf[9]), "=&{v[208:215]}"(wf[10]), "=&{v[216:223]}"(wf[11]), "=&{s[74:75]}"(bad) : BODY_IN, "{v15}"(ga), "{v16}"(hw), [tg] "s"(tag32)
                   : "memory", "scc", "vcc", "v224", "v225", "v226", "v227"); }
    { RELANE();
      if (__builtin_expect(moe && bad != 0, 0)) {
          u32x4 gq;
#pragma unroll 1
          for (unsigned spins = 0; spins < SPIN_CAP; ++spins) {
              const char* p = ws + WS_GU + (size_t)(r * 128 + w * 16) * 64 + lane * 16; const u64 v0 = ld8_sc1(p), v1 = ld8_sc1(p + 8); gq = (u32x4){(unsigned)v0, (unsigned)(v0 >> 32), (unsigned)v1, (unsigned)(v1 >> 32)};
              if (__all(gq[1] == tag32 && gq[3] == tag32)) break;
              __builtin_amdgcn_s_sleep(2); }
          float sm = __uint_as_float(gq[0]) + __uint_as_float(gq[2]);
          sm = rb(sm + __uint_as_float(__builtin_amdgcn_update_dpp(0, (int)__float_as_uint(sm), 0xB1, 0xf, 0xf, false)));
          const float uu = __uint_as_float(__builtin_amdgcn_update_dpp(0, (int)__float_as_uint(sm), 0x4E, 0xf, 0xf, false));
          if (!(lane & 3)) LD(unsigned short, M_H + w * 32 + (lane >> 2) * 2) = (unsigned short)pk2(rb(silu(sm)) * uu, 0.f);
      }
      const unsigned vm = R2 + (w >> 2) * 16384 + MROW() * 128 + ((MSWZ() ^ (w & 3)) * 32) + (lane & 3) * 8, mm = tag32, ha = M_H + (g4 >> 1) * 32 + (g4 & 1) * 8, yo = lane * 8;
      smo = (u64)(ws + WS_YG + (rm * 1024 + xcd * 128 + w * 16) * 8);
      BAR_VM(0); SEAM(6);
#define DM(h, d, c) M16("v[248:251], v[" #h ":" #h "+3], v[" #d ":" #d "+3], " c)
      asm volatile(BODY_TXT(T0(WOA(0) B64(224, "v15", 0) B64(226, "v15", 16) TR2(228, "v19", 0, 1024), WOA(1024) B64(232, "v15", 64) B64(234, "v15", 80) TR2(236, "v19", 4096, 1024), NO),
                            T1(WOB(0), WOB(1024), DM(224, 228, "0") DM(232, 236, "v[248:251]")),
                            T2(WOB(2048) B64(224, "v15", 128) B64(226, "v15", 144) TR2(228, "v19", 8192, 1024), WOB(3072) B64(232, "v15", 192) B64(234, "v15", 208) TR2(236, "v19", 12288, 1024), NO),
                            T3(KV0, KVN(1024), DM(224, 228, "v[248:251]") DM(232, 236, "v[248:251]")), T4(KVN(2048), KVN(3072), NO),
                            T5("v_mov_b32 v249, %[mm]\n", "s_mov_b32 exec_lo, %[ye]\n s_mov_b32 exec_hi, 0\n global_store_dwordx2 v16, v[248:249], s[72:73] sc1\n s_mov_b64 exec, -1\n", NO),
                            T6(NO, VM(5) TRA(24, 0) TRA(25, 1024), NO), T7(TRB(26, 0) TRB(27, 1024), TRB(28, 2048) TRB(29, 3072), NO))
                   : BODY_OUT, "=&{v[224:231]}"(wf[12]), "=&{v[232:239]}"(wf[13]), "=&{v[240:247]}"(wf[14]) : BODY_IN, "{v15}"(ha), "{v16}"(yo), [ye] "s"(ye)
                   : "memory", "scc", "v248", "v249", "v250", "v251"); }
    { BAR_VM(1); SEAM(7);
      const unsigned vm = vw ^ ((vw >> 9) & 48), mm = mb; smo = (u64)(x + (size_t)(tb * 64 + kh * 32) * 2048 + hg * 256 + cbk * 64);
      asm volatile(BODY_TXT(T0(WOA(0), NO, NO), T1(WOA(1024), NO, NO), T2(MOX(0, 0), NO, NO), T3(MOX(1024, 32768), NO, NO), T4(NO, NO, NO), T5(NO, NO, NO), T6(NO, NO, NO), T7(VM(0) TRA(30, 0) TRA(31, 1024), NO, NO))
                   : BODY_OUT, "=&{v[248:255]}"(wf[15]) : BODY_IN : "memory", "scc"); }
    {
    RELANE(); const unsigned vl = lane * 16;
    u32x4 o0, o1;
    const bool routed = tb == (idx >> 6) && kh == ((idx >> 5) & 1);
    u32x2 xr[4]; for (int q = 0; q < 4; ++q) xr[q] = LD(u32x2, mb + l31 * 64 + ((q ^ ((l31 >> 2) & 3)) * 16) + h2 * 8);
    const f32x2 stt = LD(f32x2, M_ST64 + (tbk * 32 + l31) * 8);
    {
        const u32x8 P = __builtin_shufflevector(PK, PK, 0, 1, 2, 3, 4, 5, 6, 7); f32x4 L = __builtin_bit_cast(f32x4, lo4(LS));
        asm volatile("s_nop 1\n\tv_smfmac_f32_16x16x64_bf16 %0, %1, %2, %3\n\ts_nop 11" : "+v"(L) : "v"(hi4(LS)), "v"(P), "v"(IDX));
        O = mfma32(hi4(V), hi4(P), O);
        const auto l16 = __builtin_amdgcn_permlane16_swap(__float_as_uint(L[0] + L[1]), __float_as_uint(L[2] + L[3]), false, false);
        float lsum = __uint_as_float(__builtin_amdgcn_permlane32_swap(l16[0], l16[0], false, false)[0]);
        const float om = fmaxf(fmaxf(fmaxf(fmaxf(fabsf(O[0]), fabsf(O[1])), fabsf(O[2])), fmaxf(fmaxf(fabsf(O[3]), fabsf(O[4])), fabsf(O[5]))), fmaxf(fmaxf(fmaxf(fabsf(O[6]), fabsf(O[7])), fabsf(O[8])),
                         fmaxf(fmaxf(fmaxf(fabsf(O[9]), fabsf(O[10])), fabsf(O[11])), fmaxf(fmaxf(fmaxf(fabsf(O[12]), fabsf(O[13])), fabsf(O[14])), fabsf(O[15])))));
        if (__builtin_expect(__any(!(lsum <= 1.8446744e19f) || !(om <= 3.0e38f)), 0)) {
            float mr = -INFINITY; lsum = 0.f; for (int i = 0; i < 16; ++i) O[i] = 0.f;
#pragma unroll 1
            for (int t = 0; t < 64; ++t) {
                u32x4 k0, k1, v0, v1;
                asm volatile("global_load_dwordx4 %0, %4, %5 sc1\n\tglobal_load_dwordx4 %1, %4, %5 offset:1024 sc1\n\tglobal_load_dwordx4 %2, %4, %6 offset:2048 sc1\n\tglobal_load_dwordx4 %3, %4, %7 offset:3072 sc1\n\ts_waitcnt vmcnt(0)"
                             : "=&v"(k0), "=&v"(k1), "=&v"(v0), "=&v"(v1) : "v"(vl), "s"(kv + ((t + 62) & 63) * 4096), "s"(kv + ((t + 63) & 63) * 4096), "s"(kv + t * 4096) : "memory");
                f32x16 z; for (int i = 0; i < 16; ++i) z[i] = 0.f;
                f32x16 s = mfma32(k0, lo4(Q), z); s = mfma32(k1, hi4(Q), s);
                float m = s[0]; for (int i = 1; i < 16; ++i) m = fmaxf(m, s[i]);
                m = fmaxf(mr, xmax32(m)); const float al = __builtin_amdgcn_exp2f(mr - m); mr = m;
                u32x8 pp; float ps = 0.f;
                for (int i = 0; i < 8; ++i) { pp[i] = pk2(__builtin_amdgcn_exp2f(s[2 * i] - m), __builtin_amdgcn_exp2f(s[2 * i + 1] - m)); ps += bf_lo(pp[i]) + bf_hi(pp[i]); }
                lsum = lsum * al + xsum32(ps); for (int i = 0; i < 16; ++i) O[i] *= al;
                O = mfma32(v0, lo4(pp), O); O = mfma32(v1, hi4(pp), O);
            }
        }
        const float rl = __builtin_amdgcn_rcpf(lsum);
        for (int i = 0; i < 4; ++i) { o0[i] = pk2(O[2 * i] * rl, O[2 * i + 1] * rl); o1[i] = pk2(O[8 + 2 * i] * rl, O[8 + 2 * i + 1] * rl); }
        asm volatile("global_store_dwordx4 %0, %1, %3 sc1\n\tglobal_store_dwordx4 %0, %2, %3 offset:2048 sc1\n\ts_nop 1"
                     :: "v"(vl), "v"(o0), "v"(o1), "s"(ws + WS_O + (size_t)(tb * 8 + hg) * 16384 + hd * 4096 + tbk * 1024) : "memory");
    }
    u64 yg[14];
    if (routed) for (int i = 0; i < 14; ++i) yg[i] = ld8_sc1(ws + WS_YG + ((h2 * 14 + i) * 1024 + hg * 128 + cbk * 32 + l31) * 8);
    BAR_LDS();
    const unsigned pv = w * PRIV;
    LD(u32x4, hg * PRIV + hd * 4096 + tbk * 1024 + vl) = o0; LD(u32x4, hg * PRIV + hd * 4096 + 2048 + tbk * 1024 + vl) = o1;
    unsigned xn2[8], g2[8];
    for (int i = 0; i < 8; ++i) { xn2[i] = ln2(xr[i >> 1][i & 1], stt[0], stt[1]); PIN(xn2[i]); }
    WAIT_VM(0);
    for (int i = 0; i < 14; ++i) asm volatile("" :: "v"(yg[i]));
    if (lane == 0) st8_sc1(ws + WS_FLAG4 + b * 128 + w * 8, seed);
    if (routed) {
#pragma unroll 1
        for (unsigned spins = 0; spins < SPIN_CAP; ++spins) {
            bool ok = true; for (int i = 0; i < 14; ++i) ok &= (unsigned)(yg[i] >> 32) == tag32;
            if (__all(ok)) break;
            __builtin_amdgcn_s_sleep(2);
            for (int i = 0; i < 14; ++i) yg[i] = ld8_sc1(ws + WS_YG + ((h2 * 14 + i) * 1024 + hg * 128 + cbk * 32 + l31) * 8);
        }
        float acc = 0.f; for (int i = 0; i < 14; ++i) acc += __uint_as_float((unsigned)yg[i]);
        const unsigned o16 = (gkey >> 11) & 0xffffu, rbits = (o16 & 0x8000u) ? (o16 & 0x7fffu) : (~o16 & 0xffffu);
        const float rsel = rb(__builtin_amdgcn_rcpf(1.f + __builtin_amdgcn_exp2f(-1.44269504089f * bf_lo(rbits))));
        LD(float, pv + 16384 + lane * 4) = rb(rb(xsum32(acc)) * rsel);
        for (int q = 0; q < 4; ++q) { const f32x4 y = LD(f32x4, pv + 16384 + q * 32 + h2 * 16);
            for (int i = 0; i < 2; ++i) { const unsigned t = pk2(bf_lo(xn2[2 * q + i]) + y[2 * i], bf_hi(xn2[2 * q + i]) + y[2 * i + 1]); xn2[2 * q + i] = l31 == (idx & 31) ? t : xn2[2 * q + i]; } }
    }
    for (int i = 0; i < 8; ++i) { g2[i] = pk2(silu(bf_lo(xn2[i])), silu(bf_hi(xn2[i]))); PIN(g2[i]); }
    if (w != hg) {
        const int pb = (((w & 3) * 8 + (tb & 7)) << 3) | ((tb >> 3) * 2 + (w >> 2));
#pragma unroll 1
        for (unsigned spins = 0; spins < SPIN_CAP; ++spins) { if (__all(ld8_sc1(ws + WS_FLAG4 + pb * 128 + (lane & 7) * 8) == seed)) break; __builtin_amdgcn_s_sleep(1); }
        const char* src = ws + WS_O + (size_t)(tb * 8 + w) * 16384;
        for (int i = 0; i < 4; ++i) dma4_sc1(src + i * 4096, vl, pv + i * 4096);
    }
    BAR_VM(12);
    f32x16 ao, at; for (int i = 0; i < 16; ++i) { ao[i] = 0.f; at[i] = 0.f; }
    {
        const unsigned oa = kh * 4 * PRIV + vl + kh * 1024, ob = oa ^ 1024;
        u32x4 fr[2][8]; f32x4 pt[4];
#define OAD(g, i) ((g) < 4 ? (((i) & 1) ? oa : ob) + (((g) & 1) * 2 + ((i) >> 2)) * PRIV + (((g) >> 1) * 2 + (((i) >> 1) & 1)) * 2048 : ((g) < 6 ? ob : oa) + ((i) >> 1) * PRIV + (4 + ((g) & 1) * 2 + ((i) & 1)) * 2048)
#define OWJ(g, i) ((g) < 4 ? (((g) & 1) * 2 + ((i) >> 2)) * 8 + ((g) >> 1) * 2 + (((i) >> 1) & 1) : ((i) >> 1) * 8 + 4 + ((g) & 1) * 2 + ((i) & 1))
#define ORD(p, g) do { for (int i = 0; i < 8; ++i) fr[p][i] = LD(u32x4, OAD(g, i)); } while (0)
#define WFJ(j) (((j) & 1) ? hi4(wf[(j) >> 1]) : lo4(wf[(j) >> 1]))
        __builtin_amdgcn_s_setprio(1);
        ORD(0, 0); SBAR();
#pragma unroll
        for (int g = 0; g < 8; ++g) {
            if (g == 1) asm volatile("s_waitcnt vmcnt(8)\n\ts_barrier" ::: "memory");
            if (g == 3) asm volatile("s_waitcnt vmcnt(4)\n\ts_barrier" ::: "memory");
            if (g == 4) asm volatile("s_waitcnt vmcnt(0)\n\ts_barrier" ::: "memory");
            if (g == 6) { __builtin_amdgcn_s_setprio(0); ORD(1, 7); SBAR(); for (int q = 0; q < 4; ++q) LD(f32x4, pv + 16384 + q * 1024 + vl) = (f32x4){at[4 * q], at[4 * q + 1], at[4 * q + 2], at[4 * q + 3]}; SBAR(); }
            if (g < 6) ORD((g + 1) & 1, g + 1);
            for (int i = 0; i < 8; ++i) {
                if (g < 4 ? (i & 1) : g >= 6) ao = mfma32(WFJ(OWJ(g, i)), fr[g & 1][i], ao); else at = mfma32(WFJ(OWJ(g, i)), fr[g & 1][i], at);
                if (g == 7 && i == 5) { SBAR(); __builtin_amdgcn_s_setprio(1); BAR_LDS(); for (int q = 0; q < 4; ++q) pt[q] = LD(f32x4, (w ^ 4) * PRIV + 16384 + q * 1024 + vl); SBAR(); } }
            if (g < 6) for (int i = 0; i < 8; ++i) { SGB(0x008, 1); SGB(0x100, 1); }
            SBAR();
        }
        __builtin_amdgcn_s_setprio(0);
        char* op = out + (size_t)(tb * 64 + kh * 32 + l31) * 2048 + hg * 256 + cbk * 64 + h2 * 16;
        u32x2 o[4];
        for (int q = 0; q < 4; ++q) for (int i = 0; i < 2; ++i) {
            const unsigned s2 = pk2(ao[4 * q + 2 * i] + pt[q][2 * i], ao[4 * q + 2 * i + 1] + pt[q][2 * i + 1]), gg = g2[2 * q + i];
            const unsigned p2 = pk2(bf_lo(s2) * bf_lo(gg), bf_hi(s2) * bf_hi(gg));
            o[q][i] = pk2(bf_lo(xr[q][i]) + bf_lo(p2), bf_hi(xr[q][i]) + bf_hi(p2));
        }
        for (int q = 0; q < 4; q += 2) {
            const auto s0 = __builtin_amdgcn_permlane32_swap(o[q][0], o[q + 1][0], false, false), s1 = __builtin_amdgcn_permlane32_swap(o[q][1], o[q + 1][1], false, false);
            *(u32x4*)(op + q * 16) = (u32x4){s0[0], s1[0], s0[1], s1[1]};
        }
    }
    }
}

extern "C" void kernel_launch(void* const* d_in, const int* in_sizes, int n_in, void* d_out, int out_size, void* d_ws, size_t ws_size, hipStream_t stream) {
    (void)hipFuncSetAttribute((const void*)fused_block, hipFuncAttributeMaxDynamicSharedMemorySize, LDS_BYTES);
    fused_block<<<256, 512, LDS_BYTES, stream>>>((const char*)d_in[0], (const char*)d_in[1], (const char*)d_in[2], (const char*)d_in[3], (const char*)d_in[4],
                                                 (const char*)d_in[6], (const char*)d_in[7], (const char*)d_in[8], (const char*)d_in[9], (char*)d_out, (char*)d_ws);
}
```

```cpp
#include <hip/hip_runtime.h>
#include <hip/hip_bf16.h>
#include <stdint.h>
#include <type_traits>

typedef unsigned long long u64;
typedef float f32x16 __attribute__((ext_vector_type(16)));
typedef float f32x4 __attribute__((ext_vector_type(4)));
typedef float f32x2 __attribute__((ext_vector_type(2)));
typedef unsigned u32x2 __attribute__((ext_vector_type(2)));
typedef unsigned u32x4 __attribute__((ext_vector_type(4)));
typedef unsigned u32x8 __attribute__((ext_vector_type(8)));
typedef unsigned u32x16 __attribute__((ext_vector_type(16)));
typedef __bf16 bf16x8 __attribute__((ext_vector_type(8)));
typedef __bf16 bf16x2 __attribute__((ext_vector_type(2)));
typedef short s16x4 __attribute__((ext_vector_type(4)));
#define LAS __attribute__((address_space(3)))
#define GAS __attribute__((address_space(1)))
#define DI __device__ __forceinline__
#define LD(T, a) (*(LAS T*)(unsigned)(a))

constexpr unsigned WS_EPOCH = 0, WS_FLAG1 = 4096, WS_STATS = 8192, WS_FLAG4 = 65536, WS_KV = 131072, WS_YG = 393216, WS_GU = 622592, WS_CS = 851968, WS_O = 1u << 20;
constexpr unsigned SLOT = 24576, L_X16 = 49152, L_WR = 81920, L_SP = 86016, L_PA = 73728, L_WKV = 98304, L_QX = 0, L_PRO = 16384, L_CSP = 98304, L_MISC = 155648, LDS_BYTES = 163840;
constexpr unsigned M_CTL = L_MISC, M_ST64 = L_MISC + 256, M_STX = L_MISC + 1280, M_CS = L_MISC + 6144, M_XSEL = L_MISC + 2560, M_H = L_MISC + 3584, M_GU = L_MISC + 4096;
constexpr unsigned KA = 122880, KB = 32768, R2 = 65536, R3 = 0, R4 = 98304, M_GR = 114688, PRIV = 20480;
constexpr int slot_of(int s) { return s % 6; }
constexpr unsigned SPIN_CAP = 1u << 18;
constexpr int CODE_SPAN = 40448;

DI float bf_lo(unsigned v) { return __uint_as_float(v << 16); }
DI float bf_hi(unsigned v) { return __uint_as_float(v & 0xffff0000u); }
DI unsigned pk2(float lo, float hi) { f32x2 v = {lo, hi}; return __builtin_bit_cast(unsigned, __builtin_convertvector(v, bf16x2)); }
DI float rb(float f) { return __uint_as_float((unsigned)__builtin_bit_cast(unsigned short, (__bf16)f) << 16); }
DI float xsum32(float v) { auto r = __builtin_amdgcn_permlane32_swap(__float_as_uint(v), __float_as_uint(v), false, false); return __uint_as_float(r[0]) + __uint_as_float(r[1]); }
DI float xmax32(float v) { auto r = __builtin_amdgcn_permlane32_swap(__float_as_uint(v), __float_as_uint(v), false, false); return fmaxf(__uint_as_float(r[0]), __uint_as_float(r[1])); }
DI f32x16 mfma32(u32x4 a, u32x4 b, f32x16 c) { return __builtin_amdgcn_mfma_f32_32x32x16_bf16(__builtin_bit_cast(bf16x8, a), __builtin_bit_cast(bf16x8, b), c, 0, 0, 0); }
DI f32x4 mfma16(u32x4 a, u32x4 b, f32x4 c) { return __builtin_amdgcn_mfma_f32_16x16x32_bf16(__builtin_bit_cast(bf16x8, a), __builtin_bit_cast(bf16x8, b), c, 0, 0, 0); }
DI u32x2 trd(unsigned a) { return __builtin_bit_cast(u32x2, __builtin_amdgcn_ds_read_tr16_b64_v4i16((LAS s16x4*)a)); }
DI u32x4 trfrag(unsigned a, unsigned step) { u32x2 lo = trd(a), hi = trd(a + step); return (u32x4){lo[0], lo[1], hi[0], hi[1]}; }
DI u32x4 lo4(u32x8 v) { return __builtin_shufflevector(v, v, 0, 1, 2, 3); }
DI u32x4 hi4(u32x8 v) { return __builtin_shufflevector(v, v, 4, 5, 6, 7); }
DI float max3(float a, float b, float c) { float r; asm("v_max3_f32 %0, %1, %2, %3" : "=v"(r) : "v"(a), "v"(b), "v"(c)); return r; }
DI float silu(float v) { return v * __builtin_amdgcn_rcpf(1.f + __builtin_amdgcn_exp2f(-1.44269504089f * v)); }
DI unsigned ln2(unsigned v, float m, float rs) { const unsigned d = pk2(bf_lo(v) - m, bf_hi(v) - m); return pk2(bf_lo(d) * rs, bf_hi(d) * rs); }
DI u64 ld8_sc1(const void* p) { return __hip_atomic_load((GAS const u64*)p, __ATOMIC_RELAXED, __HIP_MEMORY_SCOPE_AGENT); }
DI void st8_sc1(void* p, u64 v) { __hip_atomic_store((GAS u64*)p, v, __ATOMIC_RELAXED, __HIP_MEMORY_SCOPE_AGENT); }
DI int fresh_lane() { int l; asm volatile("v_mbcnt_lo_u32_b32 %0, -1, 0\n\tv_mbcnt_hi_u32_b32 %0, -1, %0" : "=v"(l)); return l; }
#define RELANE() const int lane = fresh_lane(), l15 = lane & 15, g4 = lane >> 4, l31 = lane & 31, h2 = lane >> 5; (void)l15; (void)g4; (void)l31; (void)h2
DI u64 mix(unsigned lo, unsigned hi) { u64 z = (((u64)hi << 32) | lo) + 0x9E3779B97F4A7C15ull; z = (z ^ (z >> 30)) * 0xBF58476D1CE4E5B9ull; z = (z ^ (z >> 27)) * 0x94D049BB133111EBull; return z ^ (z >> 31); }
DI unsigned rowmax16(unsigned k) { k = max(k, (unsigned)__builtin_amdgcn_update_dpp(0, (int)k, 0x128, 0xf, 0xf, false)); k = max(k, (unsigned)__builtin_amdgcn_update_dpp(0, (int)k, 0x124, 0xf, 0xf, false));
    k = max(k, (unsigned)__builtin_amdgcn_update_dpp(0, (int)k, 0x122, 0xf, 0xf, false)); return max(k, (unsigned)__builtin_amdgcn_update_dpp(0, (int)k, 0x121, 0xf, 0xf, false)); }
DI void dma1(const void* sbase, unsigned voff, unsigned m0v) { asm volatile("s_mov_b32 m0, %2\n\ts_nop 0\n\tglobal_load_lds_dwordx4 %0, %1" :: "v"(voff), "s"(sbase), "s"(m0v) : "memory"); }
DI void dma1_sc1(const void* sbase, unsigned voff, unsigned m0v) { asm volatile("s_mov_b32 m0, %2\n\ts_nop 0\n\tglobal_load_lds_dwordx4 %0, %1 sc1" :: "v"(voff), "s"(sbase), "s"(m0v) : "memory"); }
DI void dma4(const void* sbase, unsigned voff, unsigned m0v) {
    asm volatile("s_mov_b32 m0, %2\n\ts_nop 0\n\tglobal_load_lds_dwordx4 %0, %1\n\tglobal_load_lds_dwordx4 %0, %1 offset:1024\n\tglobal_load_lds_dwordx4 %0, %1 offset:2048\n\tglobal_load_lds_dwordx4 %0, %1 offset:3072"
                 :: "v"(voff), "s"(sbase), "s"(m0v) : "memory"); }
DI void dma4_sc1(const void* sbase, unsigned voff, unsigned m0v) {
    asm volatile("s_mov_b32 m0, %2\n\ts_nop 0\n\tglobal_load_lds_dwordx4 %0, %1 sc1\n\tglobal_load_lds_dwordx4 %0, %1 offset:1024 sc1\n\tglobal_load_lds_dwordx4 %0, %1 offset:2048 sc1\n\tglobal_load_lds_dwordx4 %0, %1 offset:3072 sc1"
                 :: "v"(voff), "s"(sbase), "s"(m0v) : "memory"); }
#define BAR_LDS() asm volatile("s_waitcnt lgkmcnt(0)\n\ts_barrier" ::: "memory")
#define BAR_VM(n) asm volatile("s_waitcnt vmcnt(" #n ") lgkmcnt(0)\n\ts_barrier" ::: "memory")
#define WAIT_VM(n) asm volatile("s_waitcnt vmcnt(" #n ")" ::: "memory")
#define PIN(v) asm volatile("" : "+v"(v))
#define SBAR() __builtin_amdgcn_sched_barrier(0)
#define SGB(m, n) __builtin_amdgcn_sched_group_barrier(m, n, 0)

#define EX(A,i) "v_exp_f32 v[" #A "+" #i "], v[" #A "+" #i "]\n"
#define E4(A,a,b,c,d) EX(A,a) EX(A,b) EX(A,c) EX(A,d)
#define CV(p,A,i) "v_cvt_pk_bf16_f32 v[104+" #p "], v[" #A "+" #i "], v[" #A "+" #i "+1]\n"
#define RDX(dst, o, k) "ds_read_b128 " dst ", v22 offset:" o "+" #k "\n"
#define M32(x) "v_mfma_f32_32x32x16_bf16 " x "\n"
#define M16(x) "v_mfma_f32_16x16x32_bf16 " x "\n"
#define TILE(A, B, o, X1, X2, Y, w1, w2) \
    M32("v[" #B ":" #B "+15], v[112:115], v[96:99], v[64:79]") RDX("v[112:115]", o, 0) X1 E4(A,0,1,2,3) \
    M32("v[80:95], v[124:127], v[108:111], v[80:95]") RDX("v[124:127]", o, 3072) E4(A,4,5,6,7) \
    "v_smfmac_f32_16x16x64_bf16 v[24:27], v[28:31], v[104:111], v23\n" "s_waitcnt lgkmcnt(" #w1 ")\n" CV(0,A,0) CV(1,A,2) CV(2,A,4) CV(3,A,6) \
    M32("v[" #B ":" #B "+15], v[116:119], v[100:103], v[" #B ":" #B "+15]") RDX("v[116:119]", o, 1024) X2 E4(A,8,9,10,11) \
    M32("v[80:95], v[120:123], v[104:107], v[80:95]") RDX("v[120:123]", o, 2048) E4(A,12,13,14,15) Y \
    "s_waitcnt lgkmcnt(" #w2 ")\n" CV(4,A,8) CV(5,A,10) CV(6,A,12) CV(7,A,14)
#define NO ""
#define WOA(k) "s_add_u32 m0, %[ma], " #k "\n s_add_u32 s70, s70, 0x8000\n s_addc_u32 s71, s71, 0\n global_load_lds_dwordx4 v20, s[70:71]\n"
#define WOB(k) "s_add_u32 m0, %[mb], " #k "\n s_add_u32 s70, s70, 0x8000\n s_addc_u32 s71, s71, 0\n global_load_lds_dwordx4 v20, s[70:71]\n"
#define MOE(k, st) "s_add_u32 m0, %[mm], " #k "\n s_add_u32 s72, s72, " #st "\n s_addc_u32 s73, s73, 0\n global_load_lds_dwordx4 v19, s[72:73] nt\n"
#define MOX(k, st) "s_add_u32 m0, %[mm], " #k "\n s_add_u32 s72, s72, " #st "\n s_addc_u32 s73, s73, 0\n global_load_lds_dwordx4 v19, s[72:73]\n"
#define KV0 "s_mov_b32 m0, %[mk]\n s_nop 0\n global_load_lds_dwordx4 v21, %[sk] sc1\n"
#define KVN(k) "global_load_lds_dwordx4 v21, %[sk] offset:" #k " sc1\n"
#define GRD "s_mov_b32 m0, %[mm]\n s_nop 0\n global_load_lds_dwordx4 v21, s[72:73] sc1\n"
#define TR2(d, a, k, st) "ds_read_b64_tr_b16 v[" #d ":" #d "+1], " a " offset:" #k "\n ds_read_b64_tr_b16 v[" #d "+2:" #d "+3], " a " offset:" #k "+" #st "\n"
#define TRA(j, k) TR2(128+4*j, "v18", k-(j&1)*992, 512)
#define TRB(j, k) TR2(128+4*j, "v17", k-(j&1)*992, 512)
#define B64(d, a, k) "ds_read_b64 v[" #d ":" #d "+1], " a " offset:" #k "\n"
#define VM(n) "s_waitcnt vmcnt(" #n ")\n"
#define BODY_TXT(T0, T1, T2, T3, T4, T5, T6, T7) "s_barrier\n s_setprio 3\n" T0 T1 T2 T3 T4 "s_setprio 2\n" T5 T6 "s_setprio 1\n" T7
#define PH "s_setprio 0\n"
#define HAND(n) "s_waitcnt vmcnt(" #n ") lgkmcnt(0)\n"
#define T0(a,b,c) TILE(32,48,"0",a,b,c,2,2)
#define U0(a,b,c,w1,w2) TILE(32,48,"0",a,b,c,w1,w2)
#define T1(a,b,c) TILE(48,32,"4096",a,b,c,2,2)
#define U1(a,b,c,w1,w2) TILE(48,32,"4096",a,b,c,w1,w2)
#define T2(a,b,c) TILE(32,48,"8192",a,b,c,2,2)
#define U2(a,b,c,w1,w2) TILE(32,48,"8192",a,b,c,w1,w2)
#define T3(a,b,c) TILE(48,32,"12288",a,b,c,2,2)
#define U3(a,b,c,w1,w2) TILE(48,32,"12288",a,b,c,w1,w2)
#define T4(a,b,c) TILE(32,48,"16384",a,b,c,2,2)
#define U4(a,b,c,w1,w2) TILE(32,48,"16384",a,b,c,w1,w2)
#define T5(a,b,c) TILE(48,32,"20480",a,b,c,2,2)
#define U5(a,b,c,w1,w2) TILE(48,32,"20480",a,b,c,w1,w2)
#define T6(a,b,c) TILE(32,48,"24576",a,b,c,2,2)
#define U6(a,b,c,w1,w2) TILE(32,48,"24576",a,b,c,w1,w2)
#define T7(a,b,c) TILE(48,32,"28672",a,PH b,c,2,2)
#define U7(a,b,c,w1,w2) TILE(48,32,"28672",a,PH b,c,w1,w2)
#define BODY_OUT "+{v[32:47]}"(SA), "=&{v[48:63]}"(SB), "+{v[80:95]}"(O), "+{v[24:31]}"(LS), "+{v[104:119]}"(PK), "+{v[120:127]}"(V), "+{s[70:71]}"(swo), "+{s[72:73]}"(smo)
#define BODY_IN "{v[64:79]}"(NEG), "{v[96:103]}"(Q), "{v23}"(IDX), "{v22}"(ad), "{v21}"(vl), "{v20}"(vw), "{v19}"(vm), "{v18}"(ta), "{v17}"(tb_), \
      [ma] "s"(ma), [mb] "s"(mb), [mm] "s"(mm), [mk] "s"(mk), [sk] "s"(sk)
#define C8(a,b,c,d,e,f,g,h) "v" #a, "v" #b, "v" #c, "v" #d, "v" #e, "v" #f, "v" #g, "v" #h

__global__ __launch_bounds__(512, 2) void fused_block(const char* __restrict__ x, const char* __restrict__ wq, const char* __restrict__ wk, const char* __restrict__ wv,
                                                      const char* __restrict__ wo, const char* __restrict__ wr, const char* __restrict__ wg, const char* __restrict__ wu,
                                                      const char* __restrict__ wd, char* __restrict__ out, char* __restrict__ ws) {
    const int tid = threadIdx.x, lane = tid & 63, w = __builtin_amdgcn_readfirstlane(tid >> 6);
    const int b = blockIdx.x, xcd = b & 7, r = b >> 3, tb = (xcd >> 1) * 8 + (r & 7), hg = (xcd & 1) * 4 + (r >> 3);
    const int isV = r >> 4, sub = (r >> 2) & 3, T0 = (xcd * 4 + (r & 3)) * 64 + sub * 16;
    const int l15 = lane & 15, g4 = lane >> 4, l31 = lane & 31, h2 = lane >> 5;
    const int hd = w & 3, tbk = w >> 2;
    const unsigned vl = lane * 16;
    {
        u64 pc; asm volatile("s_getpc_b64 %0" : "=s"(pc));
        asm volatile("s_mov_b32 m0, %2\n\ts_mov_b64 exec, 3\n\tglobal_load_lds_dword %0, %1\n\ts_mov_b64 exec, -1"
                     :: "v"(lane * 128), "s"((pc & ~127ull) + min((r * 8 + w) * 256, CODE_SPAN - 256)), "s"(L_SP + 1024 + w * 16) : "memory");
        SBAR();
        asm volatile("" :: "s"(x), "s"(wr), "s"(wq), "s"(wk), "s"(wv), "s"(ws));
    }
    const int c0 = r & 7, rm = min(r, 27), mi = xcd * 28 + rm; const char* kv = ws + WS_KV;
    f32x4 qa[2][2];
    float aone; unsigned sta, csa, g0m; u32x8 LS, PZ; f32x16 O; unsigned IDX, vw, vm0, ta, tb_;
    const unsigned ma = R4 + w * 2048, mb = R3 + w * 4096;
    u64 swo, smo, sk0;
#define SETUP() do { swo = (u64)(wo + (size_t)tbk * 512 * 2048 + hg * 256 + hd * 64) - 0x8000; smo = (u64)(wg + (size_t)((mi & 3) * 256 + w * 32) * 7168 + (mi >> 2) * 128); \
        sk0 = (u64)(kv + ((c0 + 1) & 7) * 32768 + w * 4096); asm volatile("" : "+s"(swo), "+s"(smo), "+s"(sk0) : "s"(out), "s"(wu), "s"(wd)); aone = h2 ? 0.f : 1.f; PIN(aone); sta = M_ST64 + (tbk * 32 + l15) * 8; csa = M_CS + hd * 512 + l15 * 8; g0m = g4 ? 0u : 0xffffu; PIN(sta); PIN(csa); PIN(g0m); \
        const unsigned one_ = 0x3F803F80u; LS = (u32x8){0u, 0u, 0u, 0u, l15 < 2 ? one_ : 0u, l15 < 2 ? one_ : 0u, (l15 & 14) == 2 ? one_ : 0u, (l15 & 14) == 2 ? one_ : 0u}; \
        IDX = (lane & 1) ? 0xEEEEEEEEu : 0x44444444u; vw = (lane >> 2) * 2048 + (lane & 3) * 16; vm0 = (lane >> 3) * 7168 + (((lane & 7) ^ (g4 << 1)) << 4); \
        ta = ma + (16 * (g4 >> 1) + 4 * (g4 & 1) + (l15 >> 2)) * 64 + (lane & 3) * 8; tb_ = ta + (mb - ma); for (int i = 0; i < 16; ++i) O[i] = 0.f; PZ = (u32x8){0u, 0u, 0u, 0u, 0u, 0u, 0u, 0u}; PIN(O); PIN(PZ); PIN(LS); PIN(IDX); PIN(vw); PIN(vm0); PIN(ta); PIN(tb_); } while (0)
    if (__builtin_expect(w < 4, 1)) {
        __builtin_amdgcn_s_setprio(3);
        u32x2 ev; u32x4 xq[8], aw[8], ones; unsigned wa, wa2, spa, kvo, wb[3][2][2], xb_[3][2];
        {
            const char* xs = x + (size_t)T0 * 2048 + w * 512;
            for (int i = 0; i < 8; ++i) dma1(xs, (2 * i + h2) * 2048 + ((l31 ^ (2 * i + h2)) << 4), L_X16 + w * 8192 + i * 1024);
            dma1(wr + w * 512, l31 * 16, L_WR + w * 1024);
            asm volatile("s_mov_b64 exec, 1\n\tglobal_load_dwordx2 %0, %1, %2 sc1\n\ts_mov_b64 exec, -1" : "=v"(ev) : "v"(0), "s"(ws));
            SETUP();
            wa = L_WKV + w * 16384 + (8 * g4 + (l15 >> 2)) * 64 + (g4 & 1) * 32 + (lane & 3) * 8; wa2 = wa ^ 32; spa = L_SP + ((l15 + (isV ? 4 * g4 : 0)) & 15) * 16;
            const int kt = T0 >> 5, sh = sub & 1;
            kvo = isV ? ((kt - 1 + sh) & 63) * 4096 + 2048 + sh * 1024 + ((g4 & 1) * 32 + w * 16 + l15) * 16 + (g4 >> 1) * 8
                      : ((kt - 2) & 63) * 4096 + w * 1024 + ((g4 >> 1) * 32 + sh * 16 + l15) * 16 + (g4 & 1) * 8;
            const unsigned wq_a = 8192 + (8 * g4 + (l15 >> 2)) * 256 + (((2 * hd) ^ (((l15 >> 2) << 1) | (g4 & 1))) << 5) + (lane & 3) * 8;
            ones = (u32x4){0x3F803F80u, 0x3F803F80u, 0x3F803F80u, 0x3F803F80u};
            for (int i = 0; i < 3; ++i) for (int ks = 0; ks < 2; ++ks) {
                const int kk = ks ^ (tb & 1);
                wb[i][ks][0] = wq_a + i * 2 * SLOT + kk * 8192; wb[i][ks][1] = wb[i][ks][0] ^ 32; xb_[i][ks] = l15 * 128 + (((4 * kk + g4) ^ (l15 >> 1)) << 4) + i * 2 * SLOT;
                PIN(wb[i][ks][0]); PIN(wb[i][ks][1]); PIN(xb_[i][ks]); }
            const unsigned oth = l15 == 1 ? 0x3F803F80u : 0u;
            for (int s = 0; s < 8; ++s) { aw[s] = (u32x4){oth, oth, oth, oth}; PIN(aw[s]); }
            PIN(kvo); PIN(wa); PIN(wa2); PIN(spa); PIN(ones);
            WAIT_VM(1);
            const unsigned xa = L_X16 + w * 8192 + l15 * 512 + ((g4 ^ l15) << 4);
            for (int s = 0; s < 8; ++s) xq[s] = LD(u32x4, xa ^ (s * 64));
            if (l15 == 0) for (int s = 0; s < 8; ++s) aw[s] = LD(u32x4, L_WR + w * 1024 + s * 64 + g4 * 16);
        }
        const f32x4 z4 = {0, 0, 0, 0};
        f32x4 s1 = z4, gr = z4, sw = z4;
        for (int s = 0; s < 8; ++s) { gr = mfma16(xq[s], xq[s], gr); s1 = mfma16(aw[s], xq[s], s1); sw = mfma16(aw[s], ones, sw); }
        { float d = (l15 & 2) ? ((l15 & 1) ? gr[3] : gr[2]) : ((l15 & 1) ? gr[1] : gr[0]); d = g4 == (l15 >> 2) ? d : 0.f;
          const auto e16 = __builtin_amdgcn_permlane16_swap(__float_as_uint(d), __float_as_uint(d), false, false); d = xsum32(__uint_as_float(e16[0]) + __uint_as_float(e16[1]));
          if (lane < 16) LD(f32x4, L_SP + w * 256 + vl) = (f32x4){s1[1], d, s1[0], sw[0]}; }
        BAR_LDS();
        f32x4 m4, r4; unsigned key = 0;
        {
            f32x4 p[4]; for (int q = 0; q < 4; ++q) p[q] = LD(f32x4, spa + q * 256);
            const f32x4 t = (p[0] + p[1]) + (p[2] + p[3]);
            const float mean = t[0] * (1.f / 1024.f), var = fmaxf(t[1] * (1.f / 1024.f) - mean * mean, 0.f);
            const float mrow = rb(mean), rsrow = __builtin_amdgcn_rcpf(rb(__builtin_amdgcn_sqrtf(rb(rb(var) + 1e-5f))));
            const int mi_ = (int)__float_as_uint(mrow), ri_ = (int)__float_as_uint(rsrow);
#define BC(v, n) __uint_as_float((unsigned)__builtin_amdgcn_update_dpp(0, v, 0x150 + (n), 0xf, 0xf, false))
            m4 = (f32x4){BC(mi_, 0), BC(mi_, 1), BC(mi_, 2), BC(mi_, 3)}; r4 = (f32x4){BC(ri_, 0), BC(ri_, 1), BC(ri_, 2), BC(ri_, 3)};
            if (!isV) { m4 = (f32x4){mrow, mrow, mrow, mrow}; r4 = (f32x4){rsrow, rsrow, rsrow, rsrow};
                if (w == 2) { const unsigned u = __float_as_uint(rb((t[2] - mrow * t[3]) * rsrow)) >> 16;
                    key = rowmax16(((((u & 0x8000u) ? ~u : (u | 0x8000u)) & 0xffffu) << 11) | (2047 - (T0 + l15))); } }
        }
        PIN(m4); PIN(r4);
        BAR_LDS();
        auto kvpart = [&](auto V_) {
            constexpr bool V = decltype(V_)::value;
            u32x4 w0[8], w1[8]; f32x4 a0 = z4, a1 = z4, c0_ = z4, c1_ = z4;
            for (int s = 0; s < 3; ++s) { w0[s] = trfrag(wa + s * 2048, 256); w1[s] = trfrag(wa2 + s * 2048, 256); }
            SBAR();
#pragma unroll
            for (int s = 0; s < 8; ++s) {
                if (s < 5) { w0[s + 3] = trfrag(wa + (s + 3) * 2048, 256); w1[s + 3] = trfrag(wa2 + (s + 3) * 2048, 256); }
                if (V) { a0 = mfma16(xq[s], w0[s], a0); c0_ = mfma16(ones, w0[s], c0_); a1 = mfma16(xq[s], w1[s], a1); c1_ = mfma16(ones, w1[s], c1_); }
                else { a0 = mfma16(w0[s], xq[s], a0); c0_ = mfma16(w0[s], ones, c0_); a1 = mfma16(w1[s], xq[s], a1); c1_ = mfma16(w1[s], ones, c1_); }
                if (s < 5) for (int i = 0; i < 4; ++i) { SGB(0x008, 1); SGB(0x100, 1); }
                SBAR();
            }
            LD(f32x4, L_PA + w * 2048 + vl) = (a0 - m4 * c0_) * r4; LD(f32x4, L_PA + w * 2048 + 1024 + vl) = (a1 - m4 * c1_) * r4;
        };
        if (isV) kvpart(std::true_type{}); else kvpart(std::false_type{});
        asm volatile("s_waitcnt vmcnt(0)" : "+v"(ev) :: "memory");
        if (tid == 192) { const u64 sd = mix(ev[0], ev[1]); LD(u32x4, M_CTL + 16) = (u32x4){ev[0], ev[1], 0u, 0u}; LD(u64, M_CTL) = sd; }
        BAR_LDS();
        {
            const f32x4 cs0 = mfma16(ones, trfrag(wb[0][0][0], 1024), z4), cs1 = mfma16(ones, trfrag(wb[0][0][1], 1024), z4);
            asm volatile("s_mov_b32 exec_hi, 0\n\tglobal_store_dword %0, %1, %2 sc1\n\ts_mov_b64 exec, -1" :: "v"(lane * 4), "v"((lane & 16) ? cs1[0] : cs0[0]), "s"(ws + WS_CS + (hg * 32 + tb) * 512 + w * 128) : "memory");
        }
        if (w < 2) {
            f32x4 p[4]; for (int i = 0; i < 4; ++i) p[i] = LD(f32x4, L_PA + i * 2048 + w * 1024 + vl);
            const f32x4 a = (p[0] + p[1]) + (p[2] + p[3]);
            st8_sc1(ws + WS_KV + kvo, ((u64)pk2(a[2], a[3]) << 32) | pk2(a[0], a[1]));
        } else if (w == 2 && !isV && lane < 16) st8_sc1(ws + WS_STATS + (T0 + l15) * 8, ((u64)__float_as_uint(r4[0]) << 32) | __float_as_uint(m4[0]));
        f32x4 qb[2][2]; for (int i = 0; i < 4; ++i) { qa[i >> 1][i & 1] = z4; qb[i >> 1][i & 1] = z4; }
        u32x4 af[2][2][2], bf[2][2][4];
#define QRD(p, s) do { const unsigned sl_ = (slot_of(s) & 1) * SLOT; const int bi_ = slot_of(s) >> 1; for (int ks = 0; ks < 2; ++ks) { \
            af[p][ks][0] = trfrag(wb[bi_][ks][0] + sl_, 1024); bf[p][ks][2] = LD(u32x4, xb_[bi_][ks] + sl_ + 4096); bf[p][ks][3] = LD(u32x4, xb_[bi_][ks] + sl_ + 6144); \
            af[p][ks][1] = trfrag(wb[bi_][ks][1] + sl_, 1024); bf[p][ks][0] = LD(u32x4, xb_[bi_][ks] + sl_); bf[p][ks][1] = LD(u32x4, xb_[bi_][ks] + sl_ + 2048); } } while (0)
#pragma unroll
        for (int s = 0; s < 16; ++s) {
            if (s == 2 || s == 10) WAIT_VM(0);
            if (!(s & 1)) { BAR_LDS(); }
            if (s == 0) { QRD(0, 0); BAR_LDS(); }
            if (s == 2 && tid == 128) st8_sc1(ws + WS_FLAG1 + b * 8, (LD(u64, M_CTL) & ~0xFFFFFFFull) | key);
            if (s == 8 && w >= 2) dma1_sc1(ws + WS_FLAG1 + (w - 2) * 1024, vl, M_GU + (w - 2) * 1024);
            if (s < 15) {
                QRD((s + 1) & 1, s + 1);
                for (int ks = 0; ks < 2; ++ks) for (int dt = 0; dt < 2; ++dt) for (int t = 0; t < 2; ++t) {
                    qb[dt][t] = mfma16(af[s & 1][ks][dt], bf[s & 1][ks][2 + t], qb[dt][t]); qa[dt][t] = mfma16(af[s & 1][ks][dt], bf[s & 1][ks][t], qa[dt][t]); }
                for (int i = 0; i < 16; ++i) { SGB(0x008, 1); SGB(0x100, 1); }
            } else {
                for (int ks = 0; ks < 2; ++ks) for (int i = 0; i < 4; ++i) qb[i >> 1][i & 1] = mfma16(af[1][ks][i >> 1], bf[1][ks][2 + (i & 1)], qb[i >> 1][i & 1]);
                SBAR();
                for (int i = 0; i < 4; ++i) qa[i >> 1][i & 1] = mfma16(af[1][0][i >> 1], bf[1][0][i & 1], qa[i >> 1][i & 1]);
                SBAR();
                for (int i = 0; i < 4; ++i) { LD(f32x4, L_QX + w * 4096 + i * 1024 + vl) = qb[i >> 1][i & 1]; qa[i >> 1][i & 1] = mfma16(af[1][1][i >> 1], bf[1][1][i & 1], qa[i >> 1][i & 1]); SBAR(); }
            }
            SBAR();
        }
        BAR_LDS();
    } else {
        const int lw = w - 4, koff = tb >> 1;
        __builtin_amdgcn_s_sleep(10);
        { const char* wkv = (isV ? wv : wk) + lw * 16384;
          for (int i = 0; i < 4; ++i) dma4(wkv + i * 4096, vl ^ (h2 << 5), L_WKV + lw * 16384 + i * 4096); }
        unsigned vx[2], vq[4];
        for (int i = 0; i < 2; ++i) { const int row = lw * 16 + i * 8 + (lane >> 3); vx[i] = row * 2048 + (((lane & 7) ^ ((row >> 1) & 7)) << 4); }
        for (int i = 0; i < 4; ++i) vq[i] = (lw * 16 + i * 4 + g4) * 2048 + ((((lane >> 1) & 7) ^ ((g4 << 1) | (i >> 1))) << 5) + ((lane & 1) << 4);
        const char* xb = x + (size_t)tb * 64 * 2048; const char* qb_ = wq + hg * 256;
#define RING_X(s) asm volatile("s_mov_b32 m0, %3\n\ts_nop 0\n\tglobal_load_lds_dwordx4 %0, %2\n\ts_add_u32 m0, m0, 1024\n\ts_nop 0\n\tglobal_load_lds_dwordx4 %1, %2" \
                     :: "v"(vx[0]), "v"(vx[1]), "s"(xb + (((s) + koff) & 15) * 128), "s"(slot_of(s) * SLOT + lw * 2048) : "memory", "scc")
#define RING_W(s) asm volatile("s_mov_b32 m0, %5\n\ts_nop 0\n\tglobal_load_lds_dwordx4 %0, %4\n\ts_add_u32 m0, m0, 1024\n\ts_nop 0\n\tglobal_load_lds_dwordx4 %1, %4\n\t" \
                     "s_add_u32 m0, m0, 1024\n\ts_nop 0\n\tglobal_load_lds_dwordx4 %2, %4\n\ts_add_u32 m0, m0, 1024\n\ts_nop 0\n\tglobal_load_lds_dwordx4 %3, %4" \
                     :: "v"(vq[0]), "v"(vq[1]), "v"(vq[2]), "v"(vq[3]), "s"(qb_ + (size_t)(((s) + koff) & 15) * 131072), "s"(slot_of(s) * SLOT + 8192 + lw * 4096) : "memory", "scc")
#define RING_ISSUE(s) do { RING_X(s); RING_W(s); } while (0)
        RING_ISSUE(0); SETUP();
        BAR_LDS(); RING_ISSUE(1);
        BAR_VM(12); RING_ISSUE(2); RING_W(3);
        BAR_VM(16); RING_ISSUE(4); RING_ISSUE(5);
        BAR_VM(16); RING_X(3);
        BAR_LDS(); RING_ISSUE(6);
        BAR_VM(6); RING_ISSUE(7); RING_ISSUE(8);
        BAR_VM(12); RING_ISSUE(9); RING_ISSUE(10);
        BAR_VM(12); RING_ISSUE(11); RING_ISSUE(12);
        BAR_VM(12);
        RING_ISSUE(13); RING_ISSUE(14);
        const u64 tag36 = LD(u64, M_CTL) & ~0xFFFFFFFull;
        BAR_VM(12); RING_ISSUE(15);
        u64 f[4]; { const u32x4 f0 = LD(u32x4, M_GU + vl), f1 = LD(u32x4, M_GU + 1024 + vl); f[0] = ((u64)f0[1] << 32) | f0[0]; f[1] = ((u64)f0[3] << 32) | f0[2]; f[2] = ((u64)f1[1] << 32) | f1[0]; f[3] = ((u64)f1[3] << 32) | f1[2]; }
#define FLAGS_OK() __all((((f[0] ^ tag36) | (f[1] ^ tag36) | (f[2] ^ tag36) | (f[3] ^ tag36)) >> 28) == 0)
        bool early = FLAGS_OK();
        if (!early) { for (int i = 0; i < 4; ++i) f[i] = ld8_sc1(ws + WS_FLAG1 + (i * 64 + lane) * 8); BAR_VM(6); early = FLAGS_OK(); }
        else BAR_VM(6);
        if (!early) {
            BAR_VM(0);
#pragma unroll 1
            for (unsigned spins = 0; spins < SPIN_CAP; ++spins) {
                for (int i = 0; i < 4; ++i) f[i] = ld8_sc1(ws + WS_FLAG1 + (i * 64 + lane) * 8);
                if (FLAGS_OK()) break;
                __builtin_amdgcn_s_sleep(2);
            }
            PIN(f[0]); PIN(f[1]); PIN(f[2]); PIN(f[3]);
        }
        for (int i = 0; i < 4; ++i) dma1_sc1(ws + WS_CS + hg * 16384 + i * 4096 + lw * 128, (lane >> 3) * 512 + (lane & 7) * 16, L_CSP + lw * 4096 + i * 1024);
        if (lw < 2) { dma4_sc1(kv + ((c0 * 8 + 62 + lw) & 63) * 4096, vl, L_PRO + lw * 4096); if (early) BAR_VM(8); }
        else if (lw == 2) { dma1_sc1(ws + WS_STATS + tb * 512, l31 * 16, M_ST64); if (early) BAR_VM(5); }
        else {
            unsigned key = rowmax16(max(max((unsigned)f[0] & 0xFFFFFFFu, (unsigned)f[1] & 0xFFFFFFFu), max((unsigned)f[2] & 0xFFFFFFFu, (unsigned)f[3] & 0xFFFFFFFu)));
            key = max(max((unsigned)__builtin_amdgcn_readlane(key, 0), (unsigned)__builtin_amdgcn_readlane(key, 16)), max((unsigned)__builtin_amdgcn_readlane(key, 32), (unsigned)__builtin_amdgcn_readlane(key, 48)));
            const int ix = 2047 - (key & 2047);
            dma1(x + (size_t)ix * 2048 + (mi & 3) * 512, l31 * 16, M_XSEL);
            asm volatile("s_mov_b32 m0, %2\n\ts_nop 0\n\tglobal_load_lds_dword %0, %1 sc1" :: "v"((lane & 1) * 4), "s"(ws + WS_STATS + ix * 8), "s"(M_STX) : "memory");
            if (early) BAR_VM(6);
            if (lane == 0) { LD(unsigned, M_CTL + 8) = key; if (b == 0) st8_sc1(ws + WS_EPOCH, LD(u64, M_CTL + 16) + 1); }
        }
        WAIT_VM(0);
        { float p[16]; for (int i = 0; i < 16; ++i) p[i] = LD(float, L_CSP + lw * 4096 + (h2 * 16 + i) * 128 + l31 * 4);
          const float csum = xsum32((((p[0] + p[1]) + (p[2] + p[3])) + ((p[4] + p[5]) + (p[6] + p[7]))) + (((p[8] + p[9]) + (p[10] + p[11])) + ((p[12] + p[13]) + (p[14] + p[15]))));
          const float c1 = rb(csum), c2 = rb(csum - c1); const u32x2 csp = (u32x2){pk2(c1, c2), pk2(csum - c1 - c2, 0.f)};
          LD(u32x2, M_CS + lw * 512 + lane * 8) = csp; }
        dma4_sc1(kv + c0 * 32768 + lw * 8192, vl, KA + lw * 8192); dma4_sc1(kv + c0 * 32768 + lw * 8192 + 4096, vl, KA + lw * 8192 + 4096);
        __builtin_amdgcn_s_setprio(3);
        BAR_LDS();
        for (int q = 0; q < 4; ++q) qa[q >> 1][q & 1] = LD(f32x4, L_QX + hd * 4096 + q * 1024 + vl);
    }
    u32x8 Q, V; u32x16 PK; f32x16 SA, SB, NEG;
    const u32x4 ctl = LD(u32x4, M_CTL);
    {
        const f32x2 st[2] = {LD(f32x2, sta), LD(f32x2, sta + 128)};
        const u32x2 csp[2] = {LD(u32x2, csa), LD(u32x2, csa + 128)};
        const u32x4 k00 = LD(u32x4, L_PRO + vl), k01 = LD(u32x4, L_PRO + 1024 + vl), k10 = LD(u32x4, L_PRO + 4096 + vl), k11 = LD(u32x4, L_PRO + 5120 + vl), v00 = LD(u32x4, L_PRO + 6144 + vl);
        SBAR();
        for (int t = 0; t < 2; ++t) { const unsigned nm = ((__float_as_uint(st[t][0]) >> 16) ^ 0x8000u) & g0m; const u32x4 cb = {nm * 0x10001u, nm, PZ[0], PZ[1]};
            for (int dt = 0; dt < 2; ++dt) qa[dt][t] = mfma16((u32x4){csp[dt][0], csp[dt][1], PZ[0], PZ[1]}, cb, qa[dt][t]); }
        const float qs0 = st[0][1] * (0.17677669529663687f * 1.4426950408889634f), qs1 = st[1][1] * (0.17677669529663687f * 1.4426950408889634f);
        for (int dt = 0; dt < 2; ++dt) for (int i = 0; i < 2; ++i) {
            const auto e = __builtin_amdgcn_permlane16_swap(pk2(qa[dt][0][2 * i] * qs0, qa[dt][0][2 * i + 1] * qs0), pk2(qa[dt][1][2 * i] * qs1, qa[dt][1][2 * i + 1] * qs1), false, false);
            Q[4 * dt + i] = e[0]; Q[4 * dt + 2 + i] = e[1]; }
        f32x16 s0 = mfma32(k00, lo4(Q), O); s0 = mfma32(k01, hi4(Q), s0);
        for (int i = 0; i < 4; ++i) { PK[i] = PZ[i]; PK[4 + i] = PZ[4 + i]; PK[8 + i] = k10[i]; PK[12 + i] = k11[i]; V[i] = v00[i]; V[4 + i] = k00[i]; }
        asm volatile("s_nop 7\n\ts_nop 3" : "+v"(s0));
        float m = max3(max3(s0[0], s0[1], s0[2]), max3(s0[3], s0[4], s0[5]), s0[15]);
        m = max3(max3(max3(s0[6], s0[7], s0[8]), max3(s0[9], s0[10], s0[11]), max3(s0[12], s0[13], s0[14])), m, m);
        const auto mm_ = __builtin_amdgcn_permlane32_swap(__float_as_uint(m), __float_as_uint(m), false, false);
        m = max3(__uint_as_float(mm_[0]), __uint_as_float(mm_[1]), __uint_as_float(mm_[1]));
        NEG = __builtin_amdgcn_mfma_f32_32x32x2f32(aone, -m, O, 0, 0, 0);
        PIN(NEG);
        for (int i = 0; i < 16; ++i) SA[i] = s0[i] - m;
        PIN(SA);
    }
    const u64 seed = ((u64)(unsigned)__builtin_amdgcn_readfirstlane(ctl[1]) << 32) | (unsigned)__builtin_amdgcn_readfirstlane(ctl[0]);
    const unsigned tag32 = (unsigned)seed ^ 0x5bd1e995u, gkey = __builtin_amdgcn_readfirstlane(ctl[2]);
    const int idx = 2047 - (gkey & 2047), cbk = hd, kh = tbk;
    const bool moe = r < 28;
    u32x8 wf[16];
#define SEAM(c) const unsigned ad = (((c) & 1) ? KB : KA) + vl, mk = (((c) & 1) ? KA : KB) + w * 4096; const char* sk = kv + ((c0 + (c) + 1) & 7) * 32768 + w * 4096
#define MROW() (16 * (g4 >> 1) + 4 * (g4 & 1) + (l15 >> 2))
#define MSWZ() (2 * (g4 & 1) + (l15 >> 3))
    { const unsigned ad = KA + vl, mk = KB + w * 4096, vm = vm0, mm = R2 + w * 4096; const u64 sk = sk0;
      asm volatile("" :: "s"(seed), "s"(gkey));
      asm volatile(HAND(0) BODY_TXT(T0(WOA(0), NO, NO), T1(WOA(1024), NO, NO), T2(KV0, KVN(1024), NO), T3(KVN(2048), KVN(3072), NO), T4(MOE(0, 0), MOE(1024, 57344), NO), T5(MOE(2048, 57344), MOE(3072, 57344), NO),
                            T6(NO, NO, NO), U7(VM(8) TRA(0, 0) TRA(1, 1024), NO, NO, 6, 2))
                   : BODY_OUT, "=&{v[128:135]}"(wf[0]) : BODY_IN : "memory", "scc"); }
    { SEAM(1); const unsigned vm = vm0, mm = mb; smo = (u64)(wu + (size_t)((mi & 3) * 256 + w * 32) * 7168 + (mi >> 2) * 128);
      asm volatile(HAND(4) BODY_TXT(T0(WOA(0), NO, NO), T1(WOA(1024), NO, NO), T2(KV0, KVN(1024), NO), T3(KVN(2048), KVN(3072), NO), T4(MOE(0, 0), MOE(1024, 57344), NO), T5(MOE(2048, 57344), MOE(3072, 57344), NO),
                            T6(NO, NO, NO), U7(VM(8) TRA(2, 0) TRA(3, 1024), NO, NO, 6, 2))
                   : BODY_OUT, "=&{v[136:143]}"(wf[1]) : BODY_IN : "memory", "scc"); }
    { if (w == 0) { RELANE(); const f32x2 sx = LD(f32x2, M_STX); const u32x2 v = LD(u32x2, M_XSEL + lane * 8); LD(u32x2, M_XSEL + lane * 8) = (u32x2){ln2(v[0], sx[0], sx[1]), ln2(v[1], sx[0], sx[1])}; }
      SEAM(2); const unsigned vm = 0, mm = 0;
      asm volatile(HAND(4) BODY_TXT(T0(WOA(0), NO, NO), T1(WOA(1024), NO, NO), T2(KV0, NO, NO), T3(KVN(1024), NO, NO), T4(KVN(2048), NO, NO), T5(KVN(3072), NO, NO), T6(NO, NO, NO), U7(VM(4) TRA(4, 0) TRA(5, 1024), NO, NO, 6, 2))
                   : BODY_OUT, "=&{v[144:151]}"(wf[2]) : BODY_IN : "memory", "scc"); }
    { RELANE(); const unsigned vm = R2 + w * 4096 + MROW() * 128 + MSWZ() * 32 + (lane & 3) * 8, mm = 0;
      const unsigned xa = M_XSEL + w * 64 + (g4 >> 1) * 32 + (g4 & 1) * 8, gw = M_GU + w * 256 + lane * 4;
      SEAM(3);
#define GM(d, a) M16("v[" #d ":" #d "+3], v[176:179], v[" #a ":" #a "+3], 0")
      asm volatile(HAND(0) BODY_TXT(U0(WOA(0) "v_xor_b32 v244, 32, v19\n v_xor_b32 v245, 64, v19\n v_xor_b32 v246, 0x60, v19\n v_subrev_u32 v247, 0x10000, v19\n" B64(176, "v15", 0) B64(178, "v15", 16) TR2(180, "v19", 0, 1024),
                               WOA(1024) TR2(184, "v244", 0, 1024) TR2(188, "v245", 0, 1024) "v_subrev_u32 v244, 0x10000, v244\n v_subrev_u32 v245, 0x10000, v245\n", NO, 6, 6),
                            U1(TR2(192, "v246", 0, 1024) TR2(196, "v247", 0, 1024) "v_subrev_u32 v246, 0x10000, v246\n", TR2(200, "v244", 0, 1024) TR2(204, "v245", 0, 1024), GM(212, 180) GM(216, 184), 6, 6),
                            U2(TR2(208, "v246", 0, 1024) KV0, KVN(1024), GM(220, 188) GM(224, 192), 4, 2), T3(KVN(2048) KVN(3072), WOB(0), GM(228, 196) GM(232, 200)), T4(WOB(1024), WOB(2048), GM(236, 204) GM(240, 208)),
                            T5(WOB(3072), NO, NO),
                            U6("s_mov_b32 vcc_lo, 0xffff0000\n s_mov_b32 vcc_hi, 0xffff0000\n v_cndmask_b32 v212, v212, v216, vcc\n v_cndmask_b32 v220, v220, v224, vcc\n v_cndmask_b32 v228, v228, v232, vcc\n"
                               "v_cndmask_b32 v236, v236, v240, vcc\n s_mov_b32 vcc_lo, 0\n s_mov_b32 vcc_hi, -1\n v_cndmask_b32 v212, v212, v220, vcc\n v_cndmask_b32 v228, v228, v236, vcc\n",
                               "ds_write_b32 v16, v212\n ds_write_b32 v16, v228 offset:2048\n" VM(8) TRA(6, 0) TRA(7, 1024), NO, 2, 8),
                            U7(VM(0) TRB(8, 0) TRB(9, 1024), TRB(10, 2048) TRB(11, 3072), NO, 6, 6))
                   : BODY_OUT, "=&{v[152:159]}"(wf[3]), "=&{v[160:167]}"(wf[4]), "=&{v[168:175]}"(wf[5]) : BODY_IN, "{v15}"(xa), "{v16}"(gw)
                   : "memory", "scc", C8(176,177,178,179,180,181,182,183), C8(184,185,186,187,188,189,190,191), C8(192,193,194,195,196,197,198,199), C8(200,201,202,203,204,205,206,207),
                     C8(208,209,210,211,212,213,214,215), C8(216,217,218,219,220,221,222,223), C8(224,225,226,227,228,229,230,231), C8(232,233,234,235,236,237,238,239), C8(240,241,242,243,244,245,246,247), "vcc"); }
    const unsigned ye = moe ? 0xFFFFu : 0u;
    { RELANE(); const unsigned vm = (lane >> 3) * 2048 + (((lane & 7) ^ (g4 << 1)) << 4), mm = R2 + w * 2048; smo = (u64)(wd + (size_t)(rm * 128 + w * 16) * 2048 + xcd * 256);
      const unsigned ga = M_GU + (w >> 2) * 2048 + ((w & 3) * 16 + l15) * 4, go = l15 * 64; const int mu = __builtin_amdgcn_readfirstlane(mi); const char* gs = ws + WS_GU + ((mu >> 2) * 64 + (w & 3) * 16) * 64 + (w >> 2) * 32 + (mu & 3) * 8;
      SEAM(4);
#define R2S(d, a, b) "ds_read2st64_b32 v[" #d ":" #d "+1], v15 offset0:" #a " offset1:" #b "\n"
      asm volatile(HAND(0) BODY_TXT(U0(WOA(0) R2S(224, 0, 1) R2S(226, 2, 3), WOA(1024) R2S(228, 4, 5) R2S(230, 6, 7), NO, 4, 4),
                            T1(WOB(0), WOB(1024) "v_add_f32 v224, v224, v225\n v_add_f32 v226, v226, v227\n v_add_f32 v228, v228, v229\n v_add_f32 v230, v230, v231\n v_add_f32 v224, v224, v226\n v_add_f32 v228, v228, v230\n"
                               "v_mov_b32 v225, %[tg]\n v_add_f32 v224, v224, v228\n", "s_mov_b32 exec_lo, %[ye]\n s_mov_b32 exec_hi, 0\n global_store_dwordx2 v16, v[224:225], %[gs] sc1\n s_mov_b64 exec, -1\n"),
                            T2(WOB(2048), WOB(3072), NO), T3(KV0, KVN(1024), NO), T4(KVN(2048), KVN(3072), NO), T5(MOE(0, 0), MOE(16384, 128), NO),
                            U6(MOE(1024, 16256), MOE(17408, 128) VM(8) TRA(12, 0) TRA(13, 1024), NO, 2, 6), U7(TRB(14, 0) TRB(15, 1024), TRB(16, 2048) TRB(17, 3072), NO, 6, 6))
                   : BODY_OUT, "=&{v[176:183]}"(wf[6]), "=&{v[184:191]}"(wf[7]), "=&{v[192:199]}"(wf[8]) : BODY_IN, "{v15}"(ga), "{v16}"(go), [ye] "s"(ye), [tg] "s"(tag32), [gs] "s"(gs)
                   : "memory", "scc", C8(224,225,226,227,228,229,230,231)); }
    u64 bad;
    { RELANE(); const unsigned vm = 0, mm = M_GR + w * 1024, ga = M_GR + w * 1024 + lane * 16, hw = M_H + w * 32 + (lane >> 2) * 2; smo = (u64)(ws + WS_GU + (size_t)(rm * 128 + w * 16) * 64);
      SEAM(5);
      asm volatile(HAND(4) BODY_TXT(T0(WOA(0), WOA(1024), NO), T1(WOB(0), WOB(1024), NO), T2(WOB(2048), WOB(3072), GRD), T3(KV0, KVN(1024), NO), T4(KVN(2048), KVN(3072), NO), T5(NO, NO, NO),
                            U6(VM(4) "ds_read_b128 v[224:227], v15\n" TRA(18, 0) TRA(19, 1024), TRB(20, 0) TRB(21, 1024), NO, 7, 6),
                            U7("v_add_f32 v224, v224, v226\n v_cmp_ne_u32 vcc, %[tg], v225\n v_cmp_ne_u32 s[74:75], %[tg], v227\n v_add_f32_dpp v224, v224, v224 quad_perm:[1,0,3,2] row_mask:0xf bank_mask:0xf\n"
                               "v_cvt_pk_bf16_f32 v224, v224, v224\n v_lshlrev_b32 v224, 16, v224\n v_mul_f32 v226, 0xbfb8aa3b, v224\n v_exp_f32 v226, v226\n"
                               "v_mov_b32_dpp v225, v224 quad_perm:[2,3,0,1] row_mask:0xf bank_mask:0xf\n s_or_b64 s[74:75], vcc, s[74:75]\n" TRB(22, 2048),
                               "v_add_f32 v226, 1.0, v226\n v_rcp_f32 v226, v226\n" TRB(23, 3072),
                               "v_mul_f32 v226, v224, v226\n v_cvt_pk_bf16_f32 v226, v226, v226\n v_lshlrev_b32 v226, 16, v226\n v_mul_f32 v226, v226, v225\n v_cvt_pk_bf16_f32 v226, v226, v226\n"
                               "s_mov_b32 exec_lo, 0x11111111\n s_mov_b32 exec_hi, 0x11111111\n ds_write_b16 v16, v226\n s_mov_b64 exec, -1\n", 4, 5))
                   : BODY_OUT, "=&{v[200:207]}"(wf[9]), "=&{v[208:215]}"(wf[10]), "=&{v[216:223]}"(wf[11]), "=&{s[74:75]}"(bad) : BODY_IN, "{v15}"(ga), "{v16}"(hw), [tg] "s"(tag32)
                   : "memory", "scc", "vcc", "v224", "v225", "v226", "v227"); }
    { RELANE();
      if (__builtin_expect(moe && bad != 0, 0)) {
          u32x4 gq;
#pragma unroll 1
          for (unsigned spins = 0; spins < SPIN_CAP; ++spins) {
              const char* p = ws + WS_GU + (size_t)(r * 128 + w * 16) * 64 + lane * 16; const u64 v0 = ld8_sc1(p), v1 = ld8_sc1(p + 8); gq = (u32x4){(unsigned)v0, (unsigned)(v0 >> 32), (unsigned)v1, (unsigned)(v1 >> 32)};
              if (__all(gq[1] == tag32 && gq[3] == tag32)) break;
              __builtin_amdgcn_s_sleep(2); }
          float sm = __uint_as_float(gq[0]) + __uint_as_float(gq[2]);
          sm = rb(sm + __uint_as_float(__builtin_amdgcn_update_dpp(0, (int)__float_as_uint(sm), 0xB1, 0xf, 0xf, false)));
          const float uu = __uint_as_float(__builtin_amdgcn_update_dpp(0, (int)__float_as_uint(sm), 0x4E, 0xf, 0xf, false));
          if (!(lane & 3)) LD(unsigned short, M_H + w * 32 + (lane >> 2) * 2) = (unsigned short)pk2(rb(silu(sm)) * uu, 0.f);
      }
      const unsigned vm = R2 + (w >> 2) * 16384 + MROW() * 128 + ((MSWZ() ^ (w & 3)) * 32) + (lane & 3) * 8, mm = tag32, ha = M_H + (g4 >> 1) * 32 + (g4 & 1) * 8, yo = lane * 8;
      smo = (u64)(ws + WS_YG + (rm * 1024 + xcd * 128 + w * 16) * 8);
      SEAM(6);
#define DM(h, d, c) M16("v[248:251], v[" #h ":" #h "+3], v[" #d ":" #d "+3], " c)
      asm volatile(HAND(0) BODY_TXT(U0(WOA(0) B64(224, "v15", 0) B64(226, "v15", 16) TR2(228, "v19", 0, 1024), WOA(1024) B64(232, "v15", 64) B64(234, "v15", 80) TR2(236, "v19", 4096, 1024), NO, 6, 6),
                            T1(WOB(0), WOB(1024), DM(224, 228, "0") DM(232, 236, "v[248:251]")),
                            U2(WOB(2048) B64(224, "v15", 128) B64(226, "v15", 144) TR2(228, "v19", 8192, 1024), WOB(3072) B64(232, "v15", 192) B64(234, "v15", 208) TR2(236, "v19", 12288, 1024), NO, 6, 6),
                            T3(KV0, KVN(1024), DM(224, 228, "v[248:251]") DM(232, 236, "v[248:251]")), T4(KVN(2048), KVN(3072), NO),
                            T5("v_mov_b32 v249, %[mm]\n", "s_mov_b32 exec_lo, %[ye]\n s_mov_b32 exec_hi, 0\n global_store_dwordx2 v16, v[248:249], s[72:73] sc1\n s_mov_b64 exec, -1\n", NO),
                            U6(NO, VM(4) TRA(24, 0) TRA(25, 1024), NO, 2, 6), U7(TRB(26, 0) TRB(27, 1024), TRB(28, 2048) TRB(29, 3072), NO, 6, 6))
                   : BODY_OUT, "=&{v[224:231]}"(wf[12]), "=&{v[232:239]}"(wf[13]), "=&{v[240:247]}"(wf[14]) : BODY_IN, "{v15}"(ha), "{v16}"(yo), [ye] "s"(ye)
                   : "memory", "scc", "v248", "v249", "v250", "v251"); }
    { SEAM(7);
      const unsigned vm = vw ^ ((vw >> 9) & 48), mm = mb; smo = (u64)(x + (size_t)(tb * 64 + kh * 32) * 2048 + hg * 256 + cbk * 64);
      asm volatile(HAND(0) BODY_TXT(T0(WOA(0), NO, NO), T1(WOA(1024), NO, NO), T2(MOX(0, 0), NO, NO), T3(MOX(1024, 32768), NO, NO), T4(NO, NO, NO), T5(NO, NO, NO), T6(NO, NO, NO), U7(VM(0) TRA(30, 0) TRA(31, 1024), NO, NO, 6, 2)) "s_waitcnt lgkmcnt(0)\n s_nop 7\n"
                   : BODY_OUT, "=&{v[248:255]}"(wf[15]) : BODY_IN : "memory", "scc"); }
    {
    RELANE(); const unsigned vl = lane * 16;
    u32x4 o0, o1;
    const bool routed = tb == (idx >> 6) && kh == ((idx >> 5) & 1);
    u32x4 xr[2]; for (int q = 0; q < 2; ++q) xr[q] = LD(u32x4, mb + l31 * 64 + (((2 * q + h2) ^ ((l31 >> 2) & 3)) * 16));
    const f32x2 stt = LD(f32x2, M_ST64 + (tbk * 32 + l31) * 8);
    {
        const u32x8 P = __builtin_shufflevector(PK, PK, 0, 1, 2, 3, 4, 5, 6, 7); f32x4 L = __builtin_bit_cast(f32x4, lo4(LS));
        asm volatile("s_nop 1\n\tv_smfmac_f32_16x16x64_bf16 %0, %1, %2, %3\n\ts_nop 11" : "+v"(L) : "v"(hi4(LS)), "v"(P), "v"(IDX));
        O = mfma32(hi4(V), hi4(P), O);
        const auto l16 = __builtin_amdgcn_permlane16_swap(__float_as_uint(L[0] + L[1]), __float_as_uint(L[2] + L[3]), false, false);
        float lsum = __uint_as_float(__builtin_amdgcn_permlane32_swap(l16[0], l16[0], false, false)[0]);
        const f32x2 os = (((f32x2){O[0], O[1]} + (f32x2){O[2], O[3]}) + ((f32x2){O[4], O[5]} + (f32x2){O[6], O[7]})) + (((f32x2){O[8], O[9]} + (f32x2){O[10], O[11]}) + ((f32x2){O[12], O[13]} + (f32x2){O[14], O[15]}));
        const float om = fabsf(os[0] + os[1]);
        if (__builtin_expect(__any(!(lsum <= 1.8446744e19f) || !(om <= 3.0e38f)), 0)) {
            float mr = -INFINITY; lsum = 0.f; for (int i = 0; i < 16; ++i) O[i] = 0.f;
#pragma unroll 1
            for (int t = 0; t < 64; ++t) {
                u32x4 k0, k1, v0, v1;
                asm volatile("global_load_dwordx4 %0, %4, %5 sc1\n\tglobal_load_dwordx4 %1, %4, %5 offset:1024 sc1\n\tglobal_load_dwordx4 %2, %4, %6 offset:2048 sc1\n\tglobal_load_dwordx4 %3, %4, %7 offset:3072 sc1\n\ts_waitcnt vmcnt(0)"
                             : "=&v"(k0), "=&v"(k1), "=&v"(v0), "=&v"(v1) : "v"(vl), "s"(kv + ((t + 62) & 63) * 4096), "s"(kv + ((t + 63) & 63) * 4096), "s"(kv + t * 4096) : "memory");
                f32x16 z; for (int i = 0; i < 16; ++i) z[i] = 0.f;
                f32x16 s = mfma32(k0, lo4(Q), z); s = mfma32(k1, hi4(Q), s);
                float m = s[0]; for (int i = 1; i < 16; ++i) m = fmaxf(m, s[i]);
                m = fmaxf(mr, xmax32(m)); const float al = __builtin_amdgcn_exp2f(mr - m); mr = m;
                u32x8 pp; float ps = 0.f;
                for (int i = 0; i < 8; ++i) { pp[i] = pk2(__builtin_amdgcn_exp2f(s[2 * i] - m), __builtin_amdgcn_exp2f(s[2 * i + 1] - m)); ps += bf_lo(pp[i]) + bf_hi(pp[i]); }
                lsum = lsum * al + xsum32(ps); for (int i = 0; i < 16; ++i) O[i] *= al;
                O = mfma32(v0, lo4(pp), O); O = mfma32(v1, hi4(pp), O);
            }
        }
        const float rl = __builtin_amdgcn_rcpf(lsum);
        for (int i = 0; i < 4; ++i) { o0[i] = pk2(O[2 * i] * rl, O[2 * i + 1] * rl); o1[i] = pk2(O[8 + 2 * i] * rl, O[8 + 2 * i + 1] * rl); }
        asm volatile("global_store_dwordx4 %0, %1, %3 sc1\n\tglobal_store_dwordx4 %0, %2, %3 offset:2048 sc1\n\ts_nop 1"
                     :: "v"(vl), "v"(o0), "v"(o1), "s"(ws + WS_O + (size_t)(tb * 8 + hg) * 16384 + hd * 4096 + tbk * 1024) : "memory");
    }
    u64 yg[14];
    if (routed) for (int i = 0; i < 14; ++i) yg[i] = ld8_sc1(ws + WS_YG + ((h2 * 14 + i) * 1024 + hg * 128 + cbk * 32 + l31) * 8);
    BAR_LDS();
    const unsigned pv = w * PRIV;
    LD(u32x4, hg * PRIV + hd * 4096 + tbk * 1024 + vl) = o0; LD(u32x4, hg * PRIV + hd * 4096 + 2048 + tbk * 1024 + vl) = o1;
    unsigned xn2[8], g2[8];
    for (int i = 0; i < 8; ++i) { xn2[i] = ln2(xr[i >> 2][i & 3], stt[0], stt[1]); PIN(xn2[i]); }
    WAIT_VM(0);
    for (int i = 0; i < 14; ++i) asm volatile("" :: "v"(yg[i]));
    if (lane == 0) st8_sc1(ws + WS_FLAG4 + b * 128 + w * 8, seed);
    if (routed) {
#pragma unroll 1
        for (unsigned spins = 0; spins < SPIN_CAP; ++spins) {
            bool ok = true; for (int i = 0; i < 14; ++i) ok &= (unsigned)(yg[i] >> 32) == tag32;
            if (__all(ok)) break;
            __builtin_amdgcn_s_sleep(2);
            for (int i = 0; i < 14; ++i) yg[i] = ld8_sc1(ws + WS_YG + ((h2 * 14 + i) * 1024 + hg * 128 + cbk * 32 + l31) * 8);
        }
        float acc = 0.f; for (int i = 0; i < 14; ++i) acc += __uint_as_float((unsigned)yg[i]);
        const unsigned o16 = (gkey >> 11) & 0xffffu, rbits = (o16 & 0x8000u) ? (o16 & 0x7fffu) : (~o16 & 0xffffu);
        const float rsel = rb(__builtin_amdgcn_rcpf(1.f + __builtin_amdgcn_exp2f(-1.44269504089f * bf_lo(rbits))));
        LD(float, pv + 16384 + lane * 4) = rb(rb(xsum32(acc)) * rsel);
        for (int q = 0; q < 4; ++q) { const f32x4 y = LD(f32x4, pv + 16384 + (q >> 1) * 64 + h2 * 32 + (q & 1) * 16);
            for (int i = 0; i < 2; ++i) { const unsigned t = pk2(bf_lo(xn2[2 * q + i]) + y[2 * i], bf_hi(xn2[2 * q + i]) + y[2 * i + 1]); xn2[2 * q + i] = l31 == (idx & 31) ? t : xn2[2 * q + i]; } }
    }
    for (int i = 0; i < 8; ++i) { g2[i] = pk2(silu(bf_lo(xn2[i])), silu(bf_hi(xn2[i]))); PIN(g2[i]); }
    if (w != hg) {
        const int pb = (((w & 3) * 8 + (tb & 7)) << 3) | ((tb >> 3) * 2 + (w >> 2));
#pragma unroll 1
        for (unsigned spins = 0; spins < SPIN_CAP; ++spins) { if (__all(ld8_sc1(ws + WS_FLAG4 + pb * 128 + (lane & 7) * 8) == seed)) break; __builtin_amdgcn_s_sleep(1); }
        const char* src = ws + WS_O + (size_t)(tb * 8 + w) * 16384;
        for (int i = 0; i < 4; ++i) dma4_sc1(src + i * 4096, vl, pv + i * 4096);
    }
    unsigned tw0 = (w ^ 4) * PRIV + 16384 + l31 * 64 + ((h2 ^ ((l31 >> 1) & 3)) << 4), tr0 = (w ^ 4) * PRIV + 16384 + vl;
    unsigned oo = g4 * 2048 + l15 * 16; u64 op = (u64)(out + (size_t)(tb * 64 + kh * 32 + cbk * 8) * 2048 + hg * 256), op2 = op + 8192;
    unsigned tq0 = ((kh ^ 1) * 4 + (l15 >> 2)) * PRIV + 16384 + (cbk * 8 + g4) * 64 + (((lane & 3) ^ (g4 >> 1)) << 4);
    PIN(tw0); PIN(tr0); PIN(oo); PIN(tq0); asm volatile("" : "+s"(op), "+s"(op2));
    BAR_VM(12);
    f32x4 ao[2][2], at[2][2]; for (int i = 0; i < 4; ++i) { ao[i >> 1][i & 1] = (f32x4){0, 0, 0, 0}; at[i >> 1][i & 1] = (f32x4){0, 0, 0, 0}; }
    {
        const unsigned oa = kh * 4 * PRIV + (g4 >> 1) * 2048 + (g4 & 1) * 512 + l15 * 16 + kh * 1024, ob = oa ^ 1024;
        u32x4 fr[2][8]; f32x4 pt[4];
#define OAD(g, i) ((g) < 4 ? (((i) & 1) ? oa : ob) + (((g) & 1) * 2 + ((i) >> 2)) * PRIV + ((g) >> 1) * 4096 + (((i) >> 1) & 1) * 256 : ((g) < 6 ? ob : oa) + ((i) >> 1) * PRIV + (2 + ((g) & 1)) * 4096 + ((i) & 1) * 256)
#define OWU(g, i) ((g) < 4 ? (((g) & 1) * 2 + ((i) >> 2)) * 4 + ((g) >> 1) : ((i) >> 1) * 4 + 2 + ((g) & 1))
#define ORD(p, g) do { for (int i = 0; i < 8; ++i) fr[p][i] = LD(u32x4, OAD(g, i)); } while (0)
        __builtin_amdgcn_s_setprio(1);
        ORD(0, 0); SBAR();
#pragma unroll
        for (int g = 0; g < 8; ++g) {
            if (g == 1) asm volatile("s_waitcnt vmcnt(8)\n\ts_barrier" ::: "memory");
            if (g == 3) asm volatile("s_waitcnt vmcnt(4)\n\ts_barrier" ::: "memory");
            if (g == 4) asm volatile("s_waitcnt vmcnt(0)\n\ts_barrier" ::: "memory");
            if (g == 6) { __builtin_amdgcn_s_setprio(0); ORD(1, 7); SBAR(); for (int q = 0; q < 4; ++q) LD(f32x4, pv + 16384 + q * 1024 + vl) = at[q >> 1][q & 1]; SBAR(); }
            if (g < 6) ORD((g + 1) & 1, g + 1);
            for (int i = 0; i < 8; ++i) { const int t2 = g < 4 ? (i >> 1) & 1 : i & 1; const u32x8 a = wf[OWU(g, i)];
                if (g < 4 ? (i & 1) : g >= 6) { ao[0][t2] = mfma16(lo4(a), fr[g & 1][i], ao[0][t2]); ao[1][t2] = mfma16(hi4(a), fr[g & 1][i], ao[1][t2]); }
                else { at[0][t2] = mfma16(lo4(a), fr[g & 1][i], at[0][t2]); at[1][t2] = mfma16(hi4(a), fr[g & 1][i], at[1][t2]); }
                if (g == 7 && i == 5) { SBAR(); __builtin_amdgcn_s_setprio(1); BAR_LDS(); for (int q = 0; q < 4; ++q) pt[q] = LD(f32x4, tr0 + q * 1024); SBAR(); } }
            if (g < 6) for (int i = 0; i < 8; ++i) { SGB(0x008, 2); SGB(0x100, 1); }
            SBAR();
        }
        __builtin_amdgcn_s_setprio(0);
        for (int nt = 0; nt < 2; ++nt) {
            const f32x4 u0 = ao[nt][0] + pt[2 * nt], u1 = ao[nt][1] + pt[2 * nt + 1];
            const auto e0 = __builtin_amdgcn_permlane16_swap(pk2(u0[0], u0[1]), pk2(u1[0], u1[1]), false, false), e1 = __builtin_amdgcn_permlane16_swap(pk2(u0[2], u0[3]), pk2(u1[2], u1[3]), false, false);
            const u32x4 s2 = {e0[0], e1[0], e0[1], e1[1]}; u32x4 o;
            for (int i = 0; i < 4; ++i) { const unsigned gg = g2[4 * nt + i], p2 = pk2(bf_lo(s2[i]) * bf_lo(gg), bf_hi(s2[i]) * bf_hi(gg)); o[i] = pk2(bf_lo(xr[nt][i]) + bf_lo(p2), bf_hi(xr[nt][i]) + bf_hi(p2)); }
            LD(u32x4, tw0 ^ (nt << 5)) = o;
        }
        BAR_LDS();
        const u32x4 r0 = LD(u32x4, tq0), r1 = LD(u32x4, (tq0 ^ 32) + 256);
        asm volatile("global_store_dwordx4 %0, %1, %3 sc1\n\tglobal_store_dwordx4 %0, %2, %4 sc1\n\ts_nop 1" :: "v"(oo), "v"(r0), "v"(r1), "s"(op), "s"(op2) : "memory");
    }
    }
}

extern "C" void kernel_launch(void* const* d_in, const int* in_sizes, int n_in, void* d_out, int out_size, void* d_ws, size_t ws_size, hipStream_t stream) {
    (void)hipFuncSetAttribute((const void*)fused_block, hipFuncAttributeMaxDynamicSharedMemorySize, LDS_BYTES);
    fused_block<<<256, 512, LDS_BYTES, stream>>>((const char*)d_in[0], (const char*)d_in[1], (const char*)d_in[2], (const char*)d_in[3], (const char*)d_in[4],
                                                 (const char*)d_in[6], (const char*)d_in[7], (const char*)d_in[8], (const char*)d_in[9], (char*)d_out, (char*)d_ws);
}
```
